```python
import torch

def _softmax(x, axis=-1):
    return torch.softmax(x, dim=axis)
def _gelu(x):
    return torch.nn.functional.gelu(x, approximate="tanh")
def _silu(x):
    return torch.nn.functional.silu(x)
def _sigmoid(x):
    return torch.sigmoid(x)
def _layernorm(x, axis=-1, eps=1e-5):
    m = x.mean(dim=axis, keepdim=True)
    v = x.var(dim=axis, keepdim=True, unbiased=False)
    return (x - m) / torch.sqrt(v + eps)
def _scatter_add(tgt, idx, src, axis=0):
    return tgt.clone().index_add_(axis, idx, src)
def _segment_sum(src, seg, n):
    out = torch.zeros((n,) + src.shape[1:], dtype=src.dtype)
    return out.index_add_(0, seg, src)
def _pad(x, pw):
    flat = tuple(v for pair in reversed(pw) for v in pair)
    return torch.nn.functional.pad(x, flat)

kernel_name = 'interleave_layers__sliding_block__glu_gated_bilinear__prompt'

def setup_inputs():

    _w = (lambda a: (lambda t: t.to(torch.bfloat16) if t.is_floating_point() else t)(torch.as_tensor(a)))
    def _A_setup():
        B, S, D = 1, 2048, 640
        return {
            "x": _w((lambda *s: torch.randn(*s, dtype=torch.bfloat16) * 0.05)(B, S, D)),
            "wqkv": _w((lambda *s: torch.randn(*s, dtype=torch.bfloat16) * 0.05)(D, 3 * D)),
            "wo": _w((lambda *s: torch.randn(*s, dtype=torch.bfloat16) * 0.05)(D, D)),
        }
    _A_in = _A_setup()

    def _B_setup():
        B, S, D, F = 1, 2048, 640, 1536
        return {
            "x": _w((lambda *s: torch.randn(*s, dtype=torch.bfloat16) * 0.05)(B, S, D)),
            "w_gate": _w((lambda *s: torch.randn(*s, dtype=torch.bfloat16) * 0.8)(D, F) * (D ** -0.5)),
            "w_up": _w((lambda *s: torch.randn(*s, dtype=torch.bfloat16) * 0.8)(D, F) * (D ** -0.5)),
            "w_down": _w((lambda *s: torch.randn(*s, dtype=torch.bfloat16) * 0.8)(F, D) * (F ** -0.5)),
        }
    _B_in = _B_setup()

    return {
        "A_x": _A_in["x"],
        "A_wqkv": _A_in["wqkv"],
        "A_wo": _A_in["wo"],
        "B_x": _B_in["x"],
        "B_w_gate": _B_in["w_gate"],
        "B_w_up": _B_in["w_up"],
        "B_w_down": _B_in["w_down"],
    }

def reference(A_x, A_wqkv, A_wo, B_x, B_w_gate, B_w_up, B_w_down):
    def _A_ref(x, wqkv, wo):
        B, S, D = x.shape
        H = 32; Dh = D // H; W = 256
        qkv = (lambda x, *d: x.permute(*d))((x @ wqkv).reshape(B, S, 3, H, Dh), 2, 0, 3, 1, 4)
        q, k, v = qkv[0], qkv[1], qkv[2]
        scores = torch.einsum("bhqd,bhkd->bhqk", q, k) * (Dh ** -0.5)
        idx = torch.arange(S)
        mask = (idx[:, None] - idx[None, :] >= 0) & (idx[:, None] - idx[None, :] < W)
        scores = torch.where(mask[None, None], scores, -1e9)
        p = _softmax(scores, axis=-1)
        o = torch.einsum("bhqk,bhkd->bhqd", p, v)
        return (lambda x, *d: x.permute(*d))(o, 0, 2, 1, 3).reshape(B, S, D) @ wo

    def _B_ref(x, w_gate, w_up, w_down):
        g = torch.einsum("bsd,df->bsf", x, w_gate)
        u = torch.einsum("bsd,df->bsf", x, w_up)
        h = g * u
        return torch.einsum("bsf,fd->bsd", h, w_down)

    x = A_x
    x = x + _A_ref(_layernorm(x, axis=-1), A_wqkv, A_wo)
    x = x + _B_ref(_layernorm(x, axis=-1), B_w_gate, B_w_up, B_w_down)
    x = x + _A_ref(_layernorm(x, axis=-1), A_wqkv, A_wo)
    x = x + _B_ref(_layernorm(x, axis=-1), B_w_gate, B_w_up, B_w_down)
    return x
```

```cpp
#include <hip/hip_runtime.h>
#include <hip/hip_bf16.h>
#include <stdint.h>
typedef unsigned short u16;
constexpr int S=2048, D=640, H=32, DH=20, W=256, F=1536;
__device__ __forceinline__ float b2f(u16 b){ return __uint_as_float(((unsigned)b)<<16); }
__device__ __forceinline__ u16 f2b(float f){ unsigned u=__float_as_uint(f); u += 0x7fffu + ((u>>16)&1u); return (u16)(u>>16); }
__device__ __forceinline__ float rb(float f){ return b2f(f2b(f)); }

__global__ void ln_k(const u16* x, u16* y){
  int row=blockIdx.x; int t=threadIdx.x;
  float s=0; for(int i=t;i<D;i+=64) s+=b2f(x[row*D+i]);
  for(int m=32;m>=1;m>>=1) s+=__shfl_xor(s,m);
  float mean=s/D; float v=0;
  for(int i=t;i<D;i+=64){ float d=b2f(x[row*D+i])-mean; v+=d*d; }
  for(int m=32;m>=1;m>>=1) v+=__shfl_xor(v,m);
  v/=D; float r=1.0f/sqrtf(v+1e-5f);
  for(int i=t;i<D;i+=64) y[row*D+i]=f2b((b2f(x[row*D+i])-mean)*r);
}
__global__ void gemm_k(const u16* A,const u16* B,u16* C,const u16* aux,int M,int N,int K,int mode){
  int n=blockIdx.x*64+threadIdx.x%64; int m=blockIdx.y*4+threadIdx.x/64;
  if(n>=N) return;
  float acc=0; for(int k=0;k<K;++k) acc+=b2f(A[m*K+k])*b2f(B[k*N+n]);
  if(mode==1) acc=rb(acc)+b2f(aux[m*N+n]);
  if(mode==2) acc=rb(acc)*b2f(aux[m*N+n]);
  C[m*N+n]=f2b(acc);
}
__global__ void attn_k(const u16* qkv,u16* o){
  int q=blockIdx.x; int h=blockIdx.y; int t=threadIdx.x;
  __shared__ float sc[256]; __shared__ float red[256];
  int key=q-255+t; float s=-1e30f;
  if(key>=0){ s=0; for(int d=0;d<DH;++d) s+=b2f(qkv[q*3*D+h*DH+d])*b2f(qkv[key*3*D+D+h*DH+d]); s*=0.22360679775f; }
  red[t]=s; __syncthreads();
  for(int m=128;m>=1;m>>=1){ if(t<m) red[t]=fmaxf(red[t],red[t+m]); __syncthreads(); }
  float mx=red[0]; __syncthreads();
  float p=key>=0?__expf(s-mx):0.f; sc[t]=p; red[t]=p; __syncthreads();
  for(int m=128;m>=1;m>>=1){ if(t<m) red[t]+=red[t+m]; __syncthreads(); }
  float sum=red[0];
  if(t<DH){ float a=0; for(int j=0;j<256;++j){ int kk=q-255+j; if(kk>=0) a+=sc[j]*b2f(qkv[kk*3*D+2*D+h*DH+t]); } o[q*D+h*DH+t]=f2b(a/sum); }
}
extern "C" void kernel_launch(void* const* d_in, const int* in_sizes, int n_in, void* d_out, int out_size, void* d_ws, size_t ws_size, hipStream_t stream){
  const u16* x=(const u16*)d_in[0]; const u16* wqkv=(const u16*)d_in[1]; const u16* wo=(const u16*)d_in[2];
  const u16* wg=(const u16*)d_in[4]; const u16* wu=(const u16*)d_in[5]; const u16* wd=(const u16*)d_in[6];
  u16* ws=(u16*)d_ws; u16* xn=ws; u16* qkv=xn+S*D; u16* o=qkv+S*3*D; u16* x1=o+S*D; u16* g=x1+S*D; u16* hh=g+S*F; u16* x2=hh+S*F;
  const u16* cur=x;
  for(int l=0;l<2;++l){
    ln_k<<<S,64,0,stream>>>(cur,xn);
    gemm_k<<<dim3(3*D/64,S/4),256,0,stream>>>(xn,wqkv,qkv,nullptr,S,3*D,D,0);
    attn_k<<<dim3(S,H),256,0,stream>>>(qkv,o);
    gemm_k<<<dim3(D/64,S/4),256,0,stream>>>(o,wo,x1,cur,S,D,D,1);
    ln_k<<<S,64,0,stream>>>(x1,xn);
    gemm_k<<<dim3(F/64,S/4),256,0,stream>>>(xn,wg,g,nullptr,S,F,D,0);
    gemm_k<<<dim3(F/64,S/4),256,0,stream>>>(xn,wu,hh,g,S,F,D,2);
    u16* dst = l==0? x2 : (u16*)d_out;
    gemm_k<<<dim3(D/64,S/4),256,0,stream>>>(hh,wd,dst,x1,S,D,F,1);
    cur=x2;
  }
}
```

```cpp
#include <hip/hip_runtime.h>
#include <stdint.h>

typedef unsigned short u16;
typedef unsigned u32;
typedef u32 u32x2 __attribute__((ext_vector_type(2)));
typedef u32 u32x4 __attribute__((ext_vector_type(4)));
typedef float f32x2 __attribute__((ext_vector_type(2)));
typedef float f32x4 __attribute__((ext_vector_type(4)));
typedef float f32x16 __attribute__((ext_vector_type(16)));
typedef short bf16x8 __attribute__((ext_vector_type(8)));
typedef short s16x4 __attribute__((ext_vector_type(4)));
typedef __bf16 bf16x2_t __attribute__((ext_vector_type(2)));
#define GAS __attribute__((address_space(1)))
#define LAS __attribute__((address_space(3)))
#define DI __device__ __forceinline__
#define RLX_AGENT __ATOMIC_RELAXED, __HIP_MEMORY_SCOPE_AGENT

constexpr int S = 2048, D = 640, F = 1536;
constexpr u32 KB = 1024;
constexpr u32 OFF_FLAGS = 0;
constexpr u32 OFF_TMO = 32 * KB;
constexpr u32 OFF_CSQ = 64 * KB;
constexpr u32 OFF_CSG = 72 * KB;
constexpr u32 OFF_BQ = 128 * KB;
constexpr u32 OFF_BW = OFF_BQ + 2400 * KB;
constexpr u32 OFF_BG = OFF_BW + 800 * KB;
constexpr u32 OFF_BD = OFF_BG + 3840 * KB;
constexpr u32 A_XA = 0, A_SA = 2560 * KB, A_QKV = A_SA + 128 * KB, A_OF = A_QKV + 7680 * KB, A_OO = A_OF + 2048 * KB, A_XB = A_OO + 512 * KB,
              A_SB = A_XB + 2560 * KB, A_HF = A_SB + 128 * KB, LB = A_HF + 6144 * KB;
constexpr u32 OFF_ACT = OFF_BD + 1920 * KB;
constexpr u32 OFF_DBG = OFF_ACT + 2 * LB;
constexpr u32 OOR = 0x80000000u;
constexpr u32 L_Y = 80 * KB, L_HX = 16 * KB, L_HY = 80 * KB, L_MISC = 144 * KB;
constexpr u32 TP = 3072;
constexpr u32 L_ONES = L_MISC, L_STAT = L_MISC + 512;
constexpr u32 L_CS = L_STAT + 1024;
constexpr u32 L_MASK = L_CS + 1280;
constexpr u32 L_DBG = L_MASK + 6 * KB;
constexpr u32 LDS_BYTES = L_DBG + 1024;
static_assert(LDS_BYTES <= 160 * KB, "");

struct Params { const u16* x; const u16* wqkv; const u16* wo; const u16* wg; const u16* wu; const u16* wd; u16* out; unsigned char* ws; };

DI float bf_lo(u32 v) { return __uint_as_float(v << 16); }
DI float bf_hi(u32 v) { return __uint_as_float(v & 0xffff0000u); }
DI u32 pk2(float a, float b) { f32x2 f = {a, b}; bf16x2_t h = __builtin_convertvector(f, bf16x2_t); return __builtin_bit_cast(u32, h); }
DI float rbf(float a) { return bf_lo(pk2(a, a)); }

DI u32x4 gather8(const u16* W, int ld, const int (&k)[8], int n) {
  u32x4 v;
  v.x = W[k[0] * ld + n] | ((u32)W[k[1] * ld + n] << 16); v.y = W[k[2] * ld + n] | ((u32)W[k[3] * ld + n] << 16);
  v.z = W[k[4] * ld + n] | ((u32)W[k[5] * ld + n] << 16); v.w = W[k[6] * ld + n] | ((u32)W[k[7] * ld + n] << 16);
  return v;
}
DI int ocol(int head, int ds) { return (head & 1) ? (ds < 16 ? 20 * head + 4 + ds : 20 * head + ds - 16) : 20 * head + ds; }
constexpr int NFRAG = 2400 + 800 + 3840 + 1920;
constexpr int PREP_LN_BLOCKS = S / 4, PREP_TILE_BLOCKS = NFRAG / 4, PREP_CS_BLOCKS = (1920 + 3072) / 64;
__global__ void __launch_bounds__(256) prep_kernel(Params p) {
  const int tid = threadIdx.x, lane = tid & 63, wv = tid >> 6, q4 = lane >> 4, r = lane & 15;
  int b = blockIdx.x;
  if (b == 0) { for (int i = tid; i < 256 * 32 + 64; i += 256) ((u32*)p.ws)[i] = 0; }
  if (b < PREP_LN_BLOCKS) {
    const int row = b * 4 + wv;
    const u32x4* src = (const u32x4*)(p.x + row * D);
    u32x4 v0 = src[lane], v1 = lane < 16 ? src[64 + lane] : u32x4{0, 0, 0, 0};
    float f[16];
    for (int j = 0; j < 4; ++j) { f[2 * j] = bf_lo(v0[j]); f[2 * j + 1] = bf_hi(v0[j]); f[8 + 2 * j] = bf_lo(v1[j]); f[9 + 2 * j] = bf_hi(v1[j]); }
    float s = 0; for (int j = 0; j < 16; ++j) s += f[j];
    for (int m = 32; m >= 1; m >>= 1) s += __shfl_xor(s, m);
    const float mean = s * (1.0f / D);
    float q = 0; for (int j = 0; j < 8; ++j) { float d = f[j] - mean; q += d * d; }
    if (lane < 16) for (int j = 8; j < 16; ++j) { float d = f[j] - mean; q += d * d; }
    for (int m = 32; m >= 1; m >>= 1) q += __shfl_xor(q, m);
    const float mb = rbf(mean), sd = rbf(sqrtf(rbf(rbf(q * (1.0f / D)) + 1e-5f)));
    u32x4 o0, o1;
    for (int j = 0; j < 4; ++j) { o0[j] = pk2(rbf(f[2 * j] - mb) / sd, rbf(f[2 * j + 1] - mb) / sd); o1[j] = pk2(rbf(f[8 + 2 * j] - mb) / sd, rbf(f[9 + 2 * j] - mb) / sd); }
    unsigned char* dst = p.ws + OFF_ACT + A_XA + (row >> 4) * (80 * 256) + (row & 15) * 16;
    *(u32x4*)(dst + lane * 256) = o0;
    if (lane < 16) *(u32x4*)(dst + (64 + lane) * 256) = o1;
    return;
  }
  b -= PREP_LN_BLOCKS;
  if (b < PREP_TILE_BLOCKS) {
    int fr = b * 4 + wv; int k[8]; u32x4 v;
    unsigned char* dst = p.ws + OFF_BQ + (u32)fr * KB + lane * 16;
    if (fr < 2400) {
      const int nf = fr & 1, s = (fr >> 1) % 20, cw = fr / 40;
      for (int j = 0; j < 8; ++j) k[j] = 32 * s + 8 * q4 + j;
      v = gather8(p.wqkv, 3 * D, k, 32 * cw + 8 * (r >> 2) + 4 * nf + (r & 3));
    } else if (fr < 3200) {
      fr -= 2400; const int nf = fr % 5, s = (fr / 5) % 5, w = (fr / 25) & 3, cg = fr / 100;
      for (int j = 0; j < 8; ++j) k[j] = s < 4 ? ocol(8 * w + 2 * s + (q4 >> 1), 4 * (q4 & 1) + (j & 3) + 8 * (j >> 2)) : ocol(8 * w + 2 * q4 + (j >> 2), 16 + (j & 3));
      v = gather8(p.wo, D, k, 80 * cg + 16 * nf + r);
    } else if (fr < 7040) {
      fr -= 3200; const int nf = fr % 3, s = (fr / 3) % 20, cw = fr / 60;
      for (int j = 0; j < 8; ++j) k[j] = 32 * s + 8 * q4 + j;
      v = gather8((r & 2) ? p.wu : p.wg, F, k, 24 * cw + 8 * nf + 2 * (r >> 2) + (r & 1));
    } else {
      fr -= 7040; const int nf = fr % 5, s = (fr / 5) % 12, w = (fr / 60) & 3, cg = fr / 240;
      for (int j = 0; j < 8; ++j) k[j] = 384 * w + 32 * s + 8 * q4 + j;
      v = gather8(p.wd, D, k, 80 * cg + 16 * nf + r);
    }
    *(u32x4*)dst = v;
    return;
  }
  b -= PREP_TILE_BLOCKS;
  {
    __shared__ float red[4][64];
    const int c = b * 64 + lane; const u16* W; int ld, n;
    if (c < 1920) { W = p.wqkv; ld = 3 * D; n = c; }
    else { const int e = c - 1920, rr = e & 15, nfg = e >> 4; W = (rr & 2) ? p.wu : p.wg; ld = F; n = 8 * nfg + 2 * (rr >> 2) + (rr & 1); }
    float s = 0; for (int k = wv * 160; k < wv * 160 + 160; ++k) s += bf_lo(W[k * ld + n]);
    red[wv][lane] = s; __syncthreads();
    if (wv == 0) { s = (red[0][lane] + red[1][lane]) + (red[2][lane] + red[3][lane]);
      if (c < 1920) ((float*)(p.ws + OFF_CSQ))[c] = s; else ((float*)(p.ws + OFF_CSG))[c - 1920] = s; }
  }
}

typedef __amdgpu_buffer_rsrc_t rsrc_t;
#define SB() __builtin_amdgcn_sched_barrier(0)
template <int I> struct IC { static constexpr int v = I; };
template <int B, int E, class Fn> DI void sfor(Fn&& f) { if constexpr (B < E) { f(IC<B>{}); sfor<B + 1, E>(f); } }
#define LAMBDA(S) [&](auto S) __attribute__((always_inline))
template <class T> DI void pin(T& d) { asm volatile("" : "+v"(d)); }
DI u32 sop(u32 v) { asm volatile("" : "+s"(v)); return v; }
DI u32 vop(u32 v) { asm volatile("" : "+v"(v)); return v; }
struct Ctx {
  rsrc_t rs, ra;
  u32 lb; unsigned char* ws;
  u32 l16, w, bid;
  u32 sq, sg, sw;
  u32 tq, tg;
  u32 bof, boo, bhf;
  u32 m0x, m0a;
};
template <int G, int START, int STRIDE, int N, class Fn> DI void at(Fn&& f) {
  if constexpr (G >= START && (G - START) % STRIDE == 0 && (G - START) / STRIDE < N) f(IC<(G - START) / STRIDE>{});
}
#define LP(T, a) ((LAS T*)(size_t)(a))

struct VM { int n, done;
};
template <bool W> DI void prof(VM& vm) {
}
DI void need(VM& vm, int mark) {
  if (mark > vm.done) { const int k = vm.n - mark; if (k < 63) asm volatile("s_waitcnt vmcnt(%0)" :: "i"(k) : "memory"); vm.done = mark; }
}
template <int SLOT, bool SC1, int OFF> DI int ld16(rsrc_t rs, VM& vm, u32x4& d, u32 voff, u32 soff) {
  if constexpr (SLOT >= 0 && SC1) asm volatile("buffer_load_dwordx4 a[%c0:%c1], %2, %3, %4 offen offset:%5 sc1" :: "i"(4 * SLOT), "i"(4 * SLOT + 3), "v"(voff), "s"(rs), "s"(soff), "i"(OFF) : "memory");
  else if constexpr (SLOT >= 0) asm volatile("buffer_load_dwordx4 a[%c0:%c1], %2, %3, %4 offen offset:%5" :: "i"(4 * SLOT), "i"(4 * SLOT + 3), "v"(voff), "s"(rs), "s"(soff), "i"(OFF) : "memory");
  else if constexpr (SC1) asm volatile("buffer_load_dwordx4 %0, %1, %2, %3 offen offset:%4 sc1" : "=v"(d) : "v"(voff), "s"(rs), "s"(soff), "i"(OFF) : "memory");
  else asm volatile("buffer_load_dwordx4 %0, %1, %2, %3 offen offset:%4" : "=v"(d) : "v"(voff), "s"(rs), "s"(soff), "i"(OFF) : "memory");
  return ++vm.n;
}
template <int OFF> DI int ld8c(rsrc_t rs, VM& vm, u32x2& d, u32 voff, u32 soff) {
  asm volatile("buffer_load_dwordx2 %0, %1, %2, %3 offen offset:%4 sc1" : "=v"(d) : "v"(voff), "s"(rs), "s"(soff), "i"(OFF) : "memory");
  return ++vm.n;
}
template <bool FIRST, int SA, int SBB> DI void mma(f32x4& d, const u32x4& a, const u32x4& b) {
#define MMA_(D_, C_) \
  if constexpr (SA >= 0 && SBB >= 0) asm volatile("v_mfma_f32_16x16x32_bf16 %0, a[%c1:%c2], a[%c3:%c4], " C_ : D_(d) : "i"(4 * SA), "i"(4 * SA + 3), "i"(4 * SBB), "i"(4 * SBB + 3)); \
  else if constexpr (SA >= 0) asm volatile("v_mfma_f32_16x16x32_bf16 %0, a[%c1:%c2], %3, " C_ : D_(d) : "i"(4 * SA), "i"(4 * SA + 3), "v"(b)); \
  else if constexpr (SBB >= 0) asm volatile("v_mfma_f32_16x16x32_bf16 %0, %3, a[%c1:%c2], " C_ : D_(d) : "i"(4 * SBB), "i"(4 * SBB + 3), "v"(a)); \
  else asm volatile("v_mfma_f32_16x16x32_bf16 %0, %1, %2, " C_ : D_(d) : "v"(a), "v"(b));
  if constexpr (FIRST) { MMA_("=&v", "0") } else { MMA_("+v", "%0") }
#undef MMA_
}
DI void st16c(const Ctx& c, VM& vm, u32 voff, u32x4 v) { ++vm.n; __builtin_amdgcn_raw_buffer_store_b128(v, c.ra, voff, 0, 16); }
DI void st8c(const Ctx& c, VM& vm, u32 voff, u32x2 v) { ++vm.n; __builtin_amdgcn_raw_buffer_store_b64(v, c.ra, voff, 0, 16); }
DI void st4c(const Ctx& c, VM& vm, u32 voff, u32 v) { ++vm.n; __builtin_amdgcn_raw_buffer_store_b32(v, c.ra, voff, 0, 16); }
template <u32 LOFF, u32 SOFF> DI void glds_head(const Ctx& c, VM& vm, u32& cur, u32 m0b, u32 sb, u32 voff) {
  ++vm.n;
  asm volatile("s_add_u32 m0, %1, %2\n\ts_add_u32 %0, %3, %4\n\tbuffer_load_dwordx4 %5, %6, %0 offen sc1 lds"
               : "=&s"(cur) : "s"(m0b), "i"(LOFF), "s"(sb), "i"(SOFF), "v"(voff), "s"(c.ra) : "memory", "scc");
}
template <int OFF> DI void glds_next(const Ctx& c, VM& vm, u32 cur, u32 voff) {
  ++vm.n;
  asm volatile("buffer_load_dwordx4 %0, %1, %2 offen offset:%3 sc1 lds" :: "v"(voff), "s"(c.ra), "s"(cur), "i"(OFF) : "memory");
}
#define BARL() asm volatile("s_waitcnt lgkmcnt(0)\n\ts_barrier" ::: "memory")
#define BAR0() asm volatile("s_barrier" ::: "memory")
extern "C" __device__ void carry_count_too_large() __attribute__((error("carry count too large")));

#define STAMP(k) do {} while (0)
#define TS(t, k) STAMP(1 + ((10 * l + (t)) * 5 + (k)))

struct Poll { u32 v, off, need; int mark; };
DI void poll_issue(const Ctx& c, VM& vm, Poll& p, u32 bid, u32 need) {
  p.off = OFF_FLAGS + bid * 128; p.need = need;
  asm volatile("buffer_load_dword %0, %1, %2, 0 offen sc1" : "=v"(p.v) : "v"(p.off), "s"(c.rs) : "memory");
  p.mark = ++vm.n;
}
DI void poll_check(const Ctx& c, VM& vm, Poll& p) {
  need(vm, p.mark); pin(p.v);
  if (__builtin_expect(!__all(p.v >= p.need), 0)) {
#pragma unroll 1
    for (u32 spins = 0;; ++spins) {
      u32 v;
      asm volatile("buffer_load_dword %0, %1, %2, 0 offen sc1\n\ts_waitcnt vmcnt(0)" : "=&v"(v) : "v"(p.off), "s"(c.rs) : "memory");
      if (__all(v >= p.need)) break;
      if (spins > (1u << 16)) { __hip_atomic_store((GAS u32*)(c.ws + OFF_TMO), 1u, RLX_AGENT); break; }
    }
  }
  asm volatile("" ::: "memory");
}
DI void flag_store(const Ctx& c, u32 val) { if (threadIdx.x == 0) __hip_atomic_store((GAS u32*)(c.ws + OFF_FLAGS) + c.bid * 32, val, RLX_AGENT); }
DI void publish(const Ctx& c, VM& vm, int mark, u32 val) { need(vm, mark); BAR0(); flag_store(c, val); }
DI u32 dep_x(const Ctx& c, u32 tf) { const u32 t = sop(tf); return (t >> 3) + 8 * (((t >> 1) & 3) * 8 + ((vop(c.l16) >> 4) & 7)); }
DI u32 dep_o(const Ctx& c) { const u32 h = 8 * c.w + ((vop(c.l16) >> 4) & 7); return (h >> 2) + 8 * ((h & 3) + 4 * (sop(c.sw) >> 7)); }
DI u32 dep_h(const Ctx& c) { const u32 r = sop(c.sw); return (r >> 7) + 8 * (((r >> 6) & 1) * 16 + 4 * c.w + ((vop(c.l16) >> 4) & 3)); }

template <int ID> constexpr int slotB(int j) { return ID == 0 ? (j < 10 ? -1 : 15 + j) : ID == 1 ? j : ID == 2 ? (j < 39 ? 25 + j : -1) : j; }
template <int ID_, int N> struct BF { static constexpr int ID = ID_; u32x4 f[N]; int m[N]; u32 base, cur; };
template <int J, int ID, int N> DI void ldB(const Ctx& c, VM& vm, BF<ID, N>& B) {
  if constexpr ((J & 3) == 0) B.cur = sop(B.base) + J * KB;
  B.m[J] = ld16<slotB<ID>(J), false, (J & 3) * 1024>(c.rs, vm, B.f[J], c.l16, B.cur);
}
template <int LO, int HI, class BT> DI void loadB(const Ctx& c, VM& vm, BT& B) { sfor<LO, HI>(LAMBDA(J) { ldB<J.v>(c, vm, B); }); }
template <int CTRL> DI float dpp_add(float v) { return v + __int_as_float(__builtin_amdgcn_update_dpp(0, __float_as_int(v), CTRL, 0xF, 0xF, true)); }
DI float red8(float v) { return dpp_add<0x141>(dpp_add<0x4E>(dpp_add<0xB1>(v))); }
DI float red_rows(float v) {
  auto a = __builtin_amdgcn_permlane16_swap(__float_as_uint(v), __float_as_uint(v), false, false); v = __uint_as_float(a[0]) + __uint_as_float(a[1]);
  auto b = __builtin_amdgcn_permlane32_swap(__float_as_uint(v), __float_as_uint(v), false, false); return __uint_as_float(b[0]) + __uint_as_float(b[1]);
}

template <int HALF> constexpr u32 img_off(int g) { return HALF == 0 ? g * 16 * KB : (g < 4 ? L_Y + g * 16 * KB : 0); }
struct Stage { u32x4 sv; u32 src, cur; int mg[5]; };
template <int HALF, int K> DI void stage(const Ctx& c, VM& vm, Stage& st, u32 xoff, u32 tf) {
  if constexpr (K == 0) {
    const u32 t = sop(tf), l = vop(c.l16);
    st.src = xoff + HALF * 64 * 20480 + t * 20480;
    ld16<-1, true, 0>(c.ra, vm, st.sv, ((l << 2) & 0x3C0) + ((l >> 4) & 0x30), xoff + (A_SA - A_XA) + HALF * 65536 + t * 1024);
  } else {
    constexpr int g = (K - 1) >> 2, j = (K - 1) & 3;
    if constexpr (j == 0) glds_head<img_off<HALF>(g), g * 4 * KB>(c, vm, st.cur, c.m0x, st.src, c.l16); else glds_next<j * 1024>(c, vm, st.cur, c.l16);
    st.mg[g] = vm.n;
  }
}
template <int HALF, int LO, int HI> DI void stages(const Ctx& c, VM& vm, Stage& st, u32 xoff, u32 tf) { sfor<LO, HI>(LAMBDA(K) { stage<HALF, K.v>(c, vm, st, xoff, tf); }); }
template <int HALF, int NF, class BT, class HL, class HA> DI void gemm_lds(const Ctx& c, VM& vm, Stage& st, bool use_stats, f32x4 (&acc)[NF][4], BT& B, HL hl, HA ha) {
  const u32 b0 = vop(c.lb + img_off<HALF>(0) + c.l16), b1 = vop(c.lb + img_off<HALF>(4) + c.l16);
  auto rd = [&](int s, int tf) __attribute__((always_inline)) { return *LP(const u32x4, (s < 16 ? b0 + (s >> 2) * 16 * KB : b1) + tf * 4 * KB + (s & 3) * KB); };
  prof<true>(vm);
  need(vm, st.mg[1]); BAR0(); pin(st.sv);
  u32x4 a[2][4];
#pragma unroll
  for (int tf = 0; tf < 4; ++tf) a[0][tf] = rd(0, tf);
  SB();
  sfor<0, 20>(LAMBDA(S) {
    constexpr int s = S.v;
    if (s == 7) { need(vm, st.mg[4]); BAR0(); }
    need(vm, B.m[s * NF + NF - 1]);
    __builtin_amdgcn_s_waitcnt(0xC07F);
    prof<false>(vm);
    SB();
    sfor<0, 4 * NF>(LAMBDA(J) {
      constexpr int nf = J.v >> 2, tf = J.v & 3;
      mma<s == 0, slotB<BT::ID>(s * NF + nf), -1>(acc[nf][tf], B.f[s * NF + nf], a[s & 1][tf]);
      if constexpr (s < 19 && nf == 0) { a[(s + 1) & 1][tf] = rd(s + 1, tf); SB(); }
      if constexpr (J.v >= 4 && (J.v - 4) % NF == 0) { SB(); hl(IC<s * NF + (J.v - 4) / NF>{}); SB(); }
      if constexpr (J.v == 5) { SB(); ha(S); SB(); }
    });
    if constexpr (s == 1) {
      const float m0 = __uint_as_float(st.sv.x), m1 = __uint_as_float(st.sv.z);
      float mean = red_rows(m0 + m1) * 0.125f;
      const float d0 = m0 - mean, d1 = m1 - mean;
      const float M2 = red_rows(__uint_as_float(st.sv.y) + __uint_as_float(st.sv.w) + 80.f * (d0 * d0 + d1 * d1));
      float rstd = __builtin_amdgcn_rsqf(M2 * (1.0f / D) + 1e-5f);
      if (!use_stats) { mean = 0.f; rstd = 1.f; }
      *LP(f32x2, c.lb + L_STAT + HALF * 512 + c.w * 128 + ((c.l16 >> 1) & 0x78)) = f32x2{-mean, rstd};
    }
    SB();
  });
  asm volatile("s_nop 7");
  prof<true>(vm);
  SB();
}
struct EpQ { f32x4 cs[2]; f32x2 st[4]; u32 vo; float qs; };
struct EpG { f32x4 cs[3]; f32x2 st[4]; u32 vo; };
template <int HALF> DI void pre_q(const Ctx& c, EpQ& e) {
  const u32 l = vop(c.l16), i = sop(c.bid) >> 3, cg = i % 15;
  const u32 pc = c.lb + L_CS + c.w * 128 + ((l >> 3) & 0x60), ps = c.lb + L_STAT + HALF * 512 + ((l >> 1) & 0x78);
  e.cs[0] = *LP(const f32x4, pc); e.cs[1] = *LP(const f32x4, pc + 16);
#pragma unroll
  for (int tf = 0; tf < 4; ++tf) e.st[tf] = *LP(const f32x2, ps + tf * 128);
  e.qs = cg < 5 ? 0.22360679774997896f * 1.4426950408889634f : 1.0f;
  const u32 base = i < 30 ? A_QKV + ((16 * cg + 4 * c.w) * 2048 + c.sq + HALF * 1024) * 16 : OOR;
  e.vo = base + ((l >> 8) << 15) + (l & 0xF0);
}
DI void epi_q(const Ctx& c, VM& vm, const EpQ& e, const f32x4 (&acc)[2][4]) {
#pragma unroll
  for (int tf = 0; tf < 4; ++tf) {
    const float rs = e.st[tf].y * e.qs;
    const f32x4 a = (acc[0][tf] + e.st[tf].x * e.cs[0]) * rs, b = (acc[1][tf] + e.st[tf].x * e.cs[1]) * rs;
    st16c(c, vm, e.vo + tf * 256, u32x4{pk2(a.x, a.y), pk2(a.z, a.w), pk2(b.x, b.y), pk2(b.z, b.w)});
  }
}
template <int HALF> DI void pre_g(const Ctx& c, EpG& e) {
  const u32 l = vop(c.l16), cg = (sop(c.bid) >> 3) & 15;
  const u32 pc = c.lb + L_CS + 512 + c.w * 192 + ((l >> 4) & 0x30), ps = c.lb + L_STAT + HALF * 512 + ((l >> 1) & 0x78);
#pragma unroll
  for (int nf = 0; nf < 3; ++nf) e.cs[nf] = *LP(const f32x4, pc + nf * 64);
#pragma unroll
  for (int tf = 0; tf < 4; ++tf) e.st[tf] = *LP(const f32x2, ps + tf * 128);
  e.vo = A_HF + (((c.sg + HALF * 1024) >> 4) * 192 + 12 * cg + 3 * c.w) * 256 + (l >> 8) * (192 * 256) + (l & 0xF0);
}
DI void epi_g(const Ctx& c, VM& vm, const EpG& e, const f32x4 (&acc)[3][4]) {
#pragma unroll
  for (int nf = 0; nf < 3; ++nf) {
    u32 R[4];
#pragma unroll
    for (int tf = 0; tf < 4; ++tf) { const f32x4 a = acc[nf][tf] + e.st[tf].x * e.cs[nf]; const float r2 = e.st[tf].y * e.st[tf].y; R[tf] = pk2(a.x * r2 * a.z, a.y * r2 * a.w); }
    { auto t = __builtin_amdgcn_permlane32_swap(R[0], R[2], false, false); R[0] = t[0]; R[2] = t[1]; }
    { auto t = __builtin_amdgcn_permlane32_swap(R[1], R[3], false, false); R[1] = t[0]; R[3] = t[1]; }
    { auto t = __builtin_amdgcn_permlane16_swap(R[0], R[1], false, false); R[0] = t[0]; R[1] = t[1]; }
    { auto t = __builtin_amdgcn_permlane16_swap(R[2], R[3], false, false); R[2] = t[0]; R[3] = t[1]; }
    st16c(c, vm, e.vo + nf * 256, u32x4{R[0], R[1], R[2], R[3]});
  }
}

struct AW { u32x4 f[4][2]; u32x2 o[2][2]; u32 s0, s1, vo; int m[5]; static constexpr int slot(int, int) { return -1; } };
template <bool X> struct AD { u32x4 f[12][2]; u32 cur, v1; int m[12]; static constexpr int slot(int s, int tf) { return X && s >= 10 ? 60 + 2 * (s - 10) + tf : -1; } };
template <int HALF, int K> DI void ld_w(const Ctx& c, VM& vm, AW& a) {
  constexpr int s = K >> 1, tf = K & 1;
  if constexpr (K == 0) a.s0 = sop(c.bof) + HALF * 64 * 64 * 256;
  if constexpr (K == 1) a.s1 = a.s0 + 64 * 256;
  if constexpr (K < 8) a.m[s] = ld16<-1, true, s * 1024>(c.ra, vm, a.f[s][tf], c.l16, tf ? a.s1 : a.s0);
  else {
    if constexpr (K == 8) { const u32 l = vop(c.l16); a.vo = (l & 0x300) + ((l >> 1) & 0x78); a.s0 = sop(c.boo) + HALF * 64 * 32 * 128; }
    if constexpr (K == 10) a.s0 += 32 * 128;
    a.m[4] = ld8c<tf * 128>(c.ra, vm, a.o[s - 4][tf], a.vo, a.s0);
  }
}
template <int NS, int HALF> constexpr int kord(int p) { return NS == 12 && HALF == 1 ? (p + 5) % 12 : p; }
template <int HALF, int K, bool X> DI void ld_d(const Ctx& c, VM& vm, AD<X>& a) {
  constexpr int s = kord<12, HALF>(K >> 1), tf = K & 1;
  if constexpr (K == 0) a.v1 = vop(c.l16) + 192 * 256;
  if constexpr (tf == 0 && (K == 0 || (s & 3) == 0)) a.cur = sop(c.bhf) + HALF * 64 * 192 * 256 + (s & ~3) * KB;
  a.m[s] = ld16<AD<X>::slot(s, tf), true, (s & 3) * 1024>(c.ra, vm, a.f[s][tf], tf ? a.v1 : c.l16, a.cur);
}
template <int S> DI void get_a(VM& vm, AW& a, u32x4 (&o)[2]) {
  need(vm, a.m[S]);
  if constexpr (S < 4) { o[0] = a.f[S][0]; o[1] = a.f[S][1]; }
  else { pin(a.o[0][0]); pin(a.o[0][1]); pin(a.o[1][0]); pin(a.o[1][1]);
    o[0] = u32x4{a.o[0][0].x, a.o[0][0].y, a.o[0][1].x, a.o[0][1].y}; o[1] = u32x4{a.o[1][0].x, a.o[1][0].y, a.o[1][1].x, a.o[1][1].y}; }
}
template <int S, bool X> DI void get_a(VM& vm, AD<X>& a, u32x4 (&o)[2]) { need(vm, a.m[S]); if constexpr (AD<X>::slot(S, 0) < 0) { o[0] = a.f[S][0]; o[1] = a.f[S][1]; } }
template <int HALF, int NS, class BT, class AT, class HL, class HA> DI void gemm_reg(const Ctx& c, VM& vm, BT& B, AT& a, HL hl, HA ha) {
  f32x4 acc[10];
  const u32 l = vop(c.l16), sw = ((l >> 8) ^ (l >> 5)) & 3;
  const u32 p0 = c.lb + (HALF ? L_HY : L_HX) + c.w * 10 * KB + ((l << 2) & 0x3C0) + sw * 16, p1 = p0 ^ 16;
  auto wr = [&](int j) __attribute__((always_inline)) { *LP(f32x4, (((j % 5) & 1) ? p1 : p0) + j * KB) = acc[j]; SB(); };
  sfor<0, NS>(LAMBDA(P) {
    constexpr int s = kord<NS, HALF>(P.v);
    prof<true>(vm);
    need(vm, B.m[s * 5 + 4]);
    u32x4 o[2]; get_a<s>(vm, a, o);
    SB();
    sfor<0, 10>(LAMBDA(J) { constexpr int tf = J.v / 5, nf = J.v % 5;
      mma<P.v == 0, slotB<BT::ID>(s * 5 + nf), AT::slot(s, tf)>(acc[J.v], B.f[s * 5 + nf], o[tf]);
      if constexpr ((P.v * 10 + J.v) % 4 == 1) { SB(); hl(IC<(P.v * 10 + J.v) / 4>{}); SB(); }
      if constexpr (J.v == 4) { SB(); ha(P); SB(); }
      if constexpr (P.v == NS - 1 && J.v >= 8) { SB(); wr(J.v - 8); } });
    SB();
  });
  prof<true>(vm);
  sfor<2, 9>(LAMBDA(J) { wr(J.v); });
  asm volatile("s_nop 0"); SB();
  wr(9);
  prof<true>(vm);
}
struct XR { u32x4 m; u32 t; };
template <int HALF> DI void red_out(const Ctx& c, VM& vm, XR& xr, u32 xoff, u16* out) {
  prof<true>(vm);
  const u32 lane = vop(c.l16) >> 4, cg = (sop(c.bid) >> 3) & 7;
  const u32 cc = lane & 7, t16 = (8 * c.w + (lane >> 3)) & 15, g = (t16 >> 1) & 3;
  const u32 base = c.lb + (HALF ? L_HY : L_HX) + (c.w >> 1) * 5 * KB + t16 * 64;
  const u32 pa = base + (cc >> 1) * KB + (((2 * (cc & 1)) ^ g ^ ((cc >> 1) & 1)) << 4), pb = pa ^ 16;
  const u32 pt = base + 4 * KB + (((cc >> 1) ^ g) << 4) + (cc & 1) * 8;
  f32x4 a = *LP(const f32x4, pa), b = *LP(const f32x4, pb); f32x2 t = *LP(const f32x2, pt);
#pragma unroll
  for (int ww = 1; ww < 4; ++ww) { a += *LP(const f32x4, pa + ww * 10 * KB); b += *LP(const f32x4, pb + ww * 10 * KB); t += *LP(const f32x2, pt + ww * 10 * KB); }
  a += f32x4{bf_lo(xr.m.x), bf_hi(xr.m.x), bf_lo(xr.m.y), bf_hi(xr.m.y)}; b += f32x4{bf_lo(xr.m.z), bf_hi(xr.m.z), bf_lo(xr.m.w), bf_hi(xr.m.w)}; t += f32x2{bf_lo(xr.t), bf_hi(xr.t)};
  xr.m = u32x4{pk2(a.x, a.y), pk2(a.z, a.w), pk2(b.x, b.y), pk2(b.z, b.w)}; xr.t = pk2(t.x, t.y);
  const u32 row = c.sw + HALF * 1024 + 8 * c.w + (lane >> 3);
  const u32 vo = xoff + ((row >> 4) * 80 + 10 * cg) * 256 + t16 * 16;
  st16c(c, vm, vo + cc * 256, xr.m);
  st4c(c, vm, vo + (8 + (cc >> 2)) * 256 + (cc & 3) * 4, xr.t);
  if (out) {
    *(GAS u32x4*)(out + row * D + 80 * cg + 8 * cc) = xr.m;
    *(GAS u32*)(out + row * D + 80 * cg + 64 + 2 * cc) = xr.t;
  }
  const f32x4 s4 = a + b;
  const float mean = red8((s4.x + s4.y) + (s4.z + s4.w) + (t.x + t.y)) * (1.0f / 80);
  a -= mean; b -= mean; t -= mean;
  const f32x4 q4 = a * a + b * b;
  const float M2 = red8((q4.x + q4.y) + (q4.z + q4.w) + (t.x * t.x + t.y * t.y));
  st8c(c, vm, cc == 0 ? xoff + (A_SA - A_XA) + row * 64 + cg * 8 : OOR, u32x2{__float_as_uint(mean), __float_as_uint(M2)});
  prof<true>(vm);
}

struct AIn { u32x4 Q0; u32x2 qc; u32 v0, v1, v2, cur; int r0, mark; };
DI u32 a_head(u32 bid) { return 4 * (bid & 7) + ((bid >> 3) & 3); }
DI u32 a_dep(const Ctx& c, int half) {
  const int h = a_head(c.bid), gp = half * 8 + (c.bid >> 5), clo = 5 * (h >> 1) + 2 * (h & 1), lane = c.l16 >> 4;
  const int j = lane < 36 ? lane : 0, sec = j / 12, lh = (j / 6) & 1, pi = (j >> 1) % 3, rg = j & 1;
  int g2 = gp - (sec == 0 ? 0 : pi); if (g2 < 0) g2 = gp;
  return (((g2 & 7) + 8 * (rg * 15 + ((80 * sec + clo + 2 * lh) >> 4))) << 1) | (g2 >> 3);
}
DI void a_poll(const Ctx& c, VM& vm, Poll& p, int l, u32 dep) { poll_issue(c, vm, p, dep >> 1, 10 * l + 1 + (dep & 1)); }
template <int HALF, int K> DI void a_stage(const Ctx& c, VM& vm, AIn& in) {
  if constexpr (K == 0) {
    const u32 bid = sop(c.bid), h = a_head(bid), odd = h & 1, cA = 5 * (h >> 1) + 3 * odd, cC = 5 * (h >> 1) + 2, l = vop(c.l16), r16 = l & 0x1F0; const bool hh = l >= 512;
    in.r0 = (HALF * 8 + (bid >> 5)) * 128;
    in.v0 = (hh ? 81 + cA : 80 + cA) * 32768 + r16;
    ld16<-1, true, 0>(c.ra, vm, in.Q0, in.v0, A_QKV - 80 * 32768 + (in.r0 + 32 * c.w) * 16);
    in.v1 = (hh ? 160 + cA : 80 + cC) * 32768 + r16 - 1024;
    in.v2 = (hh ? 160 + cC : 161 + cA) * 32768 + (r16 ^ (hh ? 128 : 64)) - 2048;
    in.cur = A_QKV - 80 * 32768 + (in.r0 + 32 * c.w) * 16 + odd * 8;
  } else if constexpr (K == 1) ld8c<1024>(c.ra, vm, in.qc, in.v1, in.cur);
  else {
    constexpr int u = (K - 2) / 3, j = (K - 2) % 3;
    if constexpr (j == 0) {
      int krow = in.r0 - 256 + 96 * (int)c.w + 32 * u; if (HALF == 0 && krow < 0) krow = in.r0;
      glds_head<(HALF ? L_HY : L_HX) + u * TP, A_QKV>(c, vm, in.cur, c.m0a, krow * 16, in.v0);
    } else glds_next<j * 1024>(c, vm, in.cur, j == 1 ? in.v1 : in.v2);
    in.mark = vm.n;
  }
}
DI u32x2 trread(u32 a) { return __builtin_bit_cast(u32x2, __builtin_amdgcn_ds_read_tr16_b64_v4i16(LP(s16x4, a))); }
DI float mx3(float a, float b, float c) { float r; asm("v_max3_f32 %0, %1, %2, %3" : "=v"(r) : "v"(a), "v"(b), "v"(c)); return r; }
DI void smfma0(f32x16& d, u32x4 a, u32x4 b) { asm volatile("v_mfma_f32_32x32x16_bf16 %0, %1, %2, 0" : "=&v"(d) : "v"(a), "v"(b)); }
DI void smfmac(f32x16& d, u32x4 a, u32x4 b, const f32x16& cc) { asm volatile("v_mfma_f32_32x32x16_bf16 %0, %1, %2, %3" : "=&v"(d) : "v"(a), "v"(b), "v"(cc)); }
DI void smfma1(f32x16& d, u32x2 a, u32x2 b) { asm volatile("v_mfma_f32_32x32x8_bf16 %0, %1, %2, %0" : "+v"(d) : "v"(a), "v"(b)); }
DI void omfma(f32x16& d, u32x4 a, u32x4 b) { asm volatile("v_mfma_f32_32x32x16_bf16 %0, %1, %2, %0" : "+v"(d) : "v"(a), "v"(b)); }
#define MX3(a, b, c) __builtin_fmaxf(__builtin_fmaxf((a), (b)), (c))
template <int HALF, class HL, class HA> DI void attn(const Ctx& c, VM& vm, AIn& in, HL hl, HA ha) {
  const u32 bid = sop(c.bid), h = a_head(bid), odd = h & 1, l = vop(c.l16), r16 = l & 0x1F0; const bool hh = l >= 512;
  const int q0 = (HALF * 8 + (bid >> 5)) * 128 + 32 * c.w;
  const u32 img = c.lb + (HALF ? L_HY : L_HX) + c.w * TP;
  const u32 kp0 = img + l, kp1 = img + 1024 + odd * 8 + r16, mp = c.lb + L_MASK + l;
  const int t0 = (256 - q0) >> 5;
  prof<true>(vm);
  need(vm, in.mark); BAR0(); pin(in.Q0); pin(in.qc);
  prof<true>(vm);
  u32x4 K0[9]; u32x2 K1[9];
  constexpr int ord[9] = {0, 8, 1, 2, 3, 4, 5, 6, 7};
#pragma unroll
  for (int u = 0; u < 9; ++u) { K0[ord[u]] = *LP(const u32x4, kp0 + ord[u] * TP); K1[ord[u]] = *LP(const u32x2, kp1 + ord[u] * TP); if (u == 1) SB(); }
  u32x4 sel[4];
#pragma unroll
  for (int j = 0; j < 4; ++j) sel[j] = *LP(const u32x4, mp + j * KB);
  u32x4 Q0 = in.Q0; u32x2 Q1 = hh ? u32x2{HALF == 0 ? 0x3F80u : 0u, 0} : in.qc;
  pin(Q1);
  prof<true>(vm);
  SB();
  f32x16 S[3], M;
  float m0, m1;
  sfor<0, 11>(LAMBDA(U) {
    constexpr int u = U.v, t = ord[u < 9 ? u : 0];
    auto half = [&](auto HI) __attribute__((always_inline)) {
      constexpr int o = HI.v * 8;
      if constexpr (u == 3) {
#pragma unroll
        for (int g = o; g < o + 8; ++g) M[g] = __uint_as_float((sel[g >> 2][g & 3] & __float_as_uint(S[1][g])) | (~sel[g >> 2][g & 3] & __float_as_uint(M[g])));
        if constexpr (o == 0) { m0 = MX3(M[0], M[1], M[2]); m1 = MX3(M[3], M[4], M[5]); m0 = MX3(m0, M[6], M[7]); }
        else { m1 = MX3(m1, M[8], M[9]); m0 = MX3(m0, M[10], M[11]); m1 = MX3(m1, M[12], M[13]); m0 = MX3(m0, M[14], M[15]); pin(M); }
        pin(m0); pin(m1);
      } else if constexpr (u >= 4) {
        const f32x16& X = S[(u - 2) % 3];
        m0 = MX3(m0, X[o], X[o + 1]); m1 = MX3(m1, X[o + 2], X[o + 3]); m0 = MX3(m0, X[o + 4], X[o + 5]); m1 = MX3(m1, X[o + 6], X[o + 7]);
        pin(m0); pin(m1);
      }
      SB();
    };
    if constexpr (u < 9) {
      if (HALF == 0) { K1[t].x = hh ? (t < t0 ? 0xF149u : 0u) : K1[t].x; pin(K1[t]); }
      SB();
      if constexpr (u == 0) { asm volatile("s_nop 1"); smfma0(M, K0[t], Q0); } else smfma0(S[u % 3], K0[t], Q0);
      SB();
    }
    if constexpr (u == 3) asm volatile("s_nop 7" : "+v"(M), "+v"(S[1]));
    if constexpr (u == 4 || u == 10) asm volatile("s_nop 3" : "+v"(S[(u - 2) % 3]));
    half(IC<0>{});
    if constexpr (u < 9) { if constexpr (u == 0) smfma1(M, K1[t], Q1); else smfma1(S[u % 3], K1[t], Q1); SB(); }
    half(IC<1>{});
    if constexpr (u < 9) hl(U);
    SB();
  });
  prof<true>(vm);
  ha(IC<0>{});
  prof<true>(vm);
  SB();
  const u32 lane = l >> 4, pc4 = lane & 3, db = (lane >> 4) & 1, kq = ((lane >> 3) & 4) + ((lane >> 2) & 3);
  const u32 vpl = db ? (pc4 ? 3 : 2) : pc4 >> 1;
  const u32 vb = (vpl == 3 ? c.lb + L_ONES : img + 1536 + vpl * 512) + (db ? odd : pc4 & 1) * 8, vstr = vpl == 3 ? 0 : TP;
  const u32 vlo = vb + ((kq ^ (4 * vpl)) << 4), vhi = vlo ^ 128;
  auto vfrag = [&](int t, int s) __attribute__((always_inline)) { const u32x2 lo = trread(vlo + t * vstr + s * 256), hi = trread(vhi + t * vstr + s * 256); return u32x4{lo.x, lo.y, hi.x, hi.y}; };
  const u32x4 V0a = vfrag(0, 0), V8a = vfrag(8, 0), V0b = vfrag(0, 1), V8b = vfrag(8, 1);
  const u32x4 mkA = *LP(const u32x4, mp + 4 * KB), mkB = *LP(const u32x4, mp + 5 * KB);
  float m = mx3(m0, m1, m1);
  { auto t = __builtin_amdgcn_permlane32_swap(__float_as_uint(m), __float_as_uint(m), false, false); m = mx3(__uint_as_float(t[0]), __uint_as_float(t[1]), __uint_as_float(t[1])); }
  f32x16 negm, O;
#pragma unroll
  for (int g = 0; g < 16; ++g) negm[g] = -m;
  pin(negm);
  SB();
#define EM(i) __builtin_amdgcn_exp2f(M[i] - m)
  u32x4 P; P.x = vop(pk2(EM(0), EM(1)));
  SB(); smfmac(S[1], K0[1], Q0, negm);
  P.y = vop(pk2(EM(2), EM(3))); P.z = vop(pk2(EM(4), EM(5)));
  SB(); smfma1(S[1], K1[1], Q1);
  P.w = vop(pk2(EM(6), EM(7)));
  u32x4 A8 = P & mkA, A0 = P & ~mkA; pin(A8); pin(A0);
  P.x = vop(pk2(EM(8), EM(9)));
  SB(); hl(IC<9>{}); SB(); smfma0(O, V0a, A0);
  P.y = vop(pk2(EM(10), EM(11))); P.z = vop(pk2(EM(12), EM(13)));
  SB(); hl(IC<10>{}); SB(); omfma(O, V8a, A8);
  P.w = vop(pk2(EM(14), EM(15)));
  u32x4 Pp = P & mkB, B0 = P & ~mkB, Vp = V8b; pin(Pp); pin(B0);
  SB(); hl(IC<11>{}); SB();
#define EX(i) __builtin_amdgcn_exp2f(S[t & 1][i])
  sfor<1, 8>(LAMBDA(T) {
    constexpr int t = T.v;
    prof<true>(vm);
    const u32x4 Va = vfrag(t, 0), Vb = vfrag(t, 1);
    u32x4 Pa, Pb;
    SB();
    if constexpr (t == 1) omfma(O, V0b, B0); else omfma(O, Vp, Pp);
    Pa.x = vop(pk2(EX(0), EX(1))); Pa.y = vop(pk2(EX(2), EX(3)));
    SB(); hl(IC<9 + 3 * t>{}); SB();
    if constexpr (t < 7) smfmac(S[(t + 1) & 1], K0[t + 1], Q0, negm);
    Pa.z = vop(pk2(EX(4), EX(5))); Pa.w = vop(pk2(EX(6), EX(7)));
    SB(); hl(IC<10 + 3 * t>{}); SB();
    if constexpr (t < 7) smfma1(S[(t + 1) & 1], K1[t + 1], Q1);
    Pb.x = vop(pk2(EX(8), EX(9))); Pb.y = vop(pk2(EX(10), EX(11)));
    SB();
    if constexpr (t == 1) { omfma(O, Vp, Pp); SB(); }
    ha(T);
    SB(); hl(IC<11 + 3 * t>{}); SB();
    omfma(O, Va, Pa);
    Pb.z = vop(pk2(EX(12), EX(13))); Pb.w = vop(pk2(EX(14), EX(15)));
    Pp = Pb; Vp = Vb;
    SB();
  });
  const u32 vo = A_OF + ((q0 >> 4) * 64 + 2 * h) * 256 + ((l >> 8) & 1) * (64 * 256) + (hh ? 256 : 0) + (l & 0xF0);
  const u32 vt = hh ? OOR : A_OO + ((q0 >> 4) * 32 + h) * 128 + ((l >> 8) & 1) * (32 * 128) + ((l >> 1) & 0x78);
  asm volatile("s_nop 1" :: "v"(negm), "v"(vo), "v"(vt));
  omfma(O, Vp, Pp);
  prof<true>(vm);
  ha(IC<8>{});
  asm volatile("s_nop 11" : "+v"(O));
  const float inv = __builtin_amdgcn_rcpf(O[12]);
  st16c(c, vm, vo, u32x4{pk2(O[0] * inv, O[1] * inv), pk2(O[2] * inv, O[3] * inv), pk2(O[4] * inv, O[5] * inv), pk2(O[6] * inv, O[7] * inv)});
  st8c(c, vm, vt, u32x2{pk2(O[8] * inv, O[9] * inv), pk2(O[10] * inv, O[11] * inv)});
}

constexpr int CARRY1 = 4, CARRYST = 4;
#define G decltype(Gp)::v

__global__ void __launch_bounds__(256, 1) mega_kernel(Params p) {
  extern __shared__ __attribute__((aligned(16))) unsigned char smem[];
  Ctx c;
  c.ws = p.ws; c.rs = __builtin_amdgcn_make_buffer_rsrc(p.ws, 0, (int)OFF_ACT, 0x00020000);
  c.ra = __builtin_amdgcn_make_buffer_rsrc(p.ws + OFF_ACT, 0, (int)(2 * LB), 0x00020000);
  c.lb = (u32)(size_t)(LAS unsigned char*)smem;
  c.l16 = (threadIdx.x & 63) * 16; c.w = __builtin_amdgcn_readfirstlane(threadIdx.x >> 6); c.bid = blockIdx.x;
  const u32 x = c.bid & 7, i = c.bid >> 3;
  c.sq = 128 * x + (i >= 15 ? 64 : 0); c.sg = 128 * x + 64 * (i >> 4); c.sw = 128 * x + 32 * (i >> 3);
  c.tq = (c.sq >> 4) + c.w; c.tg = (c.sg >> 4) + c.w;
  c.bof = A_OF + ((c.sw >> 4) * 64 + 16 * c.w) * 256; c.boo = A_OO + ((c.sw >> 4) * 32 + 8 * c.w) * 128; c.bhf = A_HF + ((c.sw >> 4) * 192 + 48 * c.w) * 256;
  c.m0x = c.lb + c.w * 4 * KB; c.m0a = c.lb + 3 * c.w * TP;
  { const int l = 0; (void)l; STAMP(0); }
  asm volatile("" ::: "a0", "a255");
  BF<0, 40> Bq; BF<1, 25> Bw; BF<2, 60> Bg; BF<3, 60> Bd;
  Bq.base = OFF_BQ + ((i % 15) * 4 + c.w) * 40 * KB; Bw.base = OFF_BW + ((i & 7) * 4 + c.w) * 25 * KB;
  Bg.base = OFF_BG + ((i & 15) * 4 + c.w) * 60 * KB; Bd.base = OFF_BD + ((i & 7) * 4 + c.w) * 60 * KB;
  XR xr[2];
  Stage sx, sy; Poll pl; AIn ain; VM vm = {}; int mst = 0;
  stages<0, 0, 9>(c, vm, sx, A_XA, c.tq);
  loadB<0, 24>(c, vm, Bq);
  {
    const u32 t = threadIdx.x, lane = t & 63, cc = lane & 7;
#pragma unroll
    for (int hf = 0; hf < 2; ++hf) {
      const GAS u16* src = (const GAS u16*)p.x + (c.sw + hf * 1024 + 8 * c.w + (lane >> 3)) * D + 80 * (i & 7);
      xr[hf].m = *(const GAS u32x4*)(src + 8 * cc); xr[hf].t = *(const GAS u32*)(src + 64 + 2 * cc);
    }
    if (t < 80) *LP(u32x4, c.lb + L_CS + t * 16) = *(const GAS u32x4*)(p.ws + (t < 32 ? OFF_CSQ + (i % 15) * 512 + t * 16 : OFF_CSG + (i & 15) * 768 + (t - 32) * 16));
    if (t >= 224) *LP(u32x4, c.lb + L_ONES + (t - 224) * 16) = u32x4{0x3F803F80u, 0x3F803F80u, 0x3F803F80u, 0x3F803F80u};
    const int z = (int)(lane & 31) - 4 * (int)(lane >> 5) - 8 * (int)c.w;
    *LP(u32x4, c.lb + L_MASK + c.w * KB + c.l16) = u32x4{z >= 0 ? ~0u : 0u, z >= 1 ? ~0u : 0u, z >= 2 ? ~0u : 0u, z >= 3 ? ~0u : 0u};
    *LP(u32x2, c.lb + L_MASK + (4 + (c.w >> 1)) * KB + c.l16 + (c.w & 1) * 8) = u32x2{(z >= 0 ? 0xFFFFu : 0u) | (z >= 1 ? 0xFFFF0000u : 0u), (z >= 2 ? 0xFFFFu : 0u) | (z >= 3 ? 0xFFFF0000u : 0u)};
  }
  u32 adep = a_dep(c, 0) | (a_dep(c, 1) << 16);
  asm volatile("s_waitcnt vmcnt(0)" : "+v"(adep), "+v"(xr[0].m), "+v"(xr[0].t), "+v"(xr[1].m), "+v"(xr[1].t) :: "memory");
  stages<0, 9, 13>(c, vm, sx, 0, 0);
#pragma unroll 1
  for (int l = 0; l < 2; ++l) {
    vm.n = 0; vm.done = -1000; sx.mg[1] = -CARRY1; mst = -CARRYST;
    sfor<0, 24>(LAMBDA(J) { Bq.m[J.v] = -1000; });
    asm volatile("; MARK QX");
    { f32x4 acc[2][4]; EpQ e;
      TS(0, 0);
      gemm_lds<0, 2>(c, vm, sx, l != 0, acc, Bq, LAMBDA(Gp) {
        at<G, 0, 1, 8>(LAMBDA(K) { stage<0, 13 + K.v>(c, vm, sx, 0, 0); });
        at<G, 8, 1, 16>(LAMBDA(K) { ldB<24 + K.v>(c, vm, Bq); });
        at<G, 29, 1, 11>(LAMBDA(K) { stage<1, K.v>(c, vm, sy, A_XA, c.tq); });
      }, LAMBDA(S) { constexpr int s = S.v;
        if (s == 5) { need(vm, mst); BAR0(); if (l) flag_store(c, 10); }
        if (s == 12) poll_issue(c, vm, pl, dep_x(c, c.tq), l ? 10 : 0);
        if (s == 14) { TS(0, 1); poll_check(c, vm, pl); TS(0, 2); }
        if (s == 17) pre_q<0>(c, e);
      });
      TS(0, 3);
      epi_q(c, vm, e, acc); mst = vm.n; stages<1, 11, 13>(c, vm, sy, 0, 0); TS(0, 4); }
    asm volatile("; MARK QY");
    { f32x4 acc[2][4]; EpQ e;
      TS(1, 0);
      gemm_lds<1, 2>(c, vm, sy, l != 0, acc, Bq, LAMBDA(Gp) {
        at<G, 0, 1, 8>(LAMBDA(K) { stage<1, 13 + K.v>(c, vm, sy, 0, 0); });
        at<G, 8, 1, 18>(LAMBDA(K) { ldB<K.v>(c, vm, Bw); });
        at<G, 29, 1, 11>(LAMBDA(K) { a_stage<0, K.v>(c, vm, ain); });
      }, LAMBDA(S) { constexpr int s = S.v;
        if (s == 5) publish(c, vm, mst, 10 * l + 1);
        if (s == 12) a_poll(c, vm, pl, l, adep & 0xFFFF);
        if (s == 14) { TS(1, 1); poll_check(c, vm, pl); TS(1, 2); }
        if (s == 17) pre_q<1>(c, e);
      });
      TS(1, 3);
      epi_q(c, vm, e, acc); mst = vm.n; TS(1, 4); }
    asm volatile("; MARK AX");
    AIn ain2;
    TS(2, 0);
    attn<0>(c, vm, ain, LAMBDA(Gp) {
      at<G, 0, 1, 7>(LAMBDA(K) { ldB<18 + K.v>(c, vm, Bw); });
      at<G, 7, 1, 14>(LAMBDA(K) { ldB<K.v>(c, vm, Bg); });
      at<G, 23, 1, 10>(LAMBDA(K) { a_stage<1, K.v>(c, vm, ain2); });
    }, LAMBDA(S) { constexpr int s = S.v;
      if (s == 0) publish(c, vm, mst, 10 * l + 2);
      if (s == 3) a_poll(c, vm, pl, l, adep >> 16);
      if (s == 4) { TS(2, 1); poll_check(c, vm, pl); TS(2, 2); }
      if (s == 8) { a_stage<1, 10>(c, vm, ain2); TS(2, 3); }
    });
    mst = vm.n; TS(2, 4);
    asm volatile("; MARK AY");
    AW aw;
    TS(3, 0);
    attn<1>(c, vm, ain2, LAMBDA(Gp) {
      at<G, 0, 1, 21>(LAMBDA(K) { ldB<14 + K.v>(c, vm, Bg); });
      at<G, 23, 1, 10>(LAMBDA(K) { ld_w<0, K.v>(c, vm, aw); });
    }, LAMBDA(S) { constexpr int s = S.v;
      if (s == 0) publish(c, vm, mst, 10 * l + 3);
      if (s == 3) poll_issue(c, vm, pl, dep_o(c), 10 * l + 3);
      if (s == 4) { TS(3, 1); poll_check(c, vm, pl); TS(3, 2); }
      if (s == 8) { ld_w<0, 10>(c, vm, aw); ld_w<0, 11>(c, vm, aw); TS(3, 3); }
    });
    mst = vm.n; TS(3, 4);
    asm volatile("; MARK WX");
    { TS(4, 0);
      gemm_reg<0, 5>(c, vm, Bw, aw, LAMBDA(Gp) { at<G, 0, 1, 11>(LAMBDA(K) { ldB<35 + K.v>(c, vm, Bg); }); }, LAMBDA(S) {});
      TS(4, 1);
      need(vm, mst); BARL(); flag_store(c, 10 * l + 4);
      TS(4, 2);
      red_out<0>(c, vm, xr[0], A_XB, nullptr); mst = vm.n;
      TS(4, 3);
      poll_issue(c, vm, pl, dep_o(c), 10 * l + 4);
      loadB<46, 54>(c, vm, Bg);
      publish(c, vm, mst, 10 * l + 5);
      TS(4, 4); }
    asm volatile("; MARK WY");
    { poll_check(c, vm, pl);
      TS(5, 0);
      sfor<0, 12>(LAMBDA(K) { ld_w<1, K.v>(c, vm, aw); });
      loadB<54, 60>(c, vm, Bg);
      gemm_reg<1, 5>(c, vm, Bw, aw, LAMBDA(Gp) {}, LAMBDA(S) { constexpr int s = S.v; if (s == 2) poll_issue(c, vm, pl, dep_x(c, c.tg), 10 * l + 5); });
      TS(5, 1);
      poll_check(c, vm, pl);
      TS(5, 2);
      stages<0, 0, 9>(c, vm, sx, A_XB, c.tg);
      BARL();
      TS(5, 3);
      red_out<1>(c, vm, xr[1], A_XB, nullptr); mst = vm.n;
      stages<0, 9, 13>(c, vm, sx, 0, 0);
      TS(5, 4); }
    asm volatile("; MARK GX");
    { f32x4 acc[3][4]; EpG e;
      TS(6, 0);
      gemm_lds<0, 3>(c, vm, sx, true, acc, Bg, LAMBDA(Gp) {
        at<G, 0, 1, 8>(LAMBDA(K) { stage<0, 13 + K.v>(c, vm, sx, 0, 0); });
        at<G, 8, 1, 25>(LAMBDA(K) { ldB<K.v>(c, vm, Bd); });
        at<G, 37, 1, 21>(LAMBDA(K) { stage<1, K.v>(c, vm, sy, A_XB, c.tg); });
      }, LAMBDA(S) { constexpr int s = S.v;
        if (s == 4) publish(c, vm, mst, 10 * l + 6);
        if (s == 10) poll_issue(c, vm, pl, dep_x(c, c.tg), 10 * l + 6);
        if (s == 12) { TS(6, 1); poll_check(c, vm, pl); TS(6, 2); }
        if (s == 18) pre_g<0>(c, e);
      });
      TS(6, 3);
      epi_g(c, vm, e, acc); mst = vm.n; TS(6, 4); }
    asm volatile("; MARK GY");
    AD<true> ad;
    { f32x4 acc[3][4]; EpG e;
      TS(7, 0);
      gemm_lds<1, 3>(c, vm, sy, true, acc, Bg, LAMBDA(Gp) {
        at<G, 3, 1, 31>(LAMBDA(K) { ldB<25 + K.v>(c, vm, Bd); });
        at<G, 37, 1, 23>(LAMBDA(K) { ld_d<0, K.v>(c, vm, ad); });
      }, LAMBDA(S) { constexpr int s = S.v;
        if (s == 4) publish(c, vm, mst, 10 * l + 7);
        if (s == 10) poll_issue(c, vm, pl, dep_h(c), 10 * l + 7);
        if (s == 12) { TS(7, 1); poll_check(c, vm, pl); TS(7, 2); }
        if (s == 18) pre_g<1>(c, e);
      });
      TS(7, 3);
      epi_g(c, vm, e, acc); mst = vm.n; TS(7, 4); }
    asm volatile("; MARK DX");
    if (l) Bq.base = OOR;
    AD<false> ad2;
    { TS(8, 0);
      gemm_reg<0, 12>(c, vm, Bd, ad, LAMBDA(Gp) {
        at<G, 0, 1, 1>(LAMBDA(K) { ld_d<0, 23>(c, vm, ad); });
        at<G, 1, 1, 4>(LAMBDA(K) { ldB<56 + K.v>(c, vm, Bd); });
        at<G, 5, 1, 10>(LAMBDA(K) { ldB<K.v>(c, vm, Bq); });
      }, LAMBDA(S) { constexpr int s = S.v;
        if (s == 5) publish(c, vm, mst, 10 * l + 8);
        if (s == 10) poll_issue(c, vm, pl, dep_h(c), 10 * l + 8);
      });
      TS(8, 1);
      poll_check(c, vm, pl);
      TS(8, 2);
      sfor<0, 6>(LAMBDA(K) { ld_d<1, K.v>(c, vm, ad2); });
      BARL();
      TS(8, 3);
      red_out<0>(c, vm, xr[0], LB + A_XA, l ? p.out : nullptr); mst = vm.n;
      sfor<6, 10>(LAMBDA(K) { ld_d<1, K.v>(c, vm, ad2); });
      TS(8, 4); }
    asm volatile("; MARK DY");
    { TS(9, 0);
      gemm_reg<1, 12>(c, vm, Bd, ad2, LAMBDA(Gp) {
        at<G, 0, 1, 14>(LAMBDA(K) { ld_d<1, 10 + K.v>(c, vm, ad2); });
        at<G, 14, 1, 12>(LAMBDA(K) { ldB<10 + K.v>(c, vm, Bq); });
      }, LAMBDA(S) { constexpr int s = S.v;
        if (s == 4) { need(vm, mst); BAR0(); if (l == 0) flag_store(c, 9); }
        if (s == 10) { loadB<22, 24>(c, vm, Bq); poll_issue(c, vm, pl, dep_x(c, c.tq), l ? 0 : 9); }
      });
      TS(9, 1);
      poll_check(c, vm, pl);
      TS(9, 2);
      stages<0, 0, 9>(c, vm, sx, LB + A_XA, c.tq);
      BARL();
      TS(9, 3);
      red_out<1>(c, vm, xr[1], LB + A_XA, l ? p.out : nullptr); mst = vm.n;
      stages<0, 9, 13>(c, vm, sx, 0, 0);
      TS(9, 4); }
    if (vm.n - sx.mg[1] < CARRY1 || vm.n - mst < CARRYST || vm.done < Bq.m[23]) carry_count_too_large();
    c.ra = __builtin_amdgcn_make_buffer_rsrc(p.ws + OFF_ACT + LB, 0, (int)LB, 0x00020000);
    sx.src -= LB;
    prof<true>(vm); prof<true>(vm);
    asm volatile("; MARK END");
  }
  asm volatile("s_waitcnt vmcnt(0)" ::: "memory");
}

extern "C" void kernel_launch(void* const* d_in, const int* in_sizes, int n_in, void* d_out, int out_size, void* d_ws, size_t ws_size, hipStream_t stream) {
  Params p;
  p.x = (const u16*)d_in[0]; p.wqkv = (const u16*)d_in[1]; p.wo = (const u16*)d_in[2];
  p.wg = (const u16*)d_in[4]; p.wu = (const u16*)d_in[5]; p.wd = (const u16*)d_in[6];
  p.out = (u16*)d_out; p.ws = (unsigned char*)d_ws;
  (void)hipFuncSetAttribute((const void*)mega_kernel, hipFuncAttributeMaxDynamicSharedMemorySize, LDS_BYTES);
  prep_kernel<<<PREP_LN_BLOCKS + PREP_TILE_BLOCKS + PREP_CS_BLOCKS, 256, 0, stream>>>(p);
  mega_kernel<<<256, 256, LDS_BYTES, stream>>>(p);
}
```

```cpp
#include <hip/hip_runtime.h>
#include <stdint.h>

typedef unsigned short u16;
typedef unsigned u32;
typedef u32 u32x2 __attribute__((ext_vector_type(2)));
typedef u32 u32x4 __attribute__((ext_vector_type(4)));
typedef float f32x2 __attribute__((ext_vector_type(2)));
typedef float f32x4 __attribute__((ext_vector_type(4)));
typedef float f32x16 __attribute__((ext_vector_type(16)));
typedef short bf16x8 __attribute__((ext_vector_type(8)));
typedef short s16x4 __attribute__((ext_vector_type(4)));
typedef __bf16 bf16x2_t __attribute__((ext_vector_type(2)));
#define GAS __attribute__((address_space(1)))
#define LAS __attribute__((address_space(3)))
#define DI __device__ __forceinline__
#define RLX_AGENT __ATOMIC_RELAXED, __HIP_MEMORY_SCOPE_AGENT

constexpr int S = 2048, D = 640, F = 1536;
constexpr u32 KB = 1024;
constexpr u32 OFF_FLAGS = 0;
constexpr u32 OFF_TMO = 32 * KB;
constexpr u32 OFF_CSQ = 64 * KB;
constexpr u32 OFF_CSG = 72 * KB;
constexpr u32 OFF_BQ = 128 * KB;
constexpr u32 OFF_BW = OFF_BQ + 2400 * KB;
constexpr u32 OFF_BG = OFF_BW + 800 * KB;
constexpr u32 OFF_BD = OFF_BG + 3840 * KB;
constexpr u32 A_XA = 0, A_SA = 2560 * KB, A_QKV = A_SA + 128 * KB, A_OF = A_QKV + 7680 * KB, A_OO = A_OF + 2048 * KB, A_XB = A_OO + 512 * KB,
              A_SB = A_XB + 2560 * KB, A_HF = A_SB + 128 * KB, LB = A_HF + 6144 * KB;
constexpr u32 OFF_ACT = OFF_BD + 1920 * KB;
constexpr u32 OFF_DBG = OFF_ACT + 2 * LB;
constexpr u32 OOR = 0x80000000u;
constexpr u32 L_Y = 80 * KB, L_HX = 16 * KB, L_HY = 80 * KB, L_MISC = 144 * KB;
constexpr u32 TP = 3584;
constexpr u32 L_STAT = L_MISC;
constexpr u32 L_CS = L_STAT + 1024;
constexpr u32 L_MASK = L_CS + 1280;
constexpr u32 L_DBG = L_MASK + 6 * KB;
constexpr u32 LDS_BYTES = L_DBG + 1024;
static_assert(LDS_BYTES <= 160 * KB, "");

struct Params { const u16* x; const u16* wqkv; const u16* wo; const u16* wg; const u16* wu; const u16* wd; u16* out; unsigned char* ws; };

DI float bf_lo(u32 v) { return __uint_as_float(v << 16); }
DI float bf_hi(u32 v) { return __uint_as_float(v & 0xffff0000u); }
DI u32 pk2(float a, float b) { f32x2 f = {a, b}; bf16x2_t h = __builtin_convertvector(f, bf16x2_t); return __builtin_bit_cast(u32, h); }
DI float rbf(float a) { return bf_lo(pk2(a, a)); }

DI int ocol(int head, int ds) { return (head & 1) ? (ds < 16 ? 20 * head + 4 + ds : 20 * head + ds - 16) : 20 * head + ds; }
DI u32x2 trread(u32 a) { return __builtin_bit_cast(u32x2, __builtin_amdgcn_ds_read_tr16_b64_v4i16((LAS s16x4*)(size_t)a)); }
constexpr int PB_CS = 78, PB_G = 120, PB_Q = 150, PB_D = 120, PB_W = 50, PB_LN = 256;
constexpr int PREP_BLOCKS = PB_CS + PB_G + PB_Q + PB_D + PB_W + PB_LN;
__global__ void __launch_bounds__(256) prep_kernel(Params p) {
  __shared__ __attribute__((aligned(16))) unsigned char sm[4 * 8704];
  const int tid = threadIdx.x, lane = tid & 63, wv = tid >> 6, q4 = lane >> 4;
  int b = blockIdx.x;
  if (b < PB_CS) {
    const u16* W; int ld, n0;
    if (b < 30) { W = p.wqkv; ld = 3 * D; n0 = 64 * b; } else if (b < 54) { W = p.wg; ld = F; n0 = 64 * (b - 30); } else { W = p.wu; ld = F; n0 = 64 * (b - 54); }
    const GAS u16* src = (const GAS u16*)W + (tid >> 3) * ld + n0 + 8 * (tid & 7);
    u32x4 v[20];
#pragma unroll
    for (int j = 0; j < 20; ++j) v[j] = *(const GAS u32x4*)(src + j * 32 * ld);
    float s[8] = {};
#pragma unroll
    for (int j = 0; j < 20; ++j)
#pragma unroll
      for (int e = 0; e < 4; ++e) { s[2 * e] += bf_lo(v[j][e]); s[2 * e + 1] += bf_hi(v[j][e]); }
    float* red = (float*)sm;
#pragma unroll
    for (int e = 0; e < 8; ++e) red[(tid >> 3) * 65 + 8 * (tid & 7) + e] = s[e];
    __syncthreads();
    if (tid < 64) {
      float t = 0;
#pragma unroll
      for (int j = 0; j < 32; ++j) t += red[j * 65 + tid];
      const int n = n0 + tid;
      if (b < 30) ((float*)(p.ws + OFF_CSQ))[n] = t; else ((float*)(p.ws + OFF_CSG))[(n >> 3) * 16 + ((n >> 1) & 3) * 4 + (b >= 54 ? 2 : 0) + (n & 1)] = t;
    }
    if (b == 0) { for (int i = tid; i < 256 * 32 + 64; i += 256) ((u32*)p.ws)[i] = 0; }
    return;
  }
  b -= PB_CS;
  const u32 L = (u32)(size_t)(LAS unsigned char*)sm + wv * 8704;
  const int rr = lane >> 3, ch = lane & 7;
  const int tq = (lane & 15) >> 2, tp = lane & 3;
  if (b < PB_G) {
    const int job = b * 4 + wv, s = job % 20, fb = job / 20;
    u32x4 g[4], u[4];
#pragma unroll
    for (int j = 0; j < 4; ++j) { const int o = (32 * s + 8 * j + rr) * F + 64 * fb + 8 * ch; g[j] = *(const GAS u32x4*)((const GAS u16*)p.wg + o); u[j] = *(const GAS u32x4*)((const GAS u16*)p.wu + o); }
#pragma unroll
    for (int j = 0; j < 4; ++j) { const u32 a = L + (8 * j + rr) * 272 + ch * 32;
      *(LAS u32x4*)(size_t)a = u32x4{g[j].x, u[j].x, g[j].y, u[j].y}; *(LAS u32x4*)(size_t)(a + 16) = u32x4{g[j].z, u[j].z, g[j].w, u[j].w}; }
    __builtin_amdgcn_wave_barrier();
#pragma unroll
    for (int j = 0; j < 8; ++j) { const u32 a = L + (8 * q4 + tq) * 272 + j * 32 + tp * 8; const u32x2 lo = trread(a), hi = trread(a + 4 * 272);
      const int gn = 8 * fb + j;
      *(GAS u32x4*)(p.ws + OFF_BG + (((gn / 3) * 20 + s) * 3 + gn % 3) * KB + lane * 16) = u32x4{lo.x, lo.y, hi.x, hi.y}; }
    return;
  }
  b -= PB_G;
  if (b < PB_Q + PB_D + PB_W) {
    const int kind = b < PB_Q ? 0 : b < PB_Q + PB_D ? 1 : 2;
    const int job = (b - (kind == 0 ? 0 : kind == 1 ? PB_Q : PB_Q + PB_D)) * 4 + wv, ns = kind == 1 ? 48 : 20, s = job % ns, cb = job / ns;
    const GAS u16* W = (const GAS u16*)(kind == 0 ? p.wqkv : kind == 1 ? p.wd : p.wo); const int ld = kind == 0 ? 3 * D : D;
    u32x4 v[4];
#pragma unroll
    for (int j = 0; j < 4; ++j) {
      int k = 32 * s + 8 * j + rr;
      if (kind == 2) { const int w = s / 5, sp = s % 5; k = sp < 4 ? ocol(8 * w + 2 * sp + (j >> 1), 4 * (j & 1) + (rr & 3) + 8 * (rr >> 2)) : ocol(8 * w + 2 * j + (rr >> 2), 16 + (rr & 3)); }
      v[j] = *(const GAS u32x4*)(W + k * ld + 64 * cb + 8 * ch);
    }
#pragma unroll
    for (int j = 0; j < 4; ++j) *(LAS u32x4*)(size_t)(L + (8 * j + rr) * 144 + ch * 16) = v[j];
    __builtin_amdgcn_wave_barrier();
#pragma unroll
    for (int j = 0; j < 4; ++j) {
      const u32 a = L + (8 * q4 + tq) * 144 + (kind == 0 ? (j >> 1) * 64 + tp * 16 + (j & 1) * 8 : j * 32 + tp * 8);
      const u32x2 lo = trread(a), hi = trread(a + 4 * 144);
      const int gn = 4 * cb + j, cg = gn / 5, nf = gn % 5;
      const u32 dst = kind == 0 ? OFF_BQ + (((gn >> 1) * 20 + s) * 2 + (j & 1)) * KB : kind == 1 ? OFF_BD + ((cg * 48 + s) * 5 + nf) * KB : OFF_BW + ((cg * 20 + s) * 5 + nf) * KB;
      *(GAS u32x4*)(p.ws + dst + lane * 16) = u32x4{lo.x, lo.y, hi.x, hi.y};
    }
    return;
  }
  b -= PB_Q + PB_D + PB_W;
  {
    const int row = b * 8 + wv * 2 + (lane >> 5), l32 = lane & 31;
    const GAS u32x4* src = (const GAS u32x4*)((const GAS u16*)p.x + row * D);
    u32x4 v[3]; v[0] = src[l32]; v[1] = src[32 + l32]; v[2] = l32 < 16 ? src[64 + l32] : u32x4{0, 0, 0, 0};
    float f[24], s = 0;
#pragma unroll
    for (int j = 0; j < 12; ++j) { f[2 * j] = bf_lo(v[j >> 2][j & 3]); f[2 * j + 1] = bf_hi(v[j >> 2][j & 3]); s += f[2 * j] + f[2 * j + 1]; }
    for (int m = 16; m >= 1; m >>= 1) s += __shfl_xor(s, m);
    const float mean = s * (1.0f / D);
    float q = 0, q2 = 0;
#pragma unroll
    for (int j = 0; j < 16; ++j) { const float d = f[j] - mean; q += d * d; }
#pragma unroll
    for (int j = 16; j < 24; ++j) { const float d = f[j] - mean; q2 += d * d; }
    if (l32 < 16) q += q2;
    for (int m = 16; m >= 1; m >>= 1) q += __shfl_xor(q, m);
    const float mb = rbf(mean), sd = rbf(sqrtf(rbf(rbf(q * (1.0f / D)) + 1e-5f)));
    unsigned char* dst = p.ws + OFF_ACT + A_XA + (row >> 4) * (80 * 256) + (row & 15) * 16 + l32 * 256;
#pragma unroll
    for (int c = 0; c < 3; ++c) {
      u32x4 o;
#pragma unroll
      for (int j = 0; j < 4; ++j) o[j] = pk2(rbf(f[8 * c + 2 * j] - mb) / sd, rbf(f[8 * c + 2 * j + 1] - mb) / sd);
      if (c < 2 || l32 < 16) *(u32x4*)(dst + c * 32 * 256) = o;
    }
  }
}

typedef __amdgpu_buffer_rsrc_t rsrc_t;
#define SB() __builtin_amdgcn_sched_barrier(0)
template <int I> struct IC { static constexpr int v = I; };
template <int B, int E, class Fn> DI void sfor(Fn&& f) { if constexpr (B < E) { f(IC<B>{}); sfor<B + 1, E>(f); } }
#define LAMBDA(S) [&](auto S) __attribute__((always_inline))
template <class T> DI void pin(T& d) { asm volatile("" : "+v"(d)); }
DI u32 sop(u32 v) { asm volatile("" : "+s"(v)); return v; }
DI u32 vop(u32 v) { asm volatile("" : "+v"(v)); return v; }
struct Ctx {
  rsrc_t rs, ra;
  u32 lb; unsigned char* ws;
  u32 l16, w, bid;
  u32 sq, sw, cg;
  u32 tq;
  u32 bof, boo, bhf;
  u32 m0x, m0a;
  u32 foff;
  u32 dxx, doh, daa;
  u32 rw, rpa, rpt, rvo, rvt, rvs;
};
template <int G, int START, int STRIDE, int N, class Fn> DI void at(Fn&& f) {
  if constexpr (G >= START && (G - START) % STRIDE == 0 && (G - START) / STRIDE < N) f(IC<(G - START) / STRIDE>{});
}
#define LP(T, a) ((LAS T*)(size_t)(a))

struct VM { int n, done;
};
template <bool W, bool W2 = W> DI void prof(VM& vm) {
}
DI void need(VM& vm, int mark) {
  if (mark > vm.done) { const int k = vm.n - mark; if (k < 63) asm volatile("s_waitcnt vmcnt(%0)" :: "i"(k) : "memory"); vm.done = mark; }
}
template <int SLOT, bool SC1, int OFF> DI int ld16(rsrc_t rs, VM& vm, u32x4& d, u32 voff, u32 soff) {
  if constexpr (SLOT >= 0 && SC1) asm volatile("buffer_load_dwordx4 a[%c0:%c1], %2, %3, %4 offen offset:%5 sc1" :: "i"(4 * SLOT), "i"(4 * SLOT + 3), "v"(voff), "s"(rs), "s"(soff), "i"(OFF) : "memory");
  else if constexpr (SLOT >= 0) asm volatile("buffer_load_dwordx4 a[%c0:%c1], %2, %3, %4 offen offset:%5" :: "i"(4 * SLOT), "i"(4 * SLOT + 3), "v"(voff), "s"(rs), "s"(soff), "i"(OFF) : "memory");
  else if constexpr (SC1) asm volatile("buffer_load_dwordx4 %0, %1, %2, %3 offen offset:%4 sc1" : "=v"(d) : "v"(voff), "s"(rs), "s"(soff), "i"(OFF) : "memory");
  else asm volatile("buffer_load_dwordx4 %0, %1, %2, %3 offen offset:%4" : "=v"(d) : "v"(voff), "s"(rs), "s"(soff), "i"(OFF) : "memory");
  return ++vm.n;
}
template <int OFF> DI int ld8c(rsrc_t rs, VM& vm, u32x2& d, u32 voff, u32 soff) {
  asm volatile("buffer_load_dwordx2 %0, %1, %2, %3 offen offset:%4 sc1" : "=v"(d) : "v"(voff), "s"(rs), "s"(soff), "i"(OFF) : "memory");
  return ++vm.n;
}
template <bool FIRST, int SA, int SBB> DI void mma(f32x4& d, const u32x4& a, const u32x4& b) {
#define MMA_(D_, C_) \
  if constexpr (SA >= 0 && SBB >= 0) asm volatile("v_mfma_f32_16x16x32_bf16 %0, a[%c1:%c2], a[%c3:%c4], " C_ : D_(d) : "i"(4 * SA), "i"(4 * SA + 3), "i"(4 * SBB), "i"(4 * SBB + 3)); \
  else if constexpr (SA >= 0) asm volatile("v_mfma_f32_16x16x32_bf16 %0, a[%c1:%c2], %3, " C_ : D_(d) : "i"(4 * SA), "i"(4 * SA + 3), "v"(b)); \
  else if constexpr (SBB >= 0) asm volatile("v_mfma_f32_16x16x32_bf16 %0, %3, a[%c1:%c2], " C_ : D_(d) : "i"(4 * SBB), "i"(4 * SBB + 3), "v"(a)); \
  else asm volatile("v_mfma_f32_16x16x32_bf16 %0, %1, %2, " C_ : D_(d) : "v"(a), "v"(b));
  if constexpr (FIRST) { MMA_("=&v", "0") } else { MMA_("+v", "%0") }
#undef MMA_
}
DI void st16c(const Ctx& c, VM& vm, u32 voff, u32x4 v) { ++vm.n; __builtin_amdgcn_raw_buffer_store_b128(v, c.ra, voff, 0, 16); }
DI void st8c(const Ctx& c, VM& vm, u32 voff, u32x2 v) { ++vm.n; __builtin_amdgcn_raw_buffer_store_b64(v, c.ra, voff, 0, 16); }
DI void st4c(const Ctx& c, VM& vm, u32 voff, u32 v) { ++vm.n; __builtin_amdgcn_raw_buffer_store_b32(v, c.ra, voff, 0, 16); }
template <u32 LOFF, u32 SOFF> DI void glds_head(const Ctx& c, VM& vm, u32& cur, u32 m0b, u32 sb, u32 voff) {
  ++vm.n;
  asm volatile("s_add_u32 m0, %1, %2\n\ts_add_u32 %0, %3, %4\n\tbuffer_load_dwordx4 %5, %6, %0 offen sc1 lds"
               : "=&s"(cur) : "s"(m0b), "i"(LOFF), "s"(sb), "i"(SOFF), "v"(voff), "s"(c.ra) : "memory", "scc");
}
template <int OFF> DI void glds_next(const Ctx& c, VM& vm, u32 cur, u32 voff) {
  ++vm.n;
  asm volatile("buffer_load_dwordx4 %0, %1, %2 offen offset:%3 sc1 lds" :: "v"(voff), "s"(c.ra), "s"(cur), "i"(OFF) : "memory");
}
#define BARL() asm volatile("s_waitcnt lgkmcnt(0)\n\ts_barrier" ::: "memory")
#define BAR0() asm volatile("s_barrier" ::: "memory")
extern "C" __device__ void carry_count_too_large() __attribute__((error("carry count too large")));

#define STAMP(k) do {} while (0)
#define TS(t, k) STAMP(1 + ((10 * l + (t)) * 5 + (k)))

struct Poll { u32 v, v2, off, need; int mark, mark2; };
DI void poll_issue(const Ctx& c, VM& vm, Poll& p, u32 off, u32 need) {
  p.off = off; p.need = need;
  asm volatile("buffer_load_dword %0, %1, %2, 0 offen sc1" : "=v"(p.v) : "v"(p.off), "s"(c.rs) : "memory");
  p.mark = ++vm.n;
}
DI void poll_again(const Ctx& c, VM& vm, Poll& p) {
  asm volatile("buffer_load_dword %0, %1, %2, 0 offen sc1" : "=v"(p.v2) : "v"(p.off), "s"(c.rs) : "memory");
  p.mark2 = ++vm.n;
}
template <bool TWO> DI void poll_check(const Ctx& c, VM& vm, Poll& p) {
  need(vm, p.mark); pin(p.v);
  if (__builtin_expect(!__all(p.v >= p.need), 0)) {
    bool ok = false;
    if constexpr (TWO) {
      asm volatile("s_waitcnt vmcnt(%1) ; SECOND" : "+v"(p.v2) : "i"(vm.n - p.mark2) : "memory");
      ok = __all(p.v2 >= p.need);
    }
#pragma unroll 1
    for (u32 spins = 0; !ok; ++spins) {
      u32 v;
      asm volatile("buffer_load_dword %0, %1, %2, 0 offen sc1\n\ts_waitcnt vmcnt(0)" : "=&v"(v) : "v"(p.off), "s"(c.rs) : "memory");
      ok = __all(v >= p.need);
      if (spins > (1u << 16)) { __hip_atomic_store((GAS u32*)(c.ws + OFF_TMO), 1u, RLX_AGENT); break; }
    }
  }
  asm volatile("" ::: "memory");
}
DI void poll_retire(VM& vm, Poll& p) { need(vm, p.mark2); pin(p.v2); }
DI void flag_store(const Ctx& c, VM& vm, u32 val) { ++vm.n; __builtin_amdgcn_raw_buffer_store_b32(val, c.rs, c.foff, 0, 16); }
DI void publish(const Ctx& c, VM& vm, int mark, u32 val) { need(vm, mark); BAR0(); flag_store(c, vm, val); }
DI u32 lo16(u32 v) { return vop(v) & 0xFFFF; }
DI u32 hi16(u32 v) { return vop(v) >> 16; }

template <int ID> constexpr int slotB(int j) { return ID == 0 ? (j < 10 ? -1 : 15 + j) : ID == 1 ? j : ID == 2 ? (j < 39 ? 25 + j : -1) : j; }
template <int ID_, int N> struct BF { static constexpr int ID = ID_; u32x4 f[N]; int m[N]; u32 base, cur; };
template <int J, int ID, int N> DI void ldB(const Ctx& c, VM& vm, BF<ID, N>& B) {
  if constexpr ((J & 3) == 0) B.cur = sop(B.base) + J * KB;
  B.m[J] = ld16<slotB<ID>(J), false, (J & 3) * 1024>(c.rs, vm, B.f[J], c.l16, B.cur);
}
template <int LO, int HI, class BT> DI void loadB(const Ctx& c, VM& vm, BT& B) { sfor<LO, HI>(LAMBDA(J) { ldB<J.v>(c, vm, B); }); }
template <int CTRL> DI float dpp_add(float v) { return v + __int_as_float(__builtin_amdgcn_update_dpp(0, __float_as_int(v), CTRL, 0xF, 0xF, true)); }

#define STAT0 4
template <int HALF> constexpr u32 img_off(int g) { return HALF == 0 ? g * 16 * KB : (g < 4 ? L_Y + g * 16 * KB : 0); }
struct Stage { u32x4 sv; u32 src, cur; int mg[5]; };
template <int HALF, int K> DI void stage(const Ctx& c, VM& vm, Stage& st, u32 xoff, u32 tf) {
  if constexpr (K == 0) {
    const u32 t = sop(tf), l = vop(c.l16);
    st.src = xoff + HALF * 64 * 20480 + t * 20480;
    ld16<-1, true, 0>(c.ra, vm, st.sv, ((l << 2) & 0x3C0) + ((l >> 4) & 0x30), xoff + (A_SA - A_XA) + HALF * 65536 + t * 1024);
  } else {
    constexpr int g = (K - 1) >> 2, j = (K - 1) & 3;
    if constexpr (j == 0) glds_head<img_off<HALF>(g), g * 4 * KB>(c, vm, st.cur, c.m0x, st.src, c.l16); else glds_next<j * 1024>(c, vm, st.cur, c.l16);
    st.mg[g] = vm.n;
  }
}
template <int HALF, int LO, int HI> DI void stages(const Ctx& c, VM& vm, Stage& st, u32 xoff, u32 tf) { sfor<LO, HI>(LAMBDA(K) { stage<HALF, K.v>(c, vm, st, xoff, tf); }); }
template <int HALF, int NF, int SYNC, int PG, class BT, class HL, class HA> DI void gemm_lds(const Ctx& c, VM& vm, Stage& st, bool use_stats, int mst, u32 fv, f32x4 (&acc)[NF][4], BT& B, HL hl, HA ha) {
  const u32 b0 = vop(c.lb + img_off<HALF>(0) + c.l16), b1 = vop(c.lb + img_off<HALF>(4) + c.l16);
  auto rd = [&](int s, int tf) __attribute__((always_inline)) { return *LP(const u32x4, (s < 16 ? b0 + (s >> 2) * 16 * KB : b1) + tf * 4 * KB + (s & 3) * KB); };
  prof<true>(vm);
  auto cover = [](int g) constexpr { int e = g; while (e < 4 && !((SYNC >> (e + 1)) & 1)) ++e; return e; };
  need(vm, st.mg[cover(0)]); BAR0(); pin(st.sv);
  u32x4 a[2][4]; float s1, s2;
#pragma unroll
  for (int tf = 0; tf < 4; ++tf) a[0][tf] = rd(0, tf);
  SB();
  sfor<0, 20>(LAMBDA(S) {
    constexpr int s = S.v;
    if constexpr ((s & 3) == 3 && s < 19 && ((SYNC >> ((s + 1) >> 2)) & 1)) {
      need(vm, st.mg[cover((s + 1) >> 2)]); if ((s + 1) >> 2 == PG) need(vm, mst);
      BAR0();
    }
    need(vm, B.m[s * NF + NF - 1]);
    __builtin_amdgcn_s_waitcnt(0xC07F);
    prof<false>(vm);
    SB();
    sfor<0, 4 * NF>(LAMBDA(J) {
      constexpr int nf = J.v >> 2, tf = J.v & 3;
      mma<s == 0, slotB<BT::ID>(s * NF + nf), -1>(acc[nf][tf], B.f[s * NF + nf], a[s & 1][tf]);
      if constexpr (s < 19 && nf == 0) { a[(s + 1) & 1][tf] = rd(s + 1, tf); SB(); }
      if constexpr (J.v >= 4 && (J.v - 4) % NF == 0) { SB(); hl(IC<s * NF + (J.v - 4) / NF>{}); SB(); }
      if constexpr (J.v == 0 && s == 4 * PG - 1) { flag_store(c, vm, fv); SB(); }
      if constexpr (J.v == 5) { SB(); ha(S); SB(); }
      if constexpr (J.v == 7 - (NF == 3) && s >= STAT0 && s < STAT0 + 6) {
        SB();
        if constexpr (s == STAT0) { s1 = __uint_as_float(st.sv.x) + __uint_as_float(st.sv.z); s2 = __uint_as_float(st.sv.y) + __uint_as_float(st.sv.w); pin(s1); pin(s2); }
        if constexpr (s == STAT0 + 1) { auto t = __builtin_amdgcn_permlane16_swap(__float_as_uint(s1), __float_as_uint(s2), false, false); s1 = __uint_as_float(t[0]) + __uint_as_float(t[1]); s2 = s1; pin(s1); pin(s2); }
        if constexpr (s == STAT0 + 2) { auto t = __builtin_amdgcn_permlane32_swap(__float_as_uint(s1), __float_as_uint(s2), false, false); s1 = __uint_as_float(t[0]) + __uint_as_float(t[1]); s2 = s1; pin(s1); pin(s2); }
        if constexpr (s == STAT0 + 3) { auto t = __builtin_amdgcn_permlane16_swap(__float_as_uint(s1), __float_as_uint(s2), false, false); s1 = __uint_as_float(t[0]) * (-1.0f / D); s2 = __uint_as_float(t[1]) * (1.0f / D); pin(s1); pin(s2); }
        if constexpr (s == STAT0 + 4) { s2 = __builtin_fmaxf(s2 - s1 * s1, 0.f) + 1e-5f; pin(s2); }
        if constexpr (s == STAT0 + 5) { float rstd = __builtin_amdgcn_rsqf(s2); if (!use_stats) { s1 = 0.f; rstd = 1.f; }
          *LP(f32x2, c.lb + L_STAT + HALF * 512 + c.w * 128 + ((c.l16 >> 1) & 0x78)) = f32x2{s1, rstd}; }
        SB();
      }
    });
    SB();
  });
  asm volatile("s_nop 7");
  prof<true>(vm);
  SB();
}
struct EpQ { f32x4 cs[2]; f32x2 st[4]; u32 vo; float qs; };
struct EpG { f32x4 cs[3]; f32x2 st[4]; u32 vo; };
template <int HALF> DI void pre_q(const Ctx& c, EpQ& e) {
  const u32 l = vop(c.l16), cg = sop(c.cg);
  const u32 pc = c.lb + L_CS + c.w * 128 + ((l >> 3) & 0x60), ps = c.lb + L_STAT + HALF * 512 + ((l >> 1) & 0x78);
  e.cs[0] = *LP(const f32x4, pc); e.cs[1] = *LP(const f32x4, pc + 16);
#pragma unroll
  for (int tf = 0; tf < 4; ++tf) e.st[tf] = *LP(const f32x2, ps + tf * 128);
  e.qs = cg < 5 ? 0.22360679774997896f * 1.4426950408889634f : 1.0f;
  const u32 base = cg < 15 ? A_QKV + ((16 * cg + 4 * c.w) * 2048 + c.sq + HALF * 1024) * 16 : OOR;
  e.vo = base + ((l >> 8) << 15) + (l & 0xF0);
}
DI void epi_q(const Ctx& c, VM& vm, const EpQ& e, const f32x4 (&acc)[2][4]) {
#pragma unroll
  for (int tf = 0; tf < 4; ++tf) {
    const float rs = e.st[tf].y * e.qs;
    const f32x4 a = (acc[0][tf] + e.st[tf].x * e.cs[0]) * rs, b = (acc[1][tf] + e.st[tf].x * e.cs[1]) * rs;
    st16c(c, vm, e.vo + tf * 256, u32x4{pk2(a.x, a.y), pk2(a.z, a.w), pk2(b.x, b.y), pk2(b.z, b.w)});
  }
}
template <int HALF> DI void pre_g(const Ctx& c, EpG& e) {
  const u32 l = vop(c.l16), cg = sop(c.cg);
  const u32 pc = c.lb + L_CS + 512 + c.w * 192 + ((l >> 4) & 0x30), ps = c.lb + L_STAT + HALF * 512 + ((l >> 1) & 0x78);
#pragma unroll
  for (int nf = 0; nf < 3; ++nf) e.cs[nf] = *LP(const f32x4, pc + nf * 64);
#pragma unroll
  for (int tf = 0; tf < 4; ++tf) e.st[tf] = *LP(const f32x2, ps + tf * 128);
  e.vo = A_HF + (((c.sq + HALF * 1024) >> 4) * 192 + 12 * cg + 3 * c.w) * 256 + (l >> 8) * (192 * 256) + (l & 0xF0);
}
DI void epi_g(const Ctx& c, VM& vm, const EpG& e, const f32x4 (&acc)[3][4]) {
#pragma unroll
  for (int nf = 0; nf < 3; ++nf) {
    u32 R[4];
#pragma unroll
    for (int tf = 0; tf < 4; ++tf) { const f32x4 a = acc[nf][tf] + e.st[tf].x * e.cs[nf]; const float r2 = e.st[tf].y * e.st[tf].y; R[tf] = pk2(a.x * r2 * a.z, a.y * r2 * a.w); }
    { auto t = __builtin_amdgcn_permlane32_swap(R[0], R[2], false, false); R[0] = t[0]; R[2] = t[1]; }
    { auto t = __builtin_amdgcn_permlane32_swap(R[1], R[3], false, false); R[1] = t[0]; R[3] = t[1]; }
    { auto t = __builtin_amdgcn_permlane16_swap(R[0], R[1], false, false); R[0] = t[0]; R[1] = t[1]; }
    { auto t = __builtin_amdgcn_permlane16_swap(R[2], R[3], false, false); R[2] = t[0]; R[3] = t[1]; }
    st16c(c, vm, e.vo + nf * 256, u32x4{R[0], R[1], R[2], R[3]});
  }
}

struct AW { u32x4 f[2][4]; u32x2 o[2][2]; u32 s0, vo; int m[2][5]; static constexpr int slot(int, int) { return -1; } };
template <bool X> struct AD { u32x4 f[2][12]; u32 cur; int m[2][12]; static constexpr int slot(int tf, int s) { return X && tf == 1 && s >= 8 ? 52 + s : -1; } };
template <int HALF, int K> DI void ld_w(const Ctx& c, VM& vm, AW& a) {
  constexpr int tf = K / 6, j = K % 6;
  if constexpr (j == 0) a.s0 = sop(c.bof) + (HALF * 64 + tf) * 64 * 256;
  if constexpr (K == 4) { const u32 l = vop(c.l16); a.vo = (l & 0x300) + ((l >> 1) & 0x78); }
  if constexpr (j == 4) a.s0 = sop(c.boo) + (HALF * 64 + tf) * 32 * 128;
  if constexpr (j < 4) a.m[tf][j] = ld16<-1, true, j * 1024>(c.ra, vm, a.f[tf][j], c.l16, a.s0);
  else a.m[tf][4] = ld8c<(j - 4) * 128>(c.ra, vm, a.o[tf][j - 4], a.vo, a.s0);
}
template <int NS, int HALF> constexpr int kord(int p) { return NS == 12 && HALF == 1 ? (p + 5) % 12 : p; }
template <int HALF, int K, bool X> DI void ld_d(const Ctx& c, VM& vm, AD<X>& a) {
  constexpr int tf = K / 12, s = kord<12, HALF>(K % 12);
  if constexpr (K % 12 == 0 || (s & 3) == 0) a.cur = sop(c.bhf) + (HALF * 64 + tf) * 192 * 256 + (s & ~3) * KB;
  a.m[tf][s] = ld16<AD<X>::slot(tf, s), true, (s & 3) * 1024>(c.ra, vm, a.f[tf][s], c.l16, a.cur);
}
template <int TF, int S> DI u32x4 get_a(VM& vm, AW& a) {
  need(vm, a.m[TF][S]);
  if constexpr (S < 4) return a.f[TF][S];
  else { pin(a.o[TF][0]); pin(a.o[TF][1]); return u32x4{a.o[TF][0].x, a.o[TF][0].y, a.o[TF][1].x, a.o[TF][1].y}; }
}
template <int TF, int S, bool X> DI u32x4 get_a(VM& vm, AD<X>& a) { need(vm, a.m[TF][S]); if constexpr (AD<X>::slot(TF, S) < 0) return a.f[TF][S]; else return u32x4{}; }
template <int HALF, int NS, class BT, class AT, class HL, class HA> DI void gemm_reg(const Ctx& c, VM& vm, BT& B, AT& a, HL hl, HA ha) {
  f32x4 acc[10];
  const u32 p0 = vop(c.rw) + (HALF ? L_HY - L_HX : 0), p1 = p0 ^ 16;
  auto wr = [&](int j) __attribute__((always_inline)) { *LP(f32x4, (((j % 5) & 1) ? p1 : p0) + j * KB) = acc[j]; SB(); };
  sfor<0, 2 * NS>(LAMBDA(P) {
    constexpr int tf = P.v / NS, s = kord<NS, HALF>(P.v % NS);
    if constexpr ((P.v & 1) == 0) prof<true, false>(vm);
    need(vm, B.m[s * 5 + 4]);
    const u32x4 o = get_a<tf, s>(vm, a);
    SB();
    sfor<0, 5>(LAMBDA(J) { constexpr int nf = J.v, g = P.v * 5 + nf;
      mma<P.v % NS == 0, slotB<BT::ID>(s * 5 + nf), AT::slot(tf, s)>(acc[tf * 5 + nf], B.f[s * 5 + nf], o);
      if constexpr (g % 4 == 1) { SB(); hl(IC<g / 4>{}); SB(); }
      if constexpr (g % 10 == 4) { SB(); ha(IC<g / 10>{}); SB(); }
      if constexpr (g >= NS * 5 + 3 && g < NS * 5 + 23 && (g - NS * 5 - 3) % 4 == 0) { SB(); wr((g - NS * 5 - 3) / 4); } });
    SB();
  });
  prof<true>(vm);
  asm volatile("s_nop 3"); SB();
  sfor<5, 10>(LAMBDA(J) { wr(J.v); });
  prof<true>(vm);
}
struct XR { u32x4 m; u32 t; };
struct Red { f32x4 a, b; f32x2 t; u32 pa, pb, pt, vo, vt, vs; };
template <int HALF> DI void red_pre(const Ctx& c, Red& r, const XR& xr, u32 xoff) {
  r.a = f32x4{bf_lo(xr.m.x), bf_hi(xr.m.x), bf_lo(xr.m.y), bf_hi(xr.m.y)}; r.b = f32x4{bf_lo(xr.m.z), bf_hi(xr.m.z), bf_lo(xr.m.w), bf_hi(xr.m.w)}; r.t = f32x2{bf_lo(xr.t), bf_hi(xr.t)};
  r.pa = vop(c.rpa) + (HALF ? L_HY - L_HX : 0); r.pb = r.pa ^ 16; r.pt = vop(c.rpt) + (HALF ? L_HY - L_HX : 0);
  r.vo = vop(c.rvo) + (xoff + HALF * 64 * 20480); r.vt = vop(c.rvt) + (xoff + HALF * 64 * 20480); r.vs = vop(c.rvs) + (xoff + HALF * 65536);
  pin(r.a); pin(r.b); pin(r.t); pin(r.pa); pin(r.pb); pin(r.pt); pin(r.vo); pin(r.vt); pin(r.vs);
}
template <int HALF> DI void red_out(const Ctx& c, VM& vm, Red& r, XR& xr, u16* out) {
  prof<true>(vm);
  f32x4 pa[4], pb[4]; f32x2 pt[4];
#pragma unroll
  for (int ww = 0; ww < 4; ++ww) { pa[ww] = *LP(const f32x4, r.pa + ww * 10 * KB); pb[ww] = *LP(const f32x4, r.pb + ww * 10 * KB); }
#pragma unroll
  for (int ww = 0; ww < 4; ++ww) pt[ww] = *LP(const f32x2, r.pt + ww * 10 * KB);
  SB();
  const f32x4 a = ((r.a + pa[0]) + pa[1]) + (pa[2] + pa[3]), b = ((r.b + pb[0]) + pb[1]) + (pb[2] + pb[3]);
  xr.m = u32x4{pk2(a.x, a.y), pk2(a.z, a.w), pk2(b.x, b.y), pk2(b.z, b.w)};
  st16c(c, vm, r.vo, xr.m);
  const f32x2 t = ((r.t + pt[0]) + pt[1]) + (pt[2] + pt[3]);
  xr.t = pk2(t.x, t.y);
  st4c(c, vm, r.vt, xr.t);
  const f32x2 sp = (a.xy + a.zw) + (b.xy + b.zw) + t;
  const f32x2 qp = a.xy * a.xy + (a.zw * a.zw + (b.xy * b.xy + (b.zw * b.zw + t * t)));
  float s1 = sp.x + sp.y, s2 = qp.x + qp.y;
  s1 = dpp_add<0xB1>(s1); s2 = dpp_add<0xB1>(s2); s1 = dpp_add<0x4E>(s1); s2 = dpp_add<0x4E>(s2); s1 = dpp_add<0x141>(s1); s2 = dpp_add<0x141>(s2);
  st8c(c, vm, r.vs, u32x2{__float_as_uint(s1), __float_as_uint(s2)});
  if (out) {
    const u32 lane = vop(c.l16) >> 4;
    GAS u16* o = (GAS u16*)out + (c.sw + HALF * 1024 + 8 * c.w + (lane >> 3)) * D + 80 * ((c.bid >> 3) & 7);
    *(GAS u32x4*)(o + 8 * (lane & 7)) = xr.m; *(GAS u32*)(o + 64 + 2 * (lane & 7)) = xr.t;
  }
  prof<true>(vm);
}

struct AIn { u32x4 Q0; u32x2 qc; u32 v0, v1, v2, cur; int r0, mark; };
DI u32 wg_qg(u32 RG, u32 cg) { return 2 * (RG >> 2) + (cg >> 3) + 8 * ((RG & 3) * 8 + (cg & 7)); }
DI u32 a_head(u32 bid) { return 4 * (bid & 7) + ((bid >> 3) & 3); }
DI u32 a_dep(const Ctx& c, int half) {
  const int h = a_head(c.bid), gp = half * 8 + (c.bid >> 5), clo = 5 * (h >> 1) + 2 * (h & 1), lane = c.l16 >> 4;
  const int j = lane < 36 ? lane : 0, sec = j / 12, lh = (j / 6) & 1, pi = (j >> 1) % 3, rg = j & 1;
  int g2 = gp - (sec == 0 ? 0 : pi); if (g2 < 0) g2 = gp;
  return (wg_qg(2 * (g2 & 7) + rg, (80 * sec + clo + 2 * lh) >> 4) << 7) | (g2 >> 3);
}
DI void a_poll(const Ctx& c, VM& vm, Poll& p, int l, u32 dep) { poll_issue(c, vm, p, dep & ~1u, 10 * l + 1 + (dep & 1)); }
template <int HALF, int K> DI void a_stage(const Ctx& c, VM& vm, AIn& in) {
  if constexpr (K == 0) {
    const u32 bid = sop(c.bid), h = a_head(bid), odd = h & 1, cA = 5 * (h >> 1) + 3 * odd, cC = 5 * (h >> 1) + 2, l = vop(c.l16), r16 = l & 0x1F0; const bool hh = l >= 512;
    in.r0 = (HALF * 8 + (bid >> 5)) * 128;
    in.v0 = (hh ? 81 + cA : 80 + cA) * 32768 + r16;
    ld16<-1, true, 0>(c.ra, vm, in.Q0, in.v0, A_QKV - 80 * 32768 + (in.r0 + 32 * c.w) * 16);
    in.v1 = (hh ? 160 + cA : 80 + cC) * 32768 + r16 - 1024;
    in.v2 = (hh ? 160 + cC : 161 + cA) * 32768 + (r16 ^ (hh ? 128 : 64)) - 2048;
    in.cur = A_QKV - 80 * 32768 + (in.r0 + 32 * c.w) * 16 + odd * 8;
  } else if constexpr (K == 1) {
    ld8c<1024>(c.ra, vm, in.qc, in.v1, in.cur);
    const u32 po = c.m0a + (HALF ? L_HY : L_HX) + 3072 + (vop(c.l16) >> 1);
#pragma unroll
    for (int u = 0; u < 3; ++u) *LP(u32x2, po + u * TP) = u32x2{0x3F803F80u, 0x3F803F80u};
  } else {
    constexpr int u = (K - 2) / 3, j = (K - 2) % 3;
    if constexpr (j == 0) {
      int krow = in.r0 - 256 + 96 * (int)c.w + 32 * u; if (HALF == 0 && krow < 0) krow = in.r0;
      glds_head<(HALF ? L_HY : L_HX) + u * TP, A_QKV>(c, vm, in.cur, c.m0a, krow * 16, in.v0);
    } else glds_next<j * 1024>(c, vm, in.cur, j == 1 ? in.v1 : in.v2);
    in.mark = vm.n;
  }
}
DI float mx3(float a, float b, float c) { float r; asm("v_max3_f32 %0, %1, %2, %3" : "=v"(r) : "v"(a), "v"(b), "v"(c)); return r; }
DI void smfma0(f32x16& d, u32x4 a, u32x4 b) { asm volatile("v_mfma_f32_32x32x16_bf16 %0, %1, %2, 0" : "=&v"(d) : "v"(a), "v"(b)); }
DI void smfmac(f32x16& d, u32x4 a, u32x4 b, const f32x16& cc) { asm volatile("v_mfma_f32_32x32x16_bf16 %0, %1, %2, %3" : "=&v"(d) : "v"(a), "v"(b), "v"(cc)); }
DI void smfma1(f32x16& d, u32x2 a, u32x2 b) { asm volatile("v_mfma_f32_32x32x8_bf16 %0, %1, %2, %0" : "+v"(d) : "v"(a), "v"(b)); }
DI void omfma(f32x16& d, u32x4 a, u32x4 b) { asm volatile("v_mfma_f32_32x32x16_bf16 %0, %1, %2, %0" : "+v"(d) : "v"(a), "v"(b)); }
#define MX3(a, b, c) __builtin_fmaxf(__builtin_fmaxf((a), (b)), (c))
template <int HALF, class HL, class HA> DI void attn(const Ctx& c, VM& vm, AIn& in, HL hl, HA ha) {
  const u32 bid = sop(c.bid), h = a_head(bid), odd = h & 1, l = vop(c.l16), r16 = l & 0x1F0; const bool hh = l >= 512;
  const int q0 = (HALF * 8 + (bid >> 5)) * 128 + 32 * c.w;
  const u32 img = c.lb + (HALF ? L_HY : L_HX) + c.w * TP;
  const u32 kp0 = vop(img + l), kp1 = vop(img + 1024 + odd * 8 + r16), mp = vop(c.lb + L_MASK + l);
  const u32 lane = l >> 4, pc4 = lane & 3, db = (lane >> 4) & 1, kq = ((lane >> 3) & 4) + ((lane >> 2) & 3);
  const u32 vpl = db ? (pc4 ? 3 : 2) : pc4 >> 1;
  const u32 vlo = vop(img + 1536 + vpl * 512 + (db ? odd : pc4 & 1) * 8 + ((kq ^ (4 * vpl)) << 4)), vhi = vop(vlo ^ 128);
  const int t0 = (256 - q0) >> 5;
  auto vfrag = [&](int t, int s) __attribute__((always_inline)) { const u32x2 lo = trread(vlo + t * TP + s * 256), hi = trread(vhi + t * TP + s * 256); return u32x4{lo.x, lo.y, hi.x, hi.y}; };
  u32x4 sel[4];
#pragma unroll
  for (int j = 0; j < 4; ++j) sel[j] = *LP(const u32x4, mp + j * KB);
  const u32x4 mkA = *LP(const u32x4, mp + 4 * KB), mkB = *LP(const u32x4, mp + 5 * KB);
  prof<true>(vm);
  need(vm, in.mark); BARL(); pin(in.Q0); pin(in.qc);
  prof<true>(vm);
  u32x4 K0[9]; u32x2 K1[9];
  auto kread = [&](int t) __attribute__((always_inline)) { K0[t] = *LP(const u32x4, kp0 + t * TP); K1[t] = *LP(const u32x2, kp1 + t * TP); };
  auto kfix = [&](int t) __attribute__((always_inline)) { if (HALF == 0) { K1[t].x = hh ? (t < t0 ? 0xF149u : 0u) : K1[t].x; pin(K1[t]); } };
  kread(0); kread(8);
  u32x4 Q0 = in.Q0; u32x2 Q1 = hh ? u32x2{HALF == 0 ? 0x3F80u : 0u, 0} : in.qc;
  pin(Q1);
  SB(); kread(1); kread(2); SB();
  f32x16 S[2], M;
  kfix(0); kfix(8);
  SB(); asm volatile("s_nop 0"); smfma0(M, K0[0], Q0);
  SB(); kread(3); kread(4); SB();
  smfma1(M, K1[0], Q1);
  SB(); hl(IC<0>{}); kread(5); SB();
  smfma0(S[0], K0[8], Q0);
  SB(); kread(6); kread(7); SB();
  smfma1(S[0], K1[8], Q1);
  SB(); hl(IC<1>{});
  const u32x4 V0a = vfrag(0, 0), V8a = vfrag(8, 0), V0b = vfrag(0, 1), V8b = vfrag(8, 1);
  SB(); hl(IC<2>{});
#pragma unroll
  for (int t = 1; t < 8; ++t) kfix(t);
  asm volatile("s_nop 1" : "+v"(M), "+v"(S[0]));
  prof<true>(vm);
  SB();
#pragma unroll
  for (int g = 0; g < 16; ++g) M[g] = __uint_as_float((sel[g >> 2][g & 3] & __float_as_uint(S[0][g])) | (~sel[g >> 2][g & 3] & __float_as_uint(M[g])));
  float m = mx3(mx3(mx3(M[0], M[1], M[2]), mx3(M[3], M[4], M[5]), mx3(M[6], M[7], M[8])), mx3(mx3(M[9], M[10], M[11]), mx3(M[12], M[13], M[14]), M[15]), M[15]);
  { auto t = __builtin_amdgcn_permlane32_swap(__float_as_uint(m), __float_as_uint(m), false, false); m = mx3(__uint_as_float(t[0]), __uint_as_float(t[1]), __uint_as_float(t[1])); }
  f32x16 negm, O;
#pragma unroll
  for (int g = 0; g < 16; ++g) negm[g] = -m;
  pin(negm);
  prof<true>(vm);
  SB(); ha(IC<0>{}); hl(IC<3>{});
  prof<true>(vm);
  SB();
#define EM(i) __builtin_amdgcn_exp2f(M[i] - m)
  smfmac(S[1], K0[1], Q0, negm);
  u32x4 P; P.x = vop(pk2(EM(0), EM(1))); P.y = vop(pk2(EM(2), EM(3)));
  SB(); smfma1(S[1], K1[1], Q1);
  P.z = vop(pk2(EM(4), EM(5))); P.w = vop(pk2(EM(6), EM(7)));
  u32x4 A8 = P & mkA, A0 = P & ~mkA; pin(A8); pin(A0);
  P.x = vop(pk2(EM(8), EM(9)));
  SB(); hl(IC<4>{}); SB(); smfma0(O, V0a, A0);
  P.y = vop(pk2(EM(10), EM(11))); P.z = vop(pk2(EM(12), EM(13)));
  SB(); hl(IC<5>{}); ha(IC<9>{}); SB(); omfma(O, V8a, A8);
  P.w = vop(pk2(EM(14), EM(15)));
  u32x4 Pp = P & mkB, B0 = P & ~mkB, Vp = V8b; pin(Pp); pin(B0);
  SB(); hl(IC<6>{}); SB();
#define EX(i) __builtin_amdgcn_exp2f(S[t & 1][i])
  sfor<1, 8>(LAMBDA(T) {
    constexpr int t = T.v;
    prof<true>(vm);
    const u32x4 Va = vfrag(t, 0), Vb = vfrag(t, 1);
    u32x4 Pa, Pb;
    SB();
    if constexpr (t == 1) omfma(O, V0b, B0); else omfma(O, Vp, Pp);
    Pa.x = vop(pk2(EX(0), EX(1))); Pa.y = vop(pk2(EX(2), EX(3)));
    SB(); hl(IC<4 + 3 * t>{}); SB();
    if constexpr (t < 7) smfmac(S[(t + 1) & 1], K0[t + 1], Q0, negm);
    Pa.z = vop(pk2(EX(4), EX(5))); Pa.w = vop(pk2(EX(6), EX(7)));
    SB(); hl(IC<5 + 3 * t>{}); SB();
    if constexpr (t < 7) smfma1(S[(t + 1) & 1], K1[t + 1], Q1);
    Pb.x = vop(pk2(EX(8), EX(9))); Pb.y = vop(pk2(EX(10), EX(11)));
    SB();
    if constexpr (t == 1) { omfma(O, Vp, Pp); SB(); }
    ha(T);
    SB(); hl(IC<6 + 3 * t>{});
    __builtin_amdgcn_s_waitcnt(0xC07F);
    SB();
    omfma(O, Va, Pa);
    Pb.z = vop(pk2(EX(12), EX(13))); Pb.w = vop(pk2(EX(14), EX(15)));
    Pp = Pb; Vp = Vb;
    SB();
  });
  const u32 vo = A_OF + ((q0 >> 4) * 64 + 2 * h) * 256 + ((l >> 8) & 1) * (64 * 256) + (hh ? 256 : 0) + (l & 0xF0);
  const u32 vt = hh ? OOR : A_OO + ((q0 >> 4) * 32 + h) * 128 + ((l >> 8) & 1) * (32 * 128) + ((l >> 1) & 0x78);
  asm volatile("s_nop 1" :: "v"(negm));
  omfma(O, Vp, Pp);
  prof<true>(vm);
  ha(IC<8>{});
  asm volatile("s_nop 11" : "+v"(O) : "v"(vo), "v"(vt));
  if (__builtin_expect(!__all(O[12] < 1.0995116e12f), 0)) {
    f32x16 X;
    auto score = [&](int t) __attribute__((always_inline)) {
      u32x4 k0 = *LP(const u32x4, kp0 + t * TP); u32x2 k1 = *LP(const u32x2, kp1 + t * TP);
      if (HALF == 0) k1.x = hh ? (t < t0 ? 0xF149u : 0u) : k1.x;
      asm volatile("s_nop 1\n\tv_mfma_f32_32x32x16_bf16 %0, %1, %2, 0\n\tv_mfma_f32_32x32x8_bf16 %0, %3, %4, %0\n\ts_nop 11" : "=&v"(X) : "v"(k0), "v"(Q0), "v"(k1), "v"(Q1));
      const u32 x0 = t == 0 ? ~0u : 0u, x1 = (t & 7) ? ~0u : 0u;
#pragma unroll
      for (int j = 0; j < 4; ++j) { const u32x4 ok = (*LP(const u32x4, mp + j * KB) ^ x0) | x1;
#pragma unroll
        for (int e = 0; e < 4; ++e) X[4 * j + e] = __uint_as_float((ok[e] & __float_as_uint(X[4 * j + e])) | (~ok[e] & 0xFF800000u)); }
    };
    float mt = -INFINITY;
#pragma unroll 1
    for (int t = 0; t < 9; ++t) { score(t);
#pragma unroll
      for (int g = 0; g < 16; ++g) mt = __builtin_fmaxf(mt, X[g]); }
    { auto t = __builtin_amdgcn_permlane32_swap(__float_as_uint(mt), __float_as_uint(mt), false, false); mt = __builtin_fmaxf(__uint_as_float(t[0]), __uint_as_float(t[1])); }
#pragma unroll
    for (int g = 0; g < 16; ++g) O[g] = 0.f;
#pragma unroll 1
    for (int t = 0; t < 9; ++t) { score(t);
      u32x4 pa, pb;
#pragma unroll
      for (int g = 0; g < 4; ++g) { pa[g] = pk2(__builtin_amdgcn_exp2f(X[2 * g] - mt), __builtin_amdgcn_exp2f(X[2 * g + 1] - mt)); pb[g] = pk2(__builtin_amdgcn_exp2f(X[8 + 2 * g] - mt), __builtin_amdgcn_exp2f(X[9 + 2 * g] - mt)); }
      const u32x4 va = vfrag(t, 0), vb = vfrag(t, 1);
      asm volatile("s_nop 1\n\tv_mfma_f32_32x32x16_bf16 %0, %1, %2, %0\n\tv_mfma_f32_32x32x16_bf16 %0, %3, %4, %0\n\ts_nop 11" : "+v"(O) : "v"(va), "v"(pa), "v"(vb), "v"(pb)); }
  }
  const float inv = __builtin_amdgcn_rcpf(O[12]);
  st16c(c, vm, vo, u32x4{pk2(O[0] * inv, O[1] * inv), pk2(O[2] * inv, O[3] * inv), pk2(O[4] * inv, O[5] * inv), pk2(O[6] * inv, O[7] * inv)});
  st8c(c, vm, vt, u32x2{pk2(O[8] * inv, O[9] * inv), pk2(O[10] * inv, O[11] * inv)});
}

constexpr int CARRY0 = 8, CARRY1 = 4, CARRY2 = 0, CARRYST = 4;
#define G decltype(Gp)::v
#define QSYNC 0x0B
#define QPG 1
#define QP1 8
#define QP2 11
#define QC 14
#define AP1 1
#define AP2 3
#define AC 4
#define DPUB 4
#define DP1 7
#define DP2 10

__global__ void __launch_bounds__(256, 1) mega_kernel(Params p) {
  extern __shared__ __attribute__((aligned(16))) unsigned char smem[];
  Ctx c;
  c.ws = p.ws; c.rs = __builtin_amdgcn_make_buffer_rsrc(p.ws, 0, (int)OFF_ACT, 0x00020000);
  c.ra = __builtin_amdgcn_make_buffer_rsrc(p.ws + OFF_ACT, 0, (int)(2 * LB), 0x00020000);
  c.lb = (u32)(size_t)(LAS unsigned char*)smem;
  c.l16 = (threadIdx.x & 63) * 16; c.w = __builtin_amdgcn_readfirstlane(threadIdx.x >> 6); c.bid = blockIdx.x;
  const u32 x = c.bid & 7, i = c.bid >> 3;
  c.sq = 64 * ((x >> 1) * 4 + (i >> 3)); c.cg = (x & 1) * 8 + (i & 7); c.sw = 128 * x + 32 * (i >> 3);
  const u32 cgq = c.cg < 15 ? c.cg : 14;
  c.tq = (c.sq >> 4) + c.w;
  c.bof = A_OF + ((c.sw >> 4) * 64 + 16 * c.w) * 256; c.boo = A_OO + ((c.sw >> 4) * 32 + 8 * c.w) * 128; c.bhf = A_HF + ((c.sw >> 4) * 192 + 48 * c.w) * 256;
  c.m0x = c.lb + c.w * 4 * KB; c.m0a = c.lb + 3 * c.w * TP;
  { const int l = 0; (void)l; STAMP(0); }
  asm volatile("" ::: "a0", "a255");
  BF<0, 40> Bq; BF<1, 25> Bw; BF<2, 60> Bg; BF<3, 60> Bd;
  Bq.base = OFF_BQ + (cgq * 4 + c.w) * 40 * KB; Bw.base = OFF_BW + ((i & 7) * 4 + c.w) * 25 * KB;
  Bg.base = OFF_BG + (c.cg * 4 + c.w) * 60 * KB; Bd.base = OFF_BD + ((i & 7) * 4 + c.w) * 60 * KB;
  XR xr[2];
  Stage sx, sy; Poll pl; AIn ain; VM vm = {}; int mst = 0;
  stages<0, 0, 9>(c, vm, sx, A_XA, c.tq);
  loadB<0, 24>(c, vm, Bq);
  {
    const u32 t = threadIdx.x, lane = t & 63, cc = lane & 7;
#pragma unroll
    for (int hf = 0; hf < 2; ++hf) {
      const GAS u16* src = (const GAS u16*)p.x + (c.sw + hf * 1024 + 8 * c.w + (lane >> 3)) * D + 80 * (i & 7);
      xr[hf].m = *(const GAS u32x4*)(src + 8 * cc); xr[hf].t = *(const GAS u32*)(src + 64 + 2 * cc);
    }
    if (t < 80) *LP(u32x4, c.lb + L_CS + t * 16) = *(const GAS u32x4*)(p.ws + (t < 32 ? OFF_CSQ + cgq * 512 + t * 16 : OFF_CSG + c.cg * 768 + (t - 32) * 16));
    const int z = (int)(lane & 31) - 4 * (int)(lane >> 5) - 8 * (int)c.w;
    *LP(u32x4, c.lb + L_MASK + c.w * KB + c.l16) = u32x4{z >= 0 ? ~0u : 0u, z >= 1 ? ~0u : 0u, z >= 2 ? ~0u : 0u, z >= 3 ? ~0u : 0u};
    *LP(u32x2, c.lb + L_MASK + (4 + (c.w >> 1)) * KB + c.l16 + (c.w & 1) * 8) = u32x2{(z >= 0 ? 0xFFFFu : 0u) | (z >= 1 ? 0xFFFF0000u : 0u), (z >= 2 ? 0xFFFFu : 0u) | (z >= 3 ? 0xFFFF0000u : 0u)};
  }
  {
    const u32 lane = threadIdx.x & 63, cc = lane & 7, t16 = (8 * c.w + (lane >> 3)) & 15, g = (t16 >> 1) & 3, cg = i & 7, h = 8 * c.w + cc;
    c.dxx = ((c.tq >> 3) + 8 * (((c.tq >> 1) & 3) * 8 + cc)) << 7;
    c.doh = (((h >> 2) + 8 * ((h & 3) + 4 * x)) << 7) | (wg_qg(c.sw >> 6, 4 * c.w + (lane & 3)) << 23);
    c.foff = threadIdx.x == 0 ? OFF_FLAGS + c.bid * 128 : OOR;
    c.daa = a_dep(c, 0) | (a_dep(c, 1) << 16);
    c.rw = c.lb + L_HX + c.w * 10 * KB + (lane & 15) * 64 + (((lane >> 4) ^ (lane >> 1)) & 3) * 16;
    const u32 base = c.lb + L_HX + (c.w >> 1) * 5 * KB + t16 * 64, row = c.sw + 8 * c.w + (lane >> 3);
    c.rpa = base + (cc >> 1) * KB + (((2 * (cc & 1)) ^ g ^ ((cc >> 1) & 1)) << 4);
    c.rpt = base + 4 * KB + (((cc >> 1) ^ g) << 4) + (cc & 1) * 8;
    c.rvo = ((row >> 4) * 80 + 10 * cg + cc) * 256 + t16 * 16;
    c.rvt = ((row >> 4) * 80 + 10 * cg + 8 + (cc >> 2)) * 256 + t16 * 16 + (cc & 3) * 4;
    c.rvs = cc == 0 ? (A_SA - A_XA) + row * 64 + cg * 8 : OOR;
  }
  asm volatile("s_waitcnt vmcnt(0)" : "+v"(xr[0].m), "+v"(xr[0].t), "+v"(xr[1].m), "+v"(xr[1].t) :: "memory");
  stages<0, 9, 13>(c, vm, sx, 0, 0);
#pragma unroll 1
  for (int l = 0; l < 2; ++l) {
    vm.n = 0; vm.done = -1000; sx.mg[0] = -CARRY0; sx.mg[1] = -CARRY1; sx.mg[2] = -CARRY2; mst = -CARRYST;
    sfor<0, 24>(LAMBDA(J) { Bq.m[J.v] = -1000; });
    asm volatile("; MARK QX");
    { f32x4 acc[2][4]; EpQ e;
      TS(0, 0);
      gemm_lds<0, 2, QSYNC, QPG>(c, vm, sx, l != 0, mst, 10 * l, acc, Bq, LAMBDA(Gp) {
        at<G, 0, 1, 8>(LAMBDA(K) { stage<0, 13 + K.v>(c, vm, sx, 0, 0); });
        at<G, 8, 1, 16>(LAMBDA(K) { ldB<24 + K.v>(c, vm, Bq); });
        at<G, 24, 1, 2 * QC - 23>(LAMBDA(K) { ldB<K.v>(c, vm, Bw); });
        at<G, 2 * QC + 1, 1, 13>(LAMBDA(K) { stage<1, K.v>(c, vm, sy, A_XA, c.tq); });
      }, LAMBDA(S) { constexpr int s = S.v;
        if (s == QP1) poll_issue(c, vm, pl, c.dxx, l ? 10 : 0);
        if (s == QP2) poll_again(c, vm, pl);
        if (s == QC) { TS(0, 1); poll_check<true>(c, vm, pl); TS(0, 2); }
        if (s == 17) pre_q<0>(c, e);
      });
      TS(0, 3);
      epi_q(c, vm, e, acc); mst = vm.n; stages<1, 39 - 2 * QC, 13>(c, vm, sy, 0, 0); poll_retire(vm, pl); TS(0, 4); }
    asm volatile("; MARK QY");
    { f32x4 acc[2][4]; EpQ e;
      TS(1, 0);
      gemm_lds<1, 2, QSYNC, QPG>(c, vm, sy, l != 0, mst, 10 * l + 1, acc, Bq, LAMBDA(Gp) {
        at<G, 0, 1, 8>(LAMBDA(K) { stage<1, 13 + K.v>(c, vm, sy, 0, 0); });
        at<G, 8, 1, 48 - 2 * QC>(LAMBDA(K) { ldB<2 * QC - 23 + K.v>(c, vm, Bw); });
        at<G, 2 * QC + 1, 1, 11>(LAMBDA(K) { a_stage<0, K.v>(c, vm, ain); });
      }, LAMBDA(S) { constexpr int s = S.v;
        if (s == QP1) a_poll(c, vm, pl, l, lo16(c.daa));
        if (s == QP2) poll_again(c, vm, pl);
        if (s == QC) { TS(1, 1); poll_check<true>(c, vm, pl); TS(1, 2); }
        if (s == 17) pre_q<1>(c, e);
      });
      TS(1, 3);
      sfor<39 - 2 * QC, 11>(LAMBDA(K) { a_stage<0, K.v>(c, vm, ain); });
      epi_q(c, vm, e, acc); mst = vm.n; poll_retire(vm, pl); TS(1, 4); }
    asm volatile("; MARK AX");
    AIn ain2;
    TS(2, 0);
    attn<0>(c, vm, ain, LAMBDA(Gp) {
      at<G, 0, 1, 6 + 3 * AC>(LAMBDA(K) { ldB<K.v>(c, vm, Bg); });
      at<G, 6 + 3 * AC, 1, 11>(LAMBDA(K) { a_stage<1, K.v>(c, vm, ain2); });
    }, LAMBDA(S) { constexpr int s = S.v;
      if (s == 0) publish(c, vm, mst, 10 * l + 2);
      if (s == AP1) a_poll(c, vm, pl, l, hi16(c.daa));
      if (s == AP2) poll_again(c, vm, pl);
      if (s == AC) { TS(2, 1); poll_check<true>(c, vm, pl); TS(2, 2); }
      if (s == 8) { sfor<22 - 3 * AC, 11>(LAMBDA(K) { a_stage<1, K.v>(c, vm, ain2); }); poll_retire(vm, pl); TS(2, 3); }
    });
    mst = vm.n; TS(2, 4);
    asm volatile("; MARK AY");
    AW aw;
    TS(3, 0);
    attn<1>(c, vm, ain2, LAMBDA(Gp) {
      at<G, 0, 1, 6 + 3 * AC>(LAMBDA(K) { if constexpr (6 + 3 * AC + K.v < 39) ldB<6 + 3 * AC + K.v>(c, vm, Bg); });
      at<G, 6 + 3 * AC, 1, 12>(LAMBDA(K) { ld_w<0, K.v>(c, vm, aw); });
    }, LAMBDA(S) { constexpr int s = S.v;
      if (s == 0) publish(c, vm, mst, 10 * l + 3);
      if (s == AP1) poll_issue(c, vm, pl, lo16(c.doh), 10 * l + 3);
      if (s == AP2) poll_again(c, vm, pl);
      if (s == AC) { TS(3, 1); poll_check<true>(c, vm, pl); TS(3, 2); }
      if (s == 8) { sfor<22 - 3 * AC, 12>(LAMBDA(K) { ld_w<0, K.v>(c, vm, aw); }); poll_retire(vm, pl); TS(3, 3); }
    });
    mst = vm.n; TS(3, 4);
    asm volatile("; MARK WX");
    constexpr int BG0 = 12 + 6 * AC < 39 ? 12 + 6 * AC : 39;
    { Red r;
      TS(4, 0);
      gemm_reg<0, 5>(c, vm, Bw, aw, LAMBDA(Gp) { at<G, 0, 1, 12>(LAMBDA(K) { ldB<BG0 + K.v>(c, vm, Bg); }); }, LAMBDA(S) { if (S.v == 1) red_pre<0>(c, r, xr[0], A_XB); if (S.v == 4) publish(c, vm, mst, 10 * l + 4); });
      TS(4, 1);
      BARL();
      poll_issue(c, vm, pl, lo16(c.doh), 10 * l + 4);
      TS(4, 2);
      red_out<0>(c, vm, r, xr[0], nullptr); mst = vm.n;
      TS(4, 3);
      poll_again(c, vm, pl);
      TS(4, 4); }
    asm volatile("; MARK WY");
    { Red r;
      poll_check<true>(c, vm, pl);
      TS(5, 0);
      sfor<0, 12>(LAMBDA(K) { ld_w<1, K.v>(c, vm, aw); });
      loadB<BG0 + 12, BG0 + 16>(c, vm, Bg);
      red_pre<1>(c, r, xr[1], A_XB);
      poll_retire(vm, pl);
      publish(c, vm, mst, 10 * l + 5);
      gemm_reg<1, 5>(c, vm, Bw, aw, LAMBDA(Gp) {
        at<G, 0, 1, 44 - BG0>(LAMBDA(K) { ldB<BG0 + 16 + K.v>(c, vm, Bg); });
      }, LAMBDA(S) { constexpr int s = S.v; if (s == 1) poll_issue(c, vm, pl, c.dxx, 10 * l + 5); if (s == 4) poll_again(c, vm, pl); });
      TS(5, 1); poll_check<true>(c, vm, pl); TS(5, 2);
      stages<0, 0, 9>(c, vm, sx, A_XB, c.tq);
      BARL();
      TS(5, 3);
      red_out<1>(c, vm, r, xr[1], nullptr); mst = vm.n;
      stages<0, 9, 13>(c, vm, sx, 0, 0);
      poll_retire(vm, pl);
      TS(5, 4); }
    asm volatile("; MARK GX");
    { f32x4 acc[3][4]; EpG e;
      TS(6, 0);
      gemm_lds<0, 3, 0x0B, 1>(c, vm, sx, true, mst, 10 * l + 6, acc, Bg, LAMBDA(Gp) {
        at<G, 0, 1, 8>(LAMBDA(K) { stage<0, 13 + K.v>(c, vm, sx, 0, 0); });
        at<G, 8, 1, 25>(LAMBDA(K) { ldB<K.v>(c, vm, Bd); });
        at<G, 37, 1, 21>(LAMBDA(K) { stage<1, K.v>(c, vm, sy, A_XB, c.tq); });
      }, LAMBDA(S) { constexpr int s = S.v;
        if (s == 7) poll_issue(c, vm, pl, c.dxx, 10 * l + 6);
        if (s == 10) poll_again(c, vm, pl);
        if (s == 12) { TS(6, 1); poll_check<true>(c, vm, pl); TS(6, 2); }
        if (s == 16) poll_retire(vm, pl);
        if (s == 18) pre_g<0>(c, e);
      });
      TS(6, 3);
      epi_g(c, vm, e, acc); mst = vm.n; TS(6, 4); }
    asm volatile("; MARK GY");
    AD<true> ad;
    { f32x4 acc[3][4]; EpG e;
      TS(7, 0);
      gemm_lds<1, 3, 0x13, 1>(c, vm, sy, true, mst, 10 * l + 7, acc, Bg, LAMBDA(Gp) {
        at<G, 3, 1, 31>(LAMBDA(K) { ldB<25 + K.v>(c, vm, Bd); });
        at<G, 37, 1, 23>(LAMBDA(K) { ld_d<0, K.v>(c, vm, ad); });
      }, LAMBDA(S) { constexpr int s = S.v;
        if (s == 7) poll_issue(c, vm, pl, hi16(c.doh), 10 * l + 7);
        if (s == 10) poll_again(c, vm, pl);
        if (s == 12) { TS(7, 1); poll_check<true>(c, vm, pl); TS(7, 2); }
        if (s == 16) poll_retire(vm, pl);
        if (s == 18) pre_g<1>(c, e);
      });
      TS(7, 3);
      epi_g(c, vm, e, acc); mst = vm.n; loadB<56, 60>(c, vm, Bd); TS(7, 4); }
    asm volatile("; MARK DX");
    if (l) Bq.base = OOR;
    AD<false> ad2;
    { Red r;
      TS(8, 0);
      gemm_reg<0, 12>(c, vm, Bd, ad, LAMBDA(Gp) {
        at<G, 0, 1, 1>(LAMBDA(K) { ld_d<0, 23>(c, vm, ad); });
        at<G, 1, 1, 10>(LAMBDA(K) { ldB<K.v>(c, vm, Bq); });
      }, LAMBDA(S) { constexpr int s = S.v;
        if (s == DPUB) publish(c, vm, mst, 10 * l + 8);
        if (s == DP1) poll_issue(c, vm, pl, hi16(c.doh), 10 * l + 8);
        if (s == 9) red_pre<0>(c, r, xr[0], LB + A_XA);
        if (s == DP2) poll_again(c, vm, pl);
      });
      TS(8, 1);
      poll_check<true>(c, vm, pl);
      TS(8, 2);
      sfor<0, 6>(LAMBDA(K) { ld_d<1, K.v>(c, vm, ad2); });
      BARL();
      TS(8, 3);
      red_out<0>(c, vm, r, xr[0], l ? p.out : nullptr); mst = vm.n;
      sfor<6, 12>(LAMBDA(K) { ld_d<1, K.v>(c, vm, ad2); });
      poll_retire(vm, pl);
      TS(8, 4); }
    asm volatile("; MARK DY");
    { Red r;
      TS(9, 0);
      gemm_reg<1, 12>(c, vm, Bd, ad2, LAMBDA(Gp) {
        at<G, 0, 1, 12>(LAMBDA(K) { ld_d<1, 12 + K.v>(c, vm, ad2); });
        at<G, 17, 1, 11>(LAMBDA(K) { ldB<10 + K.v>(c, vm, Bq); });
      }, LAMBDA(S) { constexpr int s = S.v;
        if (s == DPUB) publish(c, vm, mst, 10 * l + 9);
        if (s == DP1) poll_issue(c, vm, pl, c.dxx, l ? 0 : 9);
        if (s == 9) red_pre<1>(c, r, xr[1], LB + A_XA);
        if (s == 11) { loadB<21, 24>(c, vm, Bq); poll_again(c, vm, pl); }
      });
      TS(9, 1);
      poll_check<true>(c, vm, pl);
      TS(9, 2);
      stages<0, 0, 9>(c, vm, sx, LB + A_XA, c.tq);
      BARL();
      TS(9, 3);
      red_out<1>(c, vm, r, xr[1], l ? p.out : nullptr); mst = vm.n;
      stages<0, 9, 13>(c, vm, sx, 0, 0);
      poll_retire(vm, pl);
      TS(9, 4); }
    if (vm.n - sx.mg[0] < CARRY0 || vm.n - sx.mg[1] < CARRY1 || vm.n - sx.mg[2] < CARRY2 || vm.n - mst < CARRYST || vm.done < Bq.m[23]) carry_count_too_large();
    c.ra = __builtin_amdgcn_make_buffer_rsrc(p.ws + OFF_ACT + LB, 0, (int)LB, 0x00020000);
    sx.src -= LB;
    prof<true>(vm); prof<true>(vm);
    asm volatile("; MARK END");
  }
  asm volatile("s_waitcnt vmcnt(0)" ::: "memory");
}

extern "C" void kernel_launch(void* const* d_in, const int* in_sizes, int n_in, void* d_out, int out_size, void* d_ws, size_t ws_size, hipStream_t stream) {
  Params p;
  p.x = (const u16*)d_in[0]; p.wqkv = (const u16*)d_in[1]; p.wo = (const u16*)d_in[2];
  p.wg = (const u16*)d_in[4]; p.wu = (const u16*)d_in[5]; p.wd = (const u16*)d_in[6];
  p.out = (u16*)d_out; p.ws = (unsigned char*)d_ws;
  (void)hipFuncSetAttribute((const void*)mega_kernel, hipFuncAttributeMaxDynamicSharedMemorySize, LDS_BYTES);
  prep_kernel<<<PREP_BLOCKS, 256, 0, stream>>>(p);
  mega_kernel<<<256, 256, LDS_BYTES, stream>>>(p);
}
```

```cpp
#include <hip/hip_runtime.h>
#include <stdint.h>

typedef unsigned short u16;
typedef unsigned u32;
typedef u32 u32x2 __attribute__((ext_vector_type(2)));
typedef u32 u32x4 __attribute__((ext_vector_type(4)));
typedef float f32x2 __attribute__((ext_vector_type(2)));
typedef float f32x4 __attribute__((ext_vector_type(4)));
typedef float f32x16 __attribute__((ext_vector_type(16)));
typedef short bf16x8 __attribute__((ext_vector_type(8)));
typedef short s16x4 __attribute__((ext_vector_type(4)));
typedef __bf16 bf16x2_t __attribute__((ext_vector_type(2)));
#define GAS __attribute__((address_space(1)))
#define LAS __attribute__((address_space(3)))
#define DI __device__ __forceinline__
#define RLX_AGENT __ATOMIC_RELAXED, __HIP_MEMORY_SCOPE_AGENT

constexpr int S = 2048, D = 640, F = 1536;
constexpr u32 KB = 1024;
constexpr u32 OFF_FLAGS = 0;
constexpr u32 OFF_TMO = 32 * KB;
constexpr u32 OFF_EPOCH = 33 * KB;
constexpr u32 OFF_CS = 64 * KB;
constexpr u32 CS_ROW = 4992 * 4;
constexpr u32 OFF_BQ = 512 * KB;
constexpr u32 OFF_BW = OFF_BQ + 2400 * KB;
constexpr u32 OFF_BG = OFF_BW + 800 * KB;
constexpr u32 OFF_BD = OFF_BG + 3840 * KB;
constexpr u32 A_XA = 0, A_SA = 2560 * KB, A_QKV = A_SA + 128 * KB, A_OF = A_QKV + 7680 * KB, A_XB = A_OF + 2560 * KB,
              A_SB = A_XB + 2560 * KB, A_HF = A_SB + 128 * KB, LB = A_HF + 6144 * KB;
constexpr u32 OFF_ACT = OFF_BD + 1920 * KB;
constexpr u32 OFF_DBG = OFF_ACT + 2 * LB;
constexpr u32 OOR = 0x80000000u;
constexpr u32 L_Y = 80 * KB, L_HX = 16 * KB, L_HY = 80 * KB, L_MISC = 144 * KB;
constexpr u32 TP = 3584;
constexpr u32 L_STAT = L_MISC;
constexpr u32 L_CS = L_STAT + 1024;
constexpr u32 L_MASK = L_CS + 1280;
constexpr u32 L_DBG = L_MASK + 6 * KB;
constexpr u32 LDS_BYTES = L_DBG + 1024;
static_assert(LDS_BYTES <= 160 * KB, "");

struct Params { const u16* x; const u16* wqkv; const u16* wo; const u16* wg; const u16* wu; const u16* wd; u16* out; unsigned char* ws; };

DI float bf_lo(u32 v) { return __uint_as_float(v << 16); }
DI float bf_hi(u32 v) { return __uint_as_float(v & 0xffff0000u); }
DI u32 pk2(float a, float b) { f32x2 f = {a, b}; bf16x2_t h = __builtin_convertvector(f, bf16x2_t); return __builtin_bit_cast(u32, h); }
DI float rbf(float a) { return bf_lo(pk2(a, a)); }

DI u32x2 trread(u32 a) { return __builtin_bit_cast(u32x2, __builtin_amdgcn_ds_read_tr16_b64_v4i16((LAS s16x4*)(size_t)a)); }

typedef __amdgpu_buffer_rsrc_t rsrc_t;
#define SB() __builtin_amdgcn_sched_barrier(0)
template <int I> struct IC { static constexpr int v = I; };
template <int B, int E, class Fn> DI void sfor(Fn&& f) { if constexpr (B < E) { f(IC<B>{}); sfor<B + 1, E>(f); } }
#define LAMBDA(S) [&](auto S) __attribute__((always_inline))
template <class T> DI void pin(T& d) { asm volatile("" : "+v"(d)); }
DI u32 sop(u32 v) { asm volatile("" : "+s"(v)); return v; }
DI u32 vop(u32 v) { asm volatile("" : "+v"(v)); return v; }
struct Ctx {
  rsrc_t rs, ra;
  u32 lb; unsigned char* ws;
  u32 l16, w, bid;
  u32 sq, sw, cg;
  u32 tq;
  u32 bof, bhf;
  u32 m0x, m0a;
  u32 foff;
  u32 dxx, doh, daa;
  u32 rw, rpa, rpt, rvo, rvt, rvs;
};
template <int G, int START, int STRIDE, int N, class Fn> DI void at(Fn&& f) {
  if constexpr (G >= START && (G - START) % STRIDE == 0 && (G - START) / STRIDE < N) f(IC<(G - START) / STRIDE>{});
}
#define LP(T, a) ((LAS T*)(size_t)(a))

struct VM { int n, done;
};
template <bool W, bool W2 = W> DI void prof(VM& vm) {
}
DI void need(VM& vm, int mark) {
  if (mark > vm.done) { const int k = vm.n - mark; if (k < 63) asm volatile("s_waitcnt vmcnt(%0)" :: "i"(k) : "memory"); vm.done = mark; }
}
template <int SLOT, bool SC1, int OFF> DI int ld16(rsrc_t rs, VM& vm, u32x4& d, u32 voff, u32 soff) {
  if constexpr (SLOT >= 0 && SC1) asm volatile("buffer_load_dwordx4 a[%c0:%c1], %2, %3, %4 offen offset:%5 sc1" :: "i"(4 * SLOT), "i"(4 * SLOT + 3), "v"(voff), "s"(rs), "s"(soff), "i"(OFF) : "memory");
  else if constexpr (SLOT >= 0) asm volatile("buffer_load_dwordx4 a[%c0:%c1], %2, %3, %4 offen offset:%5" :: "i"(4 * SLOT), "i"(4 * SLOT + 3), "v"(voff), "s"(rs), "s"(soff), "i"(OFF) : "memory");
  else if constexpr (SC1) asm volatile("buffer_load_dwordx4 %0, %1, %2, %3 offen offset:%4 sc1" : "=v"(d) : "v"(voff), "s"(rs), "s"(soff), "i"(OFF) : "memory");
  else asm volatile("buffer_load_dwordx4 %0, %1, %2, %3 offen offset:%4" : "=v"(d) : "v"(voff), "s"(rs), "s"(soff), "i"(OFF) : "memory");
  return ++vm.n;
}
template <int OFF> DI int ld8c(rsrc_t rs, VM& vm, u32x2& d, u32 voff, u32 soff) {
  asm volatile("buffer_load_dwordx2 %0, %1, %2, %3 offen offset:%4 sc1" : "=v"(d) : "v"(voff), "s"(rs), "s"(soff), "i"(OFF) : "memory");
  return ++vm.n;
}
template <bool FIRST, int SA, int SBB> DI void mma(f32x4& d, const u32x4& a, const u32x4& b) {
#define MMA_(D_, C_) \
  if constexpr (SA >= 0 && SBB >= 0) asm volatile("v_mfma_f32_16x16x32_bf16 %0, a[%c1:%c2], a[%c3:%c4], " C_ : D_(d) : "i"(4 * SA), "i"(4 * SA + 3), "i"(4 * SBB), "i"(4 * SBB + 3)); \
  else if constexpr (SA >= 0) asm volatile("v_mfma_f32_16x16x32_bf16 %0, a[%c1:%c2], %3, " C_ : D_(d) : "i"(4 * SA), "i"(4 * SA + 3), "v"(b)); \
  else if constexpr (SBB >= 0) asm volatile("v_mfma_f32_16x16x32_bf16 %0, %3, a[%c1:%c2], " C_ : D_(d) : "i"(4 * SBB), "i"(4 * SBB + 3), "v"(a)); \
  else asm volatile("v_mfma_f32_16x16x32_bf16 %0, %1, %2, " C_ : D_(d) : "v"(a), "v"(b));
  if constexpr (FIRST) { MMA_("=&v", "0") } else { MMA_("+v", "%0") }
#undef MMA_
}
DI void st16c(const Ctx& c, VM& vm, u32 voff, u32x4 v) { ++vm.n; __builtin_amdgcn_raw_buffer_store_b128(v, c.ra, voff, 0, 16); }
DI void st8c(const Ctx& c, VM& vm, u32 voff, u32x2 v) { ++vm.n; __builtin_amdgcn_raw_buffer_store_b64(v, c.ra, voff, 0, 16); }
DI void st4c(const Ctx& c, VM& vm, u32 voff, u32 v) { ++vm.n; __builtin_amdgcn_raw_buffer_store_b32(v, c.ra, voff, 0, 16); }
template <u32 LOFF, u32 SOFF> DI void glds_head(const Ctx& c, VM& vm, u32& cur, u32 m0b, u32 sb, u32 voff) {
  ++vm.n;
  asm volatile("s_add_u32 m0, %1, %2\n\ts_add_u32 %0, %3, %4\n\tbuffer_load_dwordx4 %5, %6, %0 offen sc1 lds"
               : "=&s"(cur) : "s"(m0b), "i"(LOFF), "s"(sb), "i"(SOFF), "v"(voff), "s"(c.ra) : "memory", "scc");
}
template <int OFF> DI void glds_next(const Ctx& c, VM& vm, u32 cur, u32 voff) {
  ++vm.n;
  asm volatile("buffer_load_dwordx4 %0, %1, %2 offen offset:%3 sc1 lds" :: "v"(voff), "s"(c.ra), "s"(cur), "i"(OFF) : "memory");
}
#define BARL() asm volatile("s_waitcnt lgkmcnt(0)\n\ts_barrier" ::: "memory")
#define BAR0() asm volatile("s_barrier" ::: "memory")
extern "C" __device__ void carry_count_too_large() __attribute__((error("carry count too large")));

#define STAMP(k) do {} while (0)
#define TS(t, k) STAMP(1 + ((10 * l + (t)) * 5 + (k)))

struct Poll { u32 off, need; int mark, mark2; };
DI void poll_issue(const Ctx& c, VM& vm, Poll& p, u32 off, u32 need) {
  p.off = off; p.need = need;
  asm volatile("buffer_load_dword a252, %0, %1, 0 offen sc1" :: "v"(p.off), "s"(c.rs) : "memory");
  p.mark = ++vm.n;
}
DI void poll_again(const Ctx& c, VM& vm, Poll& p) {
  asm volatile("buffer_load_dword a253, %0, %1, 0 offen sc1" :: "v"(p.off), "s"(c.rs) : "memory");
  p.mark2 = ++vm.n;
}
template <bool TWO> DI void poll_check(const Ctx& c, VM& vm, Poll& p) {
  need(vm, p.mark);
  u32 v; asm volatile("v_accvgpr_read_b32 %0, a252" : "=v"(v));
  if (__builtin_expect(!__all(v - p.need < 32u), 0)) {
    asm volatile("buffer_load_dword a254, %1, %2, 0 offen sc1\n\ts_waitcnt vmcnt(%3) ; SECOND\n\tv_accvgpr_read_b32 %0, a253" : "=v"(v) : "v"(p.off), "s"(c.rs), "i"(vm.n - p.mark2 + 1) : "memory");
    bool ok = __all(v - p.need < 32u);
#pragma unroll 1
    for (u32 spins = 0; !ok; ++spins) {
      asm volatile("buffer_load_dword a255, %1, %2, 0 offen sc1\n\ts_waitcnt vmcnt(1)\n\tv_accvgpr_read_b32 %0, a254" : "=v"(v) : "v"(p.off), "s"(c.rs) : "memory");
      if (__all(v - p.need < 32u)) break;
      asm volatile("buffer_load_dword a254, %1, %2, 0 offen sc1\n\ts_waitcnt vmcnt(1)\n\tv_accvgpr_read_b32 %0, a255" : "=v"(v) : "v"(p.off), "s"(c.rs) : "memory");
      ok = __all(v - p.need < 32u);
      if (spins > (1u << 16)) { __hip_atomic_store((GAS u32*)(c.ws + OFF_TMO), 1u, RLX_AGENT); break; }
    }
  }
  asm volatile("" ::: "memory");
}
DI void flag_store(const Ctx& c, VM& vm, u32 val) { ++vm.n; __builtin_amdgcn_raw_buffer_store_b32(val, c.rs, c.foff, 0, 16); }
DI void publish(const Ctx& c, VM& vm, int mark, u32 val) { need(vm, mark); BAR0(); flag_store(c, vm, val); }
DI u32 lo16(u32 v) { return vop(v) & 0xFFFF; }
DI u32 hi16(u32 v) { return vop(v) >> 16; }

template <int ID> constexpr int slotB(int j) { return ID == 0 ? (j < 10 ? -1 : 15 + j) : ID == 1 ? j : ID == 2 ? (j < 38 ? 25 + j : -1) : j; }
template <int ID_, int N> struct BF { static constexpr int ID = ID_; u32x4 f[N]; int m[N]; u32 base, cur; };
template <int J, int ID, int N> DI void ldB(const Ctx& c, VM& vm, BF<ID, N>& B) {
  if constexpr ((J & 3) == 0) B.cur = sop(B.base) + J * KB;
  B.m[J] = ld16<slotB<ID>(J), true, (J & 3) * 1024>(c.rs, vm, B.f[J], c.l16, B.cur);
}
template <int LO, int HI, class BT> DI void loadB(const Ctx& c, VM& vm, BT& B) { sfor<LO, HI>(LAMBDA(J) { ldB<J.v>(c, vm, B); }); }
template <int CTRL> DI float dpp_add(float v) { return v + __int_as_float(__builtin_amdgcn_update_dpp(0, __float_as_int(v), CTRL, 0xF, 0xF, true)); }

#define STAT0 4
template <int HALF> constexpr u32 img_off(int g) { return HALF == 0 ? g * 16 * KB : (g < 4 ? L_Y + g * 16 * KB : 0); }
struct Stage { u32x4 sv; u32 src, cur; int mg[5]; };
template <int HALF, int K> DI void stage(const Ctx& c, VM& vm, Stage& st, u32 xoff, u32 tf) {
  if constexpr (K == 0) {
    const u32 t = sop(tf), l = vop(c.l16);
    st.src = xoff + HALF * 64 * 20480 + t * 20480;
    ld16<-1, true, 0>(c.ra, vm, st.sv, ((l << 2) & 0x3C0) + ((l >> 4) & 0x30), xoff + (A_SA - A_XA) + HALF * 65536 + t * 1024);
  } else {
    constexpr int g = (K - 1) >> 2, j = (K - 1) & 3;
    if constexpr (j == 0) glds_head<img_off<HALF>(g), g * 4 * KB>(c, vm, st.cur, c.m0x, st.src, c.l16); else glds_next<j * 1024>(c, vm, st.cur, c.l16);
    st.mg[g] = vm.n;
  }
}
template <int HALF, int LO, int HI> DI void stages(const Ctx& c, VM& vm, Stage& st, u32 xoff, u32 tf) { sfor<LO, HI>(LAMBDA(K) { stage<HALF, K.v>(c, vm, st, xoff, tf); }); }
template <int HALF, int NF, int SYNC, int PG, class BT, class HL, class HA> DI void gemm_lds(const Ctx& c, VM& vm, Stage& st, bool use_stats, int mst, u32 fv, f32x4 (&acc)[NF][4], BT& B, HL hl, HA ha) {
  const u32 b0 = vop(c.lb + img_off<HALF>(0) + c.l16), b1 = vop(c.lb + img_off<HALF>(4) + c.l16);
  auto rd = [&](int s, int tf) __attribute__((always_inline)) { return *LP(const u32x4, (s < 16 ? b0 + (s >> 2) * 16 * KB : b1) + tf * 4 * KB + (s & 3) * KB); };
  prof<true>(vm);
  auto cover = [](int g) constexpr { int e = g; while (e < 4 && !((SYNC >> (e + 1)) & 1)) ++e; return e; };
  need(vm, st.mg[cover(0)]); BAR0(); pin(st.sv);
  u32x4 a[2][4]; float s1, s2;
#pragma unroll
  for (int tf = 0; tf < 4; ++tf) a[0][tf] = rd(0, tf);
  SB();
  sfor<0, 20>(LAMBDA(S) {
    constexpr int s = S.v;
    if constexpr ((s & 3) == 3 && s < 19 && ((SYNC >> ((s + 1) >> 2)) & 1)) {
      need(vm, st.mg[cover((s + 1) >> 2)]); if ((s + 1) >> 2 == PG) need(vm, mst);
      BAR0();
    }
    need(vm, B.m[s * NF + NF - 1]);
    __builtin_amdgcn_s_waitcnt(0xC07F);
    prof<false>(vm);
    SB();
    sfor<0, 4 * NF>(LAMBDA(J) {
      constexpr int nf = J.v >> 2, tf = J.v & 3;
      mma<s == 0, slotB<BT::ID>(s * NF + nf), -1>(acc[nf][tf], B.f[s * NF + nf], a[s & 1][tf]);
      if constexpr (s < 19 && nf == 0) { a[(s + 1) & 1][tf] = rd(s + 1, tf); SB(); }
      if constexpr (J.v >= 4 && (J.v - 4) % NF == 0) { SB(); hl(IC<s * NF + (J.v - 4) / NF>{}); SB(); }
      if constexpr (J.v == 0 && s == 4 * PG - 1) { flag_store(c, vm, fv); SB(); }
      if constexpr (J.v == 5) { SB(); ha(S); SB(); }
      if constexpr (J.v == 7 - (NF == 3) && s >= STAT0 && s < STAT0 + 6) {
        SB();
        if constexpr (s == STAT0) { s1 = __uint_as_float(st.sv.x) + __uint_as_float(st.sv.z); s2 = __uint_as_float(st.sv.y) + __uint_as_float(st.sv.w); pin(s1); pin(s2); }
        if constexpr (s == STAT0 + 1) { auto t = __builtin_amdgcn_permlane16_swap(__float_as_uint(s1), __float_as_uint(s2), false, false); s1 = __uint_as_float(t[0]) + __uint_as_float(t[1]); s2 = s1; pin(s1); pin(s2); }
        if constexpr (s == STAT0 + 2) { auto t = __builtin_amdgcn_permlane32_swap(__float_as_uint(s1), __float_as_uint(s2), false, false); s1 = __uint_as_float(t[0]) + __uint_as_float(t[1]); s2 = s1; pin(s1); pin(s2); }
        if constexpr (s == STAT0 + 3) { auto t = __builtin_amdgcn_permlane16_swap(__float_as_uint(s1), __float_as_uint(s2), false, false); s1 = __uint_as_float(t[0]) * (-1.0f / D); s2 = __uint_as_float(t[1]) * (1.0f / D); pin(s1); pin(s2); }
        if constexpr (s == STAT0 + 4) { s2 = __builtin_fmaxf(s2 - s1 * s1, 0.f) + 1e-5f; pin(s2); }
        if constexpr (s == STAT0 + 5) { float rstd = __builtin_amdgcn_rsqf(s2); if (!use_stats) { s1 = 0.f; rstd = 1.f; }
          *LP(f32x2, c.lb + L_STAT + HALF * 512 + c.w * 128 + ((c.l16 >> 1) & 0x78)) = f32x2{s1, rstd}; }
        SB();
      }
    });
    SB();
  });
  asm volatile("s_nop 7");
  prof<true>(vm);
  SB();
}
struct EpQ { f32x4 cs[2]; f32x2 st[4]; u32 vo; float qs; };
struct EpG { f32x4 cs[3]; f32x2 st[4]; u32 vo; };
template <int HALF> DI void pre_q(const Ctx& c, EpQ& e) {
  const u32 l = vop(c.l16), cg = sop(c.cg);
  const u32 pc = c.lb + L_CS + c.w * 128 + ((l >> 3) & 0x60), ps = c.lb + L_STAT + HALF * 512 + ((l >> 1) & 0x78);
  e.cs[0] = *LP(const f32x4, pc); e.cs[1] = *LP(const f32x4, pc + 16);
#pragma unroll
  for (int tf = 0; tf < 4; ++tf) e.st[tf] = *LP(const f32x2, ps + tf * 128);
  e.qs = cg < 5 ? 0.22360679774997896f * 1.4426950408889634f : 1.0f;
  const u32 base = cg < 15 ? A_QKV + ((16 * cg + 4 * c.w) * 2048 + c.sq + HALF * 1024) * 16 : OOR;
  e.vo = base + ((l >> 8) << 15) + (l & 0xF0);
}
DI void epi_q(const Ctx& c, VM& vm, const EpQ& e, const f32x4 (&acc)[2][4]) {
#pragma unroll
  for (int tf = 0; tf < 4; ++tf) {
    const float rs = e.st[tf].y * e.qs;
    const f32x4 a = (acc[0][tf] + e.st[tf].x * e.cs[0]) * rs, b = (acc[1][tf] + e.st[tf].x * e.cs[1]) * rs;
    st16c(c, vm, e.vo + tf * 256, u32x4{pk2(a.x, a.y), pk2(a.z, a.w), pk2(b.x, b.y), pk2(b.z, b.w)});
  }
}
template <int HALF> DI void pre_g(const Ctx& c, EpG& e) {
  const u32 l = vop(c.l16), cg = sop(c.cg);
  const u32 pc = c.lb + L_CS + 512 + c.w * 192 + ((l >> 4) & 0x30), ps = c.lb + L_STAT + HALF * 512 + ((l >> 1) & 0x78);
#pragma unroll
  for (int nf = 0; nf < 3; ++nf) e.cs[nf] = *LP(const f32x4, pc + nf * 64);
#pragma unroll
  for (int tf = 0; tf < 4; ++tf) e.st[tf] = *LP(const f32x2, ps + tf * 128);
  e.vo = A_HF + (((c.sq + HALF * 1024) >> 4) * 192 + 12 * cg + 3 * c.w) * 256 + (l >> 8) * (192 * 256) + (l & 0xF0);
}
DI void epi_g(const Ctx& c, VM& vm, const EpG& e, const f32x4 (&acc)[3][4]) {
#pragma unroll
  for (int nf = 0; nf < 3; ++nf) {
    u32 R[4];
#pragma unroll
    for (int tf = 0; tf < 4; ++tf) { const f32x4 a = acc[nf][tf] + e.st[tf].x * e.cs[nf]; const float r2 = e.st[tf].y * e.st[tf].y; R[tf] = pk2(a.x * r2 * a.z, a.y * r2 * a.w); }
    { auto t = __builtin_amdgcn_permlane32_swap(R[0], R[2], false, false); R[0] = t[0]; R[2] = t[1]; }
    { auto t = __builtin_amdgcn_permlane32_swap(R[1], R[3], false, false); R[1] = t[0]; R[3] = t[1]; }
    { auto t = __builtin_amdgcn_permlane16_swap(R[0], R[1], false, false); R[0] = t[0]; R[1] = t[1]; }
    { auto t = __builtin_amdgcn_permlane16_swap(R[2], R[3], false, false); R[2] = t[0]; R[3] = t[1]; }
    st16c(c, vm, e.vo + nf * 256, u32x4{R[0], R[1], R[2], R[3]});
  }
}

struct AW { u32x4 f[2][5]; u32 s0; int m[2][5]; static constexpr int slot(int, int) { return -1; } };
template <bool X> struct AD { u32x4 f[2][12]; u32 cur; int m[2][12]; static constexpr int slot(int tf, int s) { return X && tf == 1 && s >= 9 ? 51 + s : -1; } };
template <int HALF, int K> DI void ld_w(const Ctx& c, VM& vm, AW& a) {
  constexpr int tf = K / 5, j = K % 5;
  if constexpr (j == 0 || j == 4) a.s0 = sop(c.bof) + (HALF * 64 + tf) * 20480 + j * KB;
  a.m[tf][j] = ld16<-1, true, (j & 3) * 1024>(c.ra, vm, a.f[tf][j], c.l16, a.s0);
}
template <int NS, int HALF> constexpr int kord(int p) { return NS == 12 && HALF == 1 ? (p + 5) % 12 : p; }
template <int HALF, int K, bool X> DI void ld_d(const Ctx& c, VM& vm, AD<X>& a) {
  constexpr int tf = K / 12, s = kord<12, HALF>(K % 12);
  if constexpr (K % 12 == 0 || (s & 3) == 0) a.cur = sop(c.bhf) + (HALF * 64 + tf) * 192 * 256 + (s & ~3) * KB;
  a.m[tf][s] = ld16<AD<X>::slot(tf, s), true, (s & 3) * 1024>(c.ra, vm, a.f[tf][s], c.l16, a.cur);
}
template <int TF, int S> DI u32x4 get_a(VM& vm, AW& a) { need(vm, a.m[TF][S]); return a.f[TF][S]; }
template <int TF, int S, bool X> DI u32x4 get_a(VM& vm, AD<X>& a) { need(vm, a.m[TF][S]); if constexpr (AD<X>::slot(TF, S) < 0) return a.f[TF][S]; else return u32x4{}; }
template <int HALF, int NS, class BT, class AT, class HL, class HA> DI void gemm_reg(const Ctx& c, VM& vm, BT& B, AT& a, HL hl, HA ha) {
  f32x4 acc[10];
  const u32 p0 = vop(c.rw) + (HALF ? L_HY - L_HX : 0), p1 = p0 ^ 16;
  auto wr = [&](int j) __attribute__((always_inline)) { *LP(f32x4, (((j % 5) & 1) ? p1 : p0) + j * KB) = acc[j]; SB(); };
  sfor<0, 2 * NS>(LAMBDA(P) {
    constexpr int tf = P.v / NS, s = kord<NS, HALF>(P.v % NS);
    if constexpr ((P.v & 1) == 0) prof<true, false>(vm);
    need(vm, B.m[s * 5 + 4]);
    const u32x4 o = get_a<tf, s>(vm, a);
    SB();
    sfor<0, 5>(LAMBDA(J) { constexpr int nf = J.v, g = P.v * 5 + nf;
      mma<P.v % NS == 0, slotB<BT::ID>(s * 5 + nf), AT::slot(tf, s)>(acc[tf * 5 + nf], B.f[s * 5 + nf], o);
      if constexpr (g % 4 == 1) { SB(); hl(IC<g / 4>{}); SB(); }
      if constexpr (g % 10 == 4) { SB(); ha(IC<g / 10>{}); SB(); }
      if constexpr (g >= NS * 5 + 3 && g < NS * 5 + 23 && (g - NS * 5 - 3) % 4 == 0) { SB(); wr((g - NS * 5 - 3) / 4); } });
    SB();
  });
  prof<true>(vm);
  asm volatile("s_nop 3"); SB();
  sfor<5, 10>(LAMBDA(J) { wr(J.v); });
  prof<true>(vm);
}
struct XR { u32x4 m; u32 t; };
struct Red { f32x4 a, b; f32x2 t; u32 pa, pb, pt, vo, vt, vs; };
template <int HALF> DI void red_pre(const Ctx& c, Red& r, const XR& xr, u32 xoff) {
  r.a = f32x4{bf_lo(xr.m.x), bf_hi(xr.m.x), bf_lo(xr.m.y), bf_hi(xr.m.y)}; r.b = f32x4{bf_lo(xr.m.z), bf_hi(xr.m.z), bf_lo(xr.m.w), bf_hi(xr.m.w)}; r.t = f32x2{bf_lo(xr.t), bf_hi(xr.t)};
  r.pa = vop(c.rpa) + (HALF ? L_HY - L_HX : 0); r.pb = r.pa ^ 16; r.pt = vop(c.rpt) + (HALF ? L_HY - L_HX : 0);
  r.vo = vop(c.rvo) + (xoff + HALF * 64 * 20480); r.vt = vop(c.rvt) + (xoff + HALF * 64 * 20480); r.vs = vop(c.rvs) + (xoff + HALF * 65536);
  pin(r.a); pin(r.b); pin(r.t); pin(r.pa); pin(r.pb); pin(r.pt); pin(r.vo); pin(r.vt); pin(r.vs);
}
template <int HALF> DI void red_out(const Ctx& c, VM& vm, Red& r, XR& xr, u16* out) {
  prof<true>(vm);
  f32x4 pa[4], pb[4]; f32x2 pt[4];
#pragma unroll
  for (int ww = 0; ww < 4; ++ww) { pa[ww] = *LP(const f32x4, r.pa + ww * 10 * KB); pb[ww] = *LP(const f32x4, r.pb + ww * 10 * KB); }
#pragma unroll
  for (int ww = 0; ww < 4; ++ww) pt[ww] = *LP(const f32x2, r.pt + ww * 10 * KB);
  SB();
  const f32x4 a = ((r.a + pa[0]) + pa[1]) + (pa[2] + pa[3]), b = ((r.b + pb[0]) + pb[1]) + (pb[2] + pb[3]);
  xr.m = u32x4{pk2(a.x, a.y), pk2(a.z, a.w), pk2(b.x, b.y), pk2(b.z, b.w)};
  st16c(c, vm, r.vo, xr.m);
  const f32x2 t = ((r.t + pt[0]) + pt[1]) + (pt[2] + pt[3]);
  xr.t = pk2(t.x, t.y);
  st4c(c, vm, r.vt, xr.t);
  const f32x2 sp = (a.xy + a.zw) + (b.xy + b.zw) + t;
  const f32x2 qp = a.xy * a.xy + (a.zw * a.zw + (b.xy * b.xy + (b.zw * b.zw + t * t)));
  float s1 = sp.x + sp.y, s2 = qp.x + qp.y;
  s1 = dpp_add<0xB1>(s1); s2 = dpp_add<0xB1>(s2); s1 = dpp_add<0x4E>(s1); s2 = dpp_add<0x4E>(s2); s1 = dpp_add<0x141>(s1); s2 = dpp_add<0x141>(s2);
  st8c(c, vm, r.vs, u32x2{__float_as_uint(s1), __float_as_uint(s2)});
  if (out) {
    const u32 lane = vop(c.l16) >> 4;
    GAS u16* o = (GAS u16*)out + (c.sw + HALF * 1024 + 8 * c.w + (lane >> 3)) * D + 80 * ((c.bid >> 3) & 7);
    *(GAS u32x4*)(o + 8 * (lane & 7)) = xr.m; *(GAS u32*)(o + 64 + 2 * (lane & 7)) = xr.t;
  }
  prof<true>(vm);
}

struct AIn { u32x4 Q0; u32x2 qc; u32 v0, v1, v2, cur; int r0, mark; };
DI u32 wg_qg(u32 RG, u32 cg) { return 2 * (RG >> 2) + (cg >> 3) + 8 * ((RG & 3) * 8 + (cg & 7)); }
DI u32 a_head(u32 bid) { return 4 * (bid & 7) + ((bid >> 3) & 3); }
DI u32 a_dep(const Ctx& c, int half) {
  const int h = a_head(c.bid), gp = half * 8 + (c.bid >> 5), clo = 5 * (h >> 1) + 2 * (h & 1), lane = c.l16 >> 4;
  const int j = lane < 36 ? lane : 0, sec = j / 12, lh = (j / 6) & 1, pi = (j >> 1) % 3, rg = j & 1;
  int g2 = gp - (sec == 0 ? 0 : pi); if (g2 < 0) g2 = gp;
  return (wg_qg(2 * (g2 & 7) + rg, (80 * sec + clo + 2 * lh) >> 4) << 7) | (g2 >> 3);
}
DI void a_poll(const Ctx& c, VM& vm, Poll& p, u32 fl, u32 dep) { poll_issue(c, vm, p, dep & ~1u, fl + 1 + (dep & 1)); }
template <int HALF, int K> DI void a_stage(const Ctx& c, VM& vm, AIn& in) {
  if constexpr (K == 0) {
    const u32 bid = sop(c.bid), h = a_head(bid), odd = h & 1, cA = 5 * (h >> 1) + 3 * odd, cC = 5 * (h >> 1) + 2, l = vop(c.l16), r16 = l & 0x1F0; const bool hh = l >= 512;
    in.r0 = (HALF * 8 + (bid >> 5)) * 128;
    in.v0 = (hh ? 81 + cA : 80 + cA) * 32768 + r16;
    ld16<-1, true, 0>(c.ra, vm, in.Q0, in.v0, A_QKV - 80 * 32768 + (in.r0 + 32 * c.w) * 16);
    in.v1 = (hh ? 160 + cA : 80 + cC) * 32768 + r16 - 1024;
    in.v2 = (hh ? 160 + cC : 161 + cA) * 32768 + (r16 ^ (hh ? 128 : 64)) - 2048;
    in.cur = A_QKV - 80 * 32768 + (in.r0 + 32 * c.w) * 16 + odd * 8;
  } else if constexpr (K == 1) {
    ld8c<1024>(c.ra, vm, in.qc, in.v1, in.cur);
    const u32 po = c.m0a + (HALF ? L_HY : L_HX) + 3072 + (vop(c.l16) >> 1);
#pragma unroll
    for (int u = 0; u < 3; ++u) *LP(u32x2, po + u * TP) = u32x2{0x3F803F80u, 0x3F803F80u};
  } else {
    constexpr int u = (K - 2) / 3, j = (K - 2) % 3;
    if constexpr (j == 0) {
      int krow = in.r0 - 256 + 96 * (int)c.w + 32 * u; if (HALF == 0 && krow < 0) krow = in.r0;
      glds_head<(HALF ? L_HY : L_HX) + u * TP, A_QKV>(c, vm, in.cur, c.m0a, krow * 16, in.v0);
    } else glds_next<j * 1024>(c, vm, in.cur, j == 1 ? in.v1 : in.v2);
    in.mark = vm.n;
  }
}
DI float mx3(float a, float b, float c) { float r; asm("v_max3_f32 %0, %1, %2, %3" : "=v"(r) : "v"(a), "v"(b), "v"(c)); return r; }
DI void smfma0(f32x16& d, u32x4 a, u32x4 b) { asm volatile("v_mfma_f32_32x32x16_bf16 %0, %1, %2, 0" : "=&v"(d) : "v"(a), "v"(b)); }
DI void smfmac(f32x16& d, u32x4 a, u32x4 b, const f32x16& cc) { asm volatile("v_mfma_f32_32x32x16_bf16 %0, %1, %2, %3" : "=&v"(d) : "v"(a), "v"(b), "v"(cc)); }
DI void smfma1(f32x16& d, u32x2 a, u32x2 b) { asm volatile("v_mfma_f32_32x32x8_bf16 %0, %1, %2, %0" : "+v"(d) : "v"(a), "v"(b)); }
DI void omfma(f32x16& d, u32x4 a, u32x4 b) { asm volatile("v_mfma_f32_32x32x16_bf16 %0, %1, %2, %0" : "+v"(d) : "v"(a), "v"(b)); }
#define MX3(a, b, c) __builtin_fmaxf(__builtin_fmaxf((a), (b)), (c))
template <int HALF, class HL, class HA> DI void attn(const Ctx& c, VM& vm, AIn& in, HL hl, HA ha) {
  const u32 bid = sop(c.bid), h = a_head(bid), odd = h & 1, l = vop(c.l16), r16 = l & 0x1F0; const bool hh = l >= 512;
  const int q0 = (HALF * 8 + (bid >> 5)) * 128 + 32 * c.w;
  const u32 img = c.lb + (HALF ? L_HY : L_HX) + c.w * TP;
  const u32 kp0 = vop(img + l), kp1 = vop(img + 1024 + odd * 8 + r16), mp = vop(c.lb + L_MASK + l);
  const u32 lane = l >> 4, pc4 = lane & 3, db = (lane >> 4) & 1, kq = ((lane >> 3) & 4) + ((lane >> 2) & 3);
  const u32 vpl = db ? (pc4 ? 3 : 2) : pc4 & 1;
  const u32 vlo = vop(img + 1536 + vpl * 512 + (db ? odd : pc4 >> 1) * 8 + ((kq ^ (4 * vpl)) << 4)), vhi = vop(vlo ^ 128);
  const int t0 = (256 - q0) >> 5;
  auto vfrag = [&](int t, int s) __attribute__((always_inline)) { const u32x2 lo = trread(vlo + t * TP + s * 256), hi = trread(vhi + t * TP + s * 256); return u32x4{lo.x, lo.y, hi.x, hi.y}; };
  u32x4 sel[4];
#pragma unroll
  for (int j = 0; j < 4; ++j) sel[j] = *LP(const u32x4, mp + j * KB);
  const u32x4 mkA = *LP(const u32x4, mp + 4 * KB), mkB = *LP(const u32x4, mp + 5 * KB);
  prof<true>(vm);
  need(vm, in.mark); BARL(); pin(in.Q0); pin(in.qc);
  prof<true>(vm);
  u32x4 K0[9]; u32x2 K1[9];
  auto kread = [&](int t) __attribute__((always_inline)) { K0[t] = *LP(const u32x4, kp0 + t * TP); K1[t] = *LP(const u32x2, kp1 + t * TP); };
  auto kfix = [&](int t) __attribute__((always_inline)) { if (HALF == 0) { K1[t].x = hh ? (t < t0 ? 0xF149u : 0u) : K1[t].x; pin(K1[t]); } };
  kread(0); kread(8);
  u32x4 Q0 = in.Q0; u32x2 Q1 = hh ? u32x2{HALF == 0 ? 0x3F80u : 0u, 0} : in.qc;
  pin(Q1);
  SB(); kread(1); kread(2); SB();
  f32x16 S[2], M;
  kfix(0); kfix(8);
  SB(); asm volatile("s_nop 0"); smfma0(M, K0[0], Q0);
  SB(); kread(3); kread(4); SB();
  smfma1(M, K1[0], Q1);
  SB(); hl(IC<0>{}); kread(5); SB();
  smfma0(S[0], K0[8], Q0);
  SB(); kread(6); kread(7); SB();
  smfma1(S[0], K1[8], Q1);
  SB(); hl(IC<1>{});
  const u32x4 V0a = vfrag(0, 0), V8a = vfrag(8, 0), V0b = vfrag(0, 1), V8b = vfrag(8, 1);
  SB(); hl(IC<2>{});
#pragma unroll
  for (int t = 1; t < 8; ++t) kfix(t);
  asm volatile("s_nop 1" : "+v"(M), "+v"(S[0]));
  prof<true>(vm);
  SB();
#pragma unroll
  for (int g = 0; g < 16; ++g) M[g] = __uint_as_float((sel[g >> 2][g & 3] & __float_as_uint(S[0][g])) | (~sel[g >> 2][g & 3] & __float_as_uint(M[g])));
  float m = mx3(mx3(mx3(M[0], M[1], M[2]), mx3(M[3], M[4], M[5]), mx3(M[6], M[7], M[8])), mx3(mx3(M[9], M[10], M[11]), mx3(M[12], M[13], M[14]), M[15]), M[15]);
  { auto t = __builtin_amdgcn_permlane32_swap(__float_as_uint(m), __float_as_uint(m), false, false); m = mx3(__uint_as_float(t[0]), __uint_as_float(t[1]), __uint_as_float(t[1])); }
  f32x16 negm, O;
#pragma unroll
  for (int g = 0; g < 16; ++g) negm[g] = -m;
  pin(negm);
  prof<true>(vm);
  SB(); ha(IC<0>{}); hl(IC<3>{});
  prof<true>(vm);
  SB();
#define EM(i) __builtin_amdgcn_exp2f(M[i] - m)
  smfmac(S[1], K0[1], Q0, negm);
  u32x4 P; P.x = vop(pk2(EM(0), EM(1))); P.y = vop(pk2(EM(2), EM(3)));
  SB(); smfma1(S[1], K1[1], Q1);
  P.z = vop(pk2(EM(4), EM(5))); P.w = vop(pk2(EM(6), EM(7)));
  u32x4 A8 = P & mkA, A0 = P & ~mkA; pin(A8); pin(A0);
  P.x = vop(pk2(EM(8), EM(9)));
  SB(); hl(IC<4>{}); SB(); smfma0(O, V0a, A0);
  P.y = vop(pk2(EM(10), EM(11))); P.z = vop(pk2(EM(12), EM(13)));
  SB(); hl(IC<5>{}); ha(IC<9>{}); SB(); omfma(O, V8a, A8);
  P.w = vop(pk2(EM(14), EM(15)));
  u32x4 Pp = P & mkB, B0 = P & ~mkB, Vp = V8b; pin(Pp); pin(B0);
  SB(); hl(IC<6>{}); SB();
#define EX(i) __builtin_amdgcn_exp2f(S[t & 1][i])
  sfor<1, 8>(LAMBDA(T) {
    constexpr int t = T.v;
    prof<true>(vm);
    const u32x4 Va = vfrag(t, 0), Vb = vfrag(t, 1);
    u32x4 Pa, Pb;
    SB();
    if constexpr (t == 1) omfma(O, V0b, B0); else omfma(O, Vp, Pp);
    Pa.x = vop(pk2(EX(0), EX(1))); Pa.y = vop(pk2(EX(2), EX(3)));
    SB(); hl(IC<4 + 3 * t>{}); SB();
    if constexpr (t < 7) smfmac(S[(t + 1) & 1], K0[t + 1], Q0, negm);
    Pa.z = vop(pk2(EX(4), EX(5))); Pa.w = vop(pk2(EX(6), EX(7)));
    SB(); hl(IC<5 + 3 * t>{}); SB();
    if constexpr (t < 7) smfma1(S[(t + 1) & 1], K1[t + 1], Q1);
    Pb.x = vop(pk2(EX(8), EX(9))); Pb.y = vop(pk2(EX(10), EX(11)));
    SB();
    if constexpr (t == 1) { omfma(O, Vp, Pp); SB(); }
    ha(T);
    SB(); hl(IC<6 + 3 * t>{});
    __builtin_amdgcn_s_waitcnt(0xC07F);
    SB();
    omfma(O, Va, Pa);
    Pb.z = vop(pk2(EX(12), EX(13))); Pb.w = vop(pk2(EX(14), EX(15)));
    Pp = Pb; Vp = Vb;
    SB();
  });
  const u32 vb = A_OF + ((q0 >> 4) * 80 + 5 * (h >> 1)) * 256 + ((l >> 8) & 1) * 20480 + (l & 0xF0);
  const u32 vo = vb + (3 * odd + (hh ? 1 : 0)) * 256, vt = hh ? OOR : vb + 512 + odd * 8;
  asm volatile("s_nop 1" :: "v"(negm));
  omfma(O, Vp, Pp);
  prof<true>(vm);
  ha(IC<8>{});
  asm volatile("s_nop 11" : "+v"(O) : "v"(vo), "v"(vt));
  if (__builtin_expect(!__all(O[12] < 1.0995116e12f), 0)) {
    f32x16 X;
    auto score = [&](int t) __attribute__((always_inline)) {
      u32x4 k0 = *LP(const u32x4, kp0 + t * TP); u32x2 k1 = *LP(const u32x2, kp1 + t * TP);
      if (HALF == 0) k1.x = hh ? (t < t0 ? 0xF149u : 0u) : k1.x;
      asm volatile("s_nop 1\n\tv_mfma_f32_32x32x16_bf16 %0, %1, %2, 0\n\tv_mfma_f32_32x32x8_bf16 %0, %3, %4, %0\n\ts_nop 11" : "=&v"(X) : "v"(k0), "v"(Q0), "v"(k1), "v"(Q1));
      const u32 x0 = t == 0 ? ~0u : 0u, x1 = (t & 7) ? ~0u : 0u;
#pragma unroll
      for (int j = 0; j < 4; ++j) { const u32x4 ok = (*LP(const u32x4, mp + j * KB) ^ x0) | x1;
#pragma unroll
        for (int e = 0; e < 4; ++e) X[4 * j + e] = __uint_as_float((ok[e] & __float_as_uint(X[4 * j + e])) | (~ok[e] & 0xFF800000u)); }
    };
    float mt = -INFINITY;
#pragma unroll 1
    for (int t = 0; t < 9; ++t) { score(t);
#pragma unroll
      for (int g = 0; g < 16; ++g) mt = __builtin_fmaxf(mt, X[g]); }
    { auto t = __builtin_amdgcn_permlane32_swap(__float_as_uint(mt), __float_as_uint(mt), false, false); mt = __builtin_fmaxf(__uint_as_float(t[0]), __uint_as_float(t[1])); }
#pragma unroll
    for (int g = 0; g < 16; ++g) O[g] = 0.f;
#pragma unroll 1
    for (int t = 0; t < 9; ++t) { score(t);
      u32x4 pa, pb;
#pragma unroll
      for (int g = 0; g < 4; ++g) { pa[g] = pk2(__builtin_amdgcn_exp2f(X[2 * g] - mt), __builtin_amdgcn_exp2f(X[2 * g + 1] - mt)); pb[g] = pk2(__builtin_amdgcn_exp2f(X[8 + 2 * g] - mt), __builtin_amdgcn_exp2f(X[9 + 2 * g] - mt)); }
      const u32x4 va = vfrag(t, 0), vb = vfrag(t, 1);
      asm volatile("s_nop 1\n\tv_mfma_f32_32x32x16_bf16 %0, %1, %2, %0\n\tv_mfma_f32_32x32x16_bf16 %0, %3, %4, %0\n\ts_nop 11" : "+v"(O) : "v"(va), "v"(pa), "v"(vb), "v"(pb)); }
  }
  const float inv = __builtin_amdgcn_rcpf(O[12]);
  st16c(c, vm, vo, u32x4{pk2(O[0] * inv, O[1] * inv), pk2(O[2] * inv, O[3] * inv), pk2(O[4] * inv, O[5] * inv), pk2(O[6] * inv, O[7] * inv)});
  st8c(c, vm, vt, u32x2{pk2(O[8] * inv, O[9] * inv), pk2(O[10] * inv, O[11] * inv)});
}

struct Job { u32x2 a[4], b[4]; u32 kind, dst, gn0, m, magic, stride, cs; };
DI void job_load(const Params& p, Job& j, u32 id, u32 w, u32 lane) {
  const GAS unsigned char* A; const GAS unsigned char* B; u32 ld, cb, s;
  if (id < 150) { cb = (id * 205) >> 10; s = 4 * (id - 5 * cb) + w; A = (const GAS unsigned char*)p.wqkv; B = A + 8; ld = 3840; j.kind = 0; j.dst = OFF_BQ; j.m = 2; j.magic = 32768; j.stride = 40; j.cs = OFF_CS + s * CS_ROW + cb * 256; }
  else if (id < 390) { id -= 150; cb = (id * 205) >> 10; s = 4 * (id - 5 * cb) + w; A = (const GAS unsigned char*)p.wg; B = (const GAS unsigned char*)p.wu; ld = 3072; j.kind = 1; j.dst = OFF_BG; j.m = 3; j.magic = 21846; j.stride = 60; j.cs = OFF_CS + s * CS_ROW + 7680 + cb * 256; }
  else { j.kind = 2; ld = 1280; j.m = 5; j.magic = 13108; j.cs = OOR;
    if (id < 440) { id -= 390; cb = (id * 205) >> 10; s = 4 * (id - 5 * cb) + w; A = (const GAS unsigned char*)p.wo; j.dst = OFF_BW; j.stride = 100; }
    else { const bool none = id >= 560; id = none ? 0 : id - 440; cb = (id * 171) >> 11; s = 4 * (id - 12 * cb) + w; A = (const GAS unsigned char*)p.wd; j.dst = none ? OOR : OFF_BD; j.stride = 240; }
    B = A + 8; }
  j.dst += s * j.m * KB; j.gn0 = 4 * cb;
  const u32 so = 32 * s * ld + (cb << (j.kind == 1 ? 6 : 7)), vo = (lane >> 3) * ld + ((lane & 7) << (j.kind == 1 ? 3 : 4));
#pragma unroll
  for (int q = 0; q < 4; ++q) { j.a[q] = *(const GAS u32x2*)(A + so + q * 8 * ld + vo); j.b[q] = *(const GAS u32x2*)(B + so + q * 8 * ld + vo); }
}
DI void job_run(const Ctx& c, const Job& j, u32 lane) {
  const u32 L = c.lb + 16 * KB + c.w * 4608, g = j.kind == 1 ? ~0u : 0u;
#pragma unroll
  for (int q = 0; q < 4; ++q) {
    const u32 t = (j.a[q].y ^ j.b[q].x) & g;
    *LP(u32x4, L + (8 * q + (lane >> 3)) * 144 + (lane & 7) * 16) = u32x4{j.a[q].x, j.a[q].y ^ t, j.b[q].x ^ t, j.b[q].y};
  }
  const u32 ra = L + (8 * (lane >> 4) + ((lane & 15) >> 2)) * 144 + ((lane & 3) << (j.kind == 0 ? 4 : 3)), rb = ra + (j.kind == 0 ? 8 : 32);
  u32x4 v[4]; f32x4 cs[4];
#pragma unroll
  for (int f = 0; f < 4; ++f) { const u32 a = ((f & 1) ? rb : ra) + (f >> 1) * 64; const u32x2 lo = trread(a), hi = trread(a + 4 * 144); v[f] = u32x4{lo.x, lo.y, hi.x, hi.y}; }
  const u32x4 ones = {0x3F803F80u, 0x3F803F80u, 0x3F803F80u, 0x3F803F80u};
  asm volatile("s_nop 1\n\tv_mfma_f32_16x16x32_bf16 %0, %4, %8, 0\n\tv_mfma_f32_16x16x32_bf16 %1, %5, %8, 0\n\tv_mfma_f32_16x16x32_bf16 %2, %6, %8, 0\n\tv_mfma_f32_16x16x32_bf16 %3, %7, %8, 0"
               : "=&v"(cs[0]), "=&v"(cs[1]), "=&v"(cs[2]), "=&v"(cs[3]) : "v"(v[0]), "v"(v[1]), "v"(v[2]), "v"(v[3]), "v"(ones));
#pragma unroll
  for (int f = 0; f < 4; ++f) { const u32 gn = j.gn0 + f, q = (gn * j.magic) >> 16; __builtin_amdgcn_raw_buffer_store_b128(v[f], c.rs, j.dst + (q * j.stride + gn - q * j.m) * KB + c.l16, 0, 16); }
  asm volatile("s_nop 7" : "+v"(cs[0]), "+v"(cs[1]), "+v"(cs[2]), "+v"(cs[3]));
  const u32 fsel = lane & 15;
  const f32x4 o = fsel == 0 ? cs[0] : fsel == 1 ? cs[1] : fsel == 2 ? cs[2] : cs[3];
  const u32 oo = j.kind == 0 ? (fsel >> 1) * 128 + (lane >> 4) * 32 + (fsel & 1) * 16 : fsel * 64 + (lane >> 4) * 16;
  __builtin_amdgcn_raw_buffer_store_b128(__builtin_bit_cast(u32x4, o), c.rs, fsel < 4 ? j.cs + oo : OOR, 0, 16);
}
DI float red32(float v) {
  v = dpp_add<0x140>(dpp_add<0x141>(dpp_add<0x4E>(dpp_add<0xB1>(v))));
  auto t = __builtin_amdgcn_permlane16_swap(__float_as_uint(v), __float_as_uint(v), false, false); return __uint_as_float(t[0]) + __uint_as_float(t[1]);
}
DI void ln1(const Ctx& c, VM& vm, const u32x4 (&v)[3], u32 lane) {
  const u32 row = c.bid * 8 + c.w * 2 + (lane >> 5), l32 = lane & 31;
  const float e = l32 < 16 ? 1.f : 0.f;
  float f[24], s = 0, s2 = 0;
#pragma unroll
  for (int j = 0; j < 12; ++j) { f[2 * j] = bf_lo(v[j >> 2][j & 3]); f[2 * j + 1] = bf_hi(v[j >> 2][j & 3]); if (j < 8) s += f[2 * j] + f[2 * j + 1]; else s2 += f[2 * j] + f[2 * j + 1]; }
  const float mean = red32(s + e * s2) * (1.0f / D);
  float q = 0, q2 = 0;
#pragma unroll
  for (int j = 0; j < 24; ++j) { const float d = f[j] - mean; if (j < 16) q += d * d; else q2 += d * d; }
  const float mb = rbf(mean), sd = rbf(sqrtf(rbf(rbf(red32(q + e * q2) * (1.0f / D)) + 1e-5f))), rs = 1.0f / sd;
  const u32 wa = c.lb + l32 * 144 + (row & 7) * 16;
#pragma unroll
  for (int k = 0; k < 3; ++k) {
    u32x4 o;
#pragma unroll
    for (int j = 0; j < 4; ++j) { const u32 t = pk2(f[8 * k + 2 * j] - mb, f[8 * k + 2 * j + 1] - mb); o[j] = pk2(bf_lo(t) * rs, bf_hi(t) * rs); }
    *LP(u32x4, wa + k * 32 * 144) = o;
  }
}
DI void ln1_store(const Ctx& c, VM& vm) {
  const u32 t = threadIdx.x, ra = c.lb + (t >> 3) * 144 + (t & 7) * 16, dst = A_XA + (c.bid >> 1) * (80 * 256) + (c.bid & 1) * 128 + (t >> 3) * 256 + (t & 7) * 16;
#pragma unroll
  for (int k = 0; k < 3; ++k) st16c(c, vm, (k < 2 || t < 128) ? dst + k * 32 * 256 : OOR, *LP(const u32x4, ra + k * 32 * 144));
}

constexpr int CARRY0 = 8, CARRY1 = 4, CARRY2 = 0, CARRYST = 4;
#define G decltype(Gp)::v
#define QSYNC 0x0B
#define QPG 1
#define QP1 8
#define QP2 11
#define QC 14
#define AP1 1
#define AP2 3
#define AC 4
#define DPUB 4
#define DP1 7
#define DP2 10

__global__ void __launch_bounds__(256, 1) mega_kernel(Params p) {
  extern __shared__ __attribute__((aligned(16))) unsigned char smem[];
  Ctx c;
  c.ws = p.ws; c.rs = __builtin_amdgcn_make_buffer_rsrc(p.ws, 0, (int)OFF_ACT, 0x00020000);
  c.ra = __builtin_amdgcn_make_buffer_rsrc(p.ws + OFF_ACT, 0, (int)(2 * LB), 0x00020000);
  c.lb = (u32)(size_t)(LAS unsigned char*)smem;
  c.l16 = (threadIdx.x & 63) * 16; c.w = __builtin_amdgcn_readfirstlane(threadIdx.x >> 6); c.bid = blockIdx.x;
  const u32 x = c.bid & 7, i = c.bid >> 3;
  c.sq = 64 * ((x >> 1) * 4 + (i >> 3)); c.cg = (x & 1) * 8 + (i & 7); c.sw = 128 * x + 32 * (i >> 3);
  const u32 cgq = c.cg < 15 ? c.cg : 14;
  c.tq = (c.sq >> 4) + c.w;
  c.bof = A_OF + ((c.sw >> 4) * 80 + 20 * c.w) * 256; c.bhf = A_HF + ((c.sw >> 4) * 192 + 48 * c.w) * 256;
  c.m0x = c.lb + c.w * 4 * KB; c.m0a = c.lb + 3 * c.w * TP;
  { const int l = 0; (void)l; STAMP(0); }
  asm volatile("" ::: "a0", "a255");
  BF<0, 40> Bq; BF<1, 25> Bw; BF<2, 60> Bg; BF<3, 60> Bd;
  Bq.base = OFF_BQ + (cgq * 4 + c.w) * 40 * KB; Bw.base = OFF_BW + ((i & 7) * 4 + c.w) * 25 * KB;
  Bg.base = OFF_BG + (c.cg * 4 + c.w) * 60 * KB; Bd.base = OFF_BD + ((i & 7) * 4 + c.w) * 60 * KB;
  XR xr[2];
  Stage sx, sy; Poll pl; AIn ain; VM vm = {}; int mst = 0;
  const u32 lane = threadIdx.x & 63;
  u32 ep = __hip_atomic_load((const GAS u32*)(p.ws + OFF_EPOCH), RLX_AGENT);
  u32x4 lv[3];
  { const GAS u32x4* src = (const GAS u32x4*)((const GAS u16*)p.x + (c.bid * 8 + c.w * 2 + (lane >> 5)) * D);
    lv[0] = src[lane & 31]; lv[1] = src[32 + (lane & 31)]; lv[2] = src[64 + (lane & 15)]; }
  Job job[3];
  sfor<0, 3>(LAMBDA(J) { job_load(p, job[J.v], c.bid + 256 * J.v, c.w, lane); SB(); });
  {
    const u32 cc = lane & 7;
#pragma unroll
    for (int hf = 0; hf < 2; ++hf) {
      const GAS u16* src = (const GAS u16*)p.x + (c.sw + hf * 1024 + 8 * c.w + (lane >> 3)) * D + 80 * (i & 7);
      xr[hf].m = *(const GAS u32x4*)(src + 8 * cc); xr[hf].t = *(const GAS u32*)(src + 64 + 2 * cc);
    }
    if (threadIdx.x < 80) *LP(u32x4, c.lb + L_CS + threadIdx.x * 16) = u32x4{0, 0, 0, 0};
    const int z = (int)(lane & 31) - 4 * (int)(lane >> 5) - 8 * (int)c.w;
    *LP(u32x4, c.lb + L_MASK + c.w * KB + c.l16) = u32x4{z >= 0 ? ~0u : 0u, z >= 1 ? ~0u : 0u, z >= 2 ? ~0u : 0u, z >= 3 ? ~0u : 0u};
    *LP(u32x2, c.lb + L_MASK + (4 + (c.w >> 1)) * KB + c.l16 + (c.w & 1) * 8) = u32x2{(z >= 0 ? 0xFFFFu : 0u) | (z >= 1 ? 0xFFFF0000u : 0u), (z >= 2 ? 0xFFFFu : 0u) | (z >= 3 ? 0xFFFF0000u : 0u)};
    const u32 t16 = (8 * c.w + (lane >> 3)) & 15, g = (t16 >> 1) & 3, cg = i & 7, h = 8 * c.w + cc;
    c.dxx = ((c.tq >> 3) + 8 * (((c.tq >> 1) & 3) * 8 + cc)) << 7;
    c.doh = (((h >> 2) + 8 * ((h & 3) + 4 * x)) << 7) | (wg_qg(c.sw >> 6, 4 * c.w + (lane & 3)) << 23);
    c.foff = threadIdx.x == 0 ? OFF_FLAGS + c.bid * 128 : OOR;
    c.daa = a_dep(c, 0) | (a_dep(c, 1) << 16);
    c.rw = c.lb + L_HX + c.w * 10 * KB + (lane & 15) * 64 + (((lane >> 4) ^ (lane >> 1)) & 3) * 16;
    const u32 base = c.lb + L_HX + (c.w >> 1) * 5 * KB + t16 * 64, row = c.sw + 8 * c.w + (lane >> 3);
    c.rpa = base + (cc >> 1) * KB + (((2 * (cc & 1)) ^ g ^ ((cc >> 1) & 1)) << 4);
    c.rpt = base + 4 * KB + (((cc >> 1) ^ g) << 4) + (cc & 1) * 8;
    c.rvo = ((row >> 4) * 80 + 10 * cg + cc) * 256 + t16 * 16;
    c.rvt = ((row >> 4) * 80 + 10 * cg + 8 + (cc >> 2)) * 256 + t16 * 16 + (cc & 3) * 4;
    c.rvs = cc == 0 ? (A_SA - A_XA) + row * 64 + cg * 8 : OOR;
  }
  STAMP(104);
  ep = __builtin_amdgcn_readfirstlane(ep);
  STAMP(105);
  flag_store(c, vm, ep);
  ln1(c, vm, lv, lane);
  BARL();
  ln1_store(c, vm);
  STAMP(106);
  job_run(c, job[0], lane);
  STAMP(107);
  asm volatile("s_waitcnt vmcnt(0)\n\ts_barrier" : "+v"(xr[0].m), "+v"(xr[0].t), "+v"(xr[1].m), "+v"(xr[1].t) :: "memory");
  STAMP(108);
  flag_store(c, vm, ep + 1);
  job_run(c, job[1], lane);
  STAMP(109);
  vm.n = 0; vm.done = 0;
  poll_issue(c, vm, pl, (lane < 4 ? 2 * (c.tq + (lane >> 1) * 64) + (lane & 1) : 5 * ((cgq * 4 + c.w) >> 1) + (lane - 4) % 5) << 7, ep + 1);
  if (c.bid < 48) job_run(c, job[2], lane);
  poll_again(c, vm, pl);
  STAMP(110);
  poll_check<true>(c, vm, pl);
  STAMP(111);
  loadB<0, 24>(c, vm, Bq);
  BARL();
  stages<0, 0, 13>(c, vm, sx, A_XA, c.tq);
  STAMP(112);
  need(vm, Bq.m[9]);
  STAMP(113);
  u32 fl = ep + 2, dq = threadIdx.x << 7;
#pragma unroll 1
  for (int l = 0; l < 2; ++l) {
    vm.n = 0; vm.done = -1000; sx.mg[0] = -CARRY0; sx.mg[1] = -CARRY1; sx.mg[2] = -CARRY2; mst = -CARRYST;
    sfor<0, 24>(LAMBDA(J) { Bq.m[J.v] = -CARRY0; });
    asm volatile("; MARK QX");
    { f32x4 acc[2][4]; EpQ e;
      TS(0, 0);
      gemm_lds<0, 2, QSYNC, QPG>(c, vm, sx, l != 0, mst, fl, acc, Bq, LAMBDA(Gp) {
        at<G, 0, 1, 8>(LAMBDA(K) { stage<0, 13 + K.v>(c, vm, sx, 0, 0); });
        at<G, 8, 1, 16>(LAMBDA(K) { ldB<24 + K.v>(c, vm, Bq); });
        at<G, 2 * QC + 1, 1, 13>(LAMBDA(K) { stage<1, K.v>(c, vm, sy, A_XA, c.tq); });
      }, LAMBDA(S) { constexpr int s = S.v;
        if (s == QP1) poll_issue(c, vm, pl, dq, fl);
        if (s == QP2) poll_again(c, vm, pl);
        if (s == QC) { TS(0, 1); poll_check<true>(c, vm, pl); TS(0, 2); }
        if (s == 17) pre_q<0>(c, e);
      });
      TS(0, 3);
      epi_q(c, vm, e, acc); mst = vm.n; stages<1, 39 - 2 * QC, 13>(c, vm, sy, 0, 0); TS(0, 4); }
    asm volatile("; MARK QY");
    { f32x4 acc[2][4]; EpQ e;
      TS(1, 0);
      gemm_lds<1, 2, QSYNC, QPG>(c, vm, sy, l != 0, mst, fl + 1, acc, Bq, LAMBDA(Gp) {
        at<G, 0, 1, 8>(LAMBDA(K) { stage<1, 13 + K.v>(c, vm, sy, 0, 0); });
        at<G, 8, 1, 2 * QC - 7>(LAMBDA(K) { ldB<K.v>(c, vm, Bw); });
        at<G, 2 * QC + 1, 1, 11>(LAMBDA(K) { a_stage<0, K.v>(c, vm, ain); });
      }, LAMBDA(S) { constexpr int s = S.v;
        if (s == QP1) a_poll(c, vm, pl, fl, lo16(c.daa));
        if (s == QP2) poll_again(c, vm, pl);
        if (s == QC) { TS(1, 1); poll_check<true>(c, vm, pl); TS(1, 2); }
        if (s == 17) pre_q<1>(c, e);
      });
      TS(1, 3);
      sfor<39 - 2 * QC, 11>(LAMBDA(K) { a_stage<0, K.v>(c, vm, ain); });
      epi_q(c, vm, e, acc); mst = vm.n; TS(1, 4); }
    asm volatile("; MARK AX");
    AIn ain2;
    constexpr int BWR = 32 - 2 * QC;
    TS(2, 0);
    attn<0>(c, vm, ain, LAMBDA(Gp) {
      at<G, 0, 1, BWR>(LAMBDA(K) { ldB<25 - BWR + K.v>(c, vm, Bw); });
      at<G, BWR, 1, 6 + 3 * AC - BWR>(LAMBDA(K) { ldB<K.v>(c, vm, Bg); });
      at<G, 6 + 3 * AC, 1, 11>(LAMBDA(K) { a_stage<1, K.v>(c, vm, ain2); });
    }, LAMBDA(S) { constexpr int s = S.v;
      if (s == 0) publish(c, vm, mst, fl + 2);
      if (s == AP1) a_poll(c, vm, pl, fl, hi16(c.daa));
      if (s == AP2) poll_again(c, vm, pl);
      if (s == AC) { TS(2, 1); poll_check<true>(c, vm, pl); TS(2, 2); }
      if (s == 8) { sfor<22 - 3 * AC, 11>(LAMBDA(K) { a_stage<1, K.v>(c, vm, ain2); }); TS(2, 3); }
    });
    mst = vm.n; TS(2, 4);
    asm volatile("; MARK AY");
    AW aw;
    TS(3, 0);
    attn<1>(c, vm, ain2, LAMBDA(Gp) {
      at<G, 0, 1, 6 + 3 * AC>(LAMBDA(K) { if constexpr (6 + 3 * AC - BWR + K.v < 38) ldB<6 + 3 * AC - BWR + K.v>(c, vm, Bg); });
      at<G, 6 + 3 * AC, 1, 10>(LAMBDA(K) { ld_w<0, K.v>(c, vm, aw); });
    }, LAMBDA(S) { constexpr int s = S.v;
      if (s == 0) publish(c, vm, mst, fl + 3);
      if (s == AP1) poll_issue(c, vm, pl, lo16(c.doh), fl + 3);
      if (s == AP2) poll_again(c, vm, pl);
      if (s == AC) { TS(3, 1); poll_check<true>(c, vm, pl); TS(3, 2); }
      if (s == 8) { sfor<22 - 3 * AC, 10>(LAMBDA(K) { ld_w<0, K.v>(c, vm, aw); }); TS(3, 3); }
    });
    mst = vm.n; TS(3, 4);
    asm volatile("; MARK WX");
    constexpr int BG0 = 12 + 6 * AC - BWR < 38 ? 12 + 6 * AC - BWR : 38;
    static_assert(44 - BG0 <= 13, "");
    { Red r;
      TS(4, 0);
      gemm_reg<0, 5>(c, vm, Bw, aw, LAMBDA(Gp) { at<G, 0, 1, 12>(LAMBDA(K) { ldB<BG0 + K.v>(c, vm, Bg); }); }, LAMBDA(S) { if (S.v == 1) red_pre<0>(c, r, xr[0], A_XB); if (S.v == 4) publish(c, vm, mst, fl + 4); });
      TS(4, 1);
      BARL();
      poll_issue(c, vm, pl, lo16(c.doh), fl + 4);
      TS(4, 2);
      red_out<0>(c, vm, r, xr[0], nullptr); mst = vm.n;
      TS(4, 3);
      poll_again(c, vm, pl);
      TS(4, 4); }
    asm volatile("; MARK WY");
    if (l == 0 && threadIdx.x < 80) {
      const u32 t = threadIdx.x, o = OFF_CS + (t < 32 ? cgq * 512 + t * 16 : 7680 + c.cg * 768 + (t - 32) * 16);
      f32x4 v[20];
#pragma unroll
      for (int j = 0; j < 20; ++j) v[j] = __builtin_bit_cast(f32x4, __builtin_amdgcn_raw_buffer_load_b128(c.rs, o, j * CS_ROW, 16));
#pragma unroll
      for (int j = 1; j < 20; ++j) v[0] += v[j];
      *LP(f32x4, c.lb + L_CS + t * 16) = v[0];
    }
    { Red r;
      poll_check<true>(c, vm, pl);
      TS(5, 0);
      sfor<0, 10>(LAMBDA(K) { ld_w<1, K.v>(c, vm, aw); });
      loadB<BG0 + 12, BG0 + 16>(c, vm, Bg);
      red_pre<1>(c, r, xr[1], A_XB);

      publish(c, vm, mst, fl + 5);
      gemm_reg<1, 5>(c, vm, Bw, aw, LAMBDA(Gp) {
        at<G, 0, 1, 44 - BG0>(LAMBDA(K) { ldB<BG0 + 16 + K.v>(c, vm, Bg); });
      }, LAMBDA(S) { constexpr int s = S.v; if (s == 1) poll_issue(c, vm, pl, c.dxx, fl + 5); if (s == 4) poll_again(c, vm, pl); });
      TS(5, 1); poll_check<true>(c, vm, pl); TS(5, 2);
      stages<0, 0, 9>(c, vm, sx, A_XB, c.tq);
      BARL();
      TS(5, 3);
      red_out<1>(c, vm, r, xr[1], nullptr); mst = vm.n;
      stages<0, 9, 13>(c, vm, sx, 0, 0);

      TS(5, 4); }
    asm volatile("; MARK GX");
    { f32x4 acc[3][4]; EpG e;
      TS(6, 0);
      gemm_lds<0, 3, 0x0B, 1>(c, vm, sx, true, mst, fl + 6, acc, Bg, LAMBDA(Gp) {
        at<G, 0, 1, 8>(LAMBDA(K) { stage<0, 13 + K.v>(c, vm, sx, 0, 0); });
        at<G, 8, 1, 25>(LAMBDA(K) { ldB<K.v>(c, vm, Bd); });
        at<G, 37, 1, 21>(LAMBDA(K) { stage<1, K.v>(c, vm, sy, A_XB, c.tq); });
      }, LAMBDA(S) { constexpr int s = S.v;
        if (s == 7) poll_issue(c, vm, pl, c.dxx, fl + 6);
        if (s == 10) poll_again(c, vm, pl);
        if (s == 12) { TS(6, 1); poll_check<true>(c, vm, pl); TS(6, 2); }
        if (s == 18) pre_g<0>(c, e);
      });
      TS(6, 3);
      epi_g(c, vm, e, acc); mst = vm.n; TS(6, 4); }
    asm volatile("; MARK GY");
    AD<true> ad;
    { f32x4 acc[3][4]; EpG e;
      TS(7, 0);
      gemm_lds<1, 3, 0x13, 1>(c, vm, sy, true, mst, fl + 7, acc, Bg, LAMBDA(Gp) {
        at<G, 3, 1, 31>(LAMBDA(K) { ldB<25 + K.v>(c, vm, Bd); });
        at<G, 37, 1, 23>(LAMBDA(K) { ld_d<0, K.v>(c, vm, ad); });
      }, LAMBDA(S) { constexpr int s = S.v;
        if (s == 7) poll_issue(c, vm, pl, hi16(c.doh), fl + 7);
        if (s == 10) poll_again(c, vm, pl);
        if (s == 12) { TS(7, 1); poll_check<true>(c, vm, pl); TS(7, 2); }
        if (s == 18) pre_g<1>(c, e);
      });
      TS(7, 3);
      epi_g(c, vm, e, acc); mst = vm.n; loadB<56, 60>(c, vm, Bd); TS(7, 4); }
    asm volatile("; MARK DX");
    if (l) Bq.base = OOR;
    AD<false> ad2;
    { Red r;
      TS(8, 0);
      gemm_reg<0, 12>(c, vm, Bd, ad, LAMBDA(Gp) {
        at<G, 0, 1, 1>(LAMBDA(K) { ld_d<0, 23>(c, vm, ad); });
        at<G, 1, 1, 10>(LAMBDA(K) { ldB<K.v>(c, vm, Bq); });
      }, LAMBDA(S) { constexpr int s = S.v;
        if (s == DPUB) publish(c, vm, mst, fl + 8);
        if (s == DP1) poll_issue(c, vm, pl, hi16(c.doh), fl + 8);
        if (s == 9) red_pre<0>(c, r, xr[0], LB + A_XA);
        if (s == DP2) poll_again(c, vm, pl);
      });
      TS(8, 1);
      poll_check<true>(c, vm, pl);
      TS(8, 2);
      sfor<0, 6>(LAMBDA(K) { ld_d<1, K.v>(c, vm, ad2); });
      BARL();
      TS(8, 3);
      red_out<0>(c, vm, r, xr[0], l ? p.out : nullptr); mst = vm.n;
      sfor<6, 12>(LAMBDA(K) { ld_d<1, K.v>(c, vm, ad2); });

      TS(8, 4); }
    asm volatile("; MARK DY");
    { Red r;
      TS(9, 0);
      gemm_reg<1, 12>(c, vm, Bd, ad2, LAMBDA(Gp) {
        at<G, 0, 1, 12>(LAMBDA(K) { ld_d<1, 12 + K.v>(c, vm, ad2); });
        at<G, 17, 1, 11>(LAMBDA(K) { ldB<10 + K.v>(c, vm, Bq); });
      }, LAMBDA(S) { constexpr int s = S.v;
        if (s == DPUB) publish(c, vm, mst, fl + 9);
        if (s == DP1) poll_issue(c, vm, pl, c.dxx, l ? fl : fl + 9);
        if (s == 9) red_pre<1>(c, r, xr[1], LB + A_XA);
        if (s == 11) { loadB<21, 24>(c, vm, Bq); poll_again(c, vm, pl); }
      });
      TS(9, 1);
      poll_check<true>(c, vm, pl);
      TS(9, 2);
      stages<0, 0, 9>(c, vm, sx, LB + A_XA, c.tq);
      BARL();
      TS(9, 3);
      red_out<1>(c, vm, r, xr[1], l ? p.out : nullptr); mst = vm.n;
      stages<0, 9, 13>(c, vm, sx, 0, 0);

      TS(9, 4); }
    if (vm.n - sx.mg[0] < CARRY0 || vm.n - sx.mg[1] < CARRY1 || vm.n - sx.mg[2] < CARRY2 || vm.n - mst < CARRYST || vm.done < Bq.m[9] || Bq.m[23] > sx.mg[0]) carry_count_too_large();
    c.ra = __builtin_amdgcn_make_buffer_rsrc(p.ws + OFF_ACT + LB, 0, (int)LB, 0x00020000);
    sx.src -= LB; fl += 10; dq = c.dxx;
    prof<true>(vm); prof<true>(vm);
    asm volatile("; MARK END");
  }
  if (blockIdx.x == 0 && threadIdx.x == 0) __hip_atomic_store((GAS u32*)(p.ws + OFF_EPOCH), ep + 64, RLX_AGENT);
  asm volatile("s_waitcnt vmcnt(0)" ::: "memory");
}

extern "C" void kernel_launch(void* const* d_in, const int* in_sizes, int n_in, void* d_out, int out_size, void* d_ws, size_t ws_size, hipStream_t stream) {
  Params p;
  p.x = (const u16*)d_in[0]; p.wqkv = (const u16*)d_in[1]; p.wo = (const u16*)d_in[2];
  p.wg = (const u16*)d_in[4]; p.wu = (const u16*)d_in[5]; p.wd = (const u16*)d_in[6];
  p.out = (u16*)d_out; p.ws = (unsigned char*)d_ws;
  (void)hipFuncSetAttribute((const void*)mega_kernel, hipFuncAttributeMaxDynamicSharedMemorySize, LDS_BYTES);
  mega_kernel<<<256, 256, LDS_BYTES, stream>>>(p);
}
```

```cpp
#include <hip/hip_runtime.h>
#include <stdint.h>

typedef unsigned short u16;
typedef unsigned u32;
typedef u32 u32x2 __attribute__((ext_vector_type(2)));
typedef u32 u32x4 __attribute__((ext_vector_type(4)));
typedef float f32x2 __attribute__((ext_vector_type(2)));
typedef float f32x4 __attribute__((ext_vector_type(4)));
typedef float f32x16 __attribute__((ext_vector_type(16)));
typedef short bf16x8 __attribute__((ext_vector_type(8)));
typedef short s16x4 __attribute__((ext_vector_type(4)));
typedef __bf16 bf16x2_t __attribute__((ext_vector_type(2)));
#define GAS __attribute__((address_space(1)))
#define LAS __attribute__((address_space(3)))
#define DI __device__ __forceinline__
#define RLX_AGENT __ATOMIC_RELAXED, __HIP_MEMORY_SCOPE_AGENT

constexpr int S = 2048, D = 640, F = 1536;
constexpr u32 KB = 1024;
constexpr u32 OFF_FLAGS = 0;
constexpr u32 OFF_TMO = 32 * KB;
constexpr u32 OFF_EPOCH = 33 * KB;
constexpr u32 OFF_CS = 64 * KB;
constexpr u32 CS_ROW = 4992 * 4;
constexpr u32 OFF_BQ = 512 * KB;
constexpr u32 OFF_BW = OFF_BQ + 2400 * KB;
constexpr u32 OFF_BG = OFF_BW + 800 * KB;
constexpr u32 OFF_BD = OFF_BG + 3840 * KB;
constexpr u32 A_XA = 0, A_SA = 2560 * KB, A_QKV = A_SA + 128 * KB, A_OF = A_QKV + 7680 * KB, A_XB = A_OF + 2560 * KB,
              A_SB = A_XB + 2560 * KB, A_HF = A_SB + 128 * KB, LB = A_HF + 6144 * KB;
constexpr u32 OFF_ACT = OFF_BD + 1920 * KB;
constexpr u32 OFF_DBG = OFF_ACT + 2 * LB;
constexpr u32 OOR = 0x80000000u;
constexpr u32 L_Y = 80 * KB, L_HX = 16 * KB, L_HY = 80 * KB, L_MISC = 144 * KB;
constexpr u32 TP = 3584;
constexpr u32 L_STAT = L_MISC;
constexpr u32 L_CS = L_STAT + 1024;
constexpr u32 L_MASK = L_CS + 1280;
constexpr u32 L_DBG = L_MASK + 6 * KB;
constexpr u32 LDS_BYTES = L_DBG + 1024;
static_assert(LDS_BYTES <= 160 * KB, "");

struct Params { const u16* x; const u16* wqkv; const u16* wo; const u16* wg; const u16* wu; const u16* wd; u16* out; unsigned char* ws; };

DI float bf_lo(u32 v) { return __uint_as_float(v << 16); }
DI float bf_hi(u32 v) { return __uint_as_float(v & 0xffff0000u); }
DI u32 pk2(float a, float b) { f32x2 f = {a, b}; bf16x2_t h = __builtin_convertvector(f, bf16x2_t); return __builtin_bit_cast(u32, h); }
DI float rbf(float a) { return bf_lo(pk2(a, a)); }

DI u32x2 trread(u32 a) { return __builtin_bit_cast(u32x2, __builtin_amdgcn_ds_read_tr16_b64_v4i16((LAS s16x4*)(size_t)a)); }

typedef __amdgpu_buffer_rsrc_t rsrc_t;
#define SB() __builtin_amdgcn_sched_barrier(0)
template <int I> struct IC { static constexpr int v = I; };
template <int B, int E, class Fn> DI void sfor(Fn&& f) { if constexpr (B < E) { f(IC<B>{}); sfor<B + 1, E>(f); } }
#define LAMBDA(S) [&](auto S) __attribute__((always_inline))
template <class T> DI void pin(T& d) { asm volatile("" : "+v"(d)); }
DI u32 sop(u32 v) { asm volatile("" : "+s"(v)); return v; }
DI u32 vop(u32 v) { asm volatile("" : "+v"(v)); return v; }
struct Ctx {
  rsrc_t rs, ra;
  u32 lb; unsigned char* ws;
  u32 l16, w, bid;
  u32 sq, sw, cg;
  u32 tq;
  u32 bof, bhf;
  u32 m0x, m0a;
  u32 foff;
  u32 dxx, doh, daa;
  u32 rw, rpa, rpt, rvo, rvt, rvs;
};
template <int G, int START, int STRIDE, int N, class Fn> DI void at(Fn&& f) {
  if constexpr (G >= START && (G - START) % STRIDE == 0 && (G - START) / STRIDE < N) f(IC<(G - START) / STRIDE>{});
}
#define LP(T, a) ((LAS T*)(size_t)(a))

struct VM { int n, done;
};
template <bool W, bool W2 = W> DI void prof(VM& vm) {
}
DI void need(VM& vm, int mark) {
  if (mark > vm.done) { const int k = vm.n - mark; if (k < 63) asm volatile("s_waitcnt vmcnt(%0)" :: "i"(k) : "memory"); vm.done = mark; }
}
template <int SLOT, bool SC1, int OFF> DI int ld16(rsrc_t rs, VM& vm, u32x4& d, u32 voff, u32 soff) {
  if constexpr (SLOT >= 0 && SC1) asm volatile("buffer_load_dwordx4 a[%c0:%c1], %2, %3, %4 offen offset:%5 sc1" :: "i"(4 * SLOT), "i"(4 * SLOT + 3), "v"(voff), "s"(rs), "s"(soff), "i"(OFF) : "memory");
  else if constexpr (SLOT >= 0) asm volatile("buffer_load_dwordx4 a[%c0:%c1], %2, %3, %4 offen offset:%5" :: "i"(4 * SLOT), "i"(4 * SLOT + 3), "v"(voff), "s"(rs), "s"(soff), "i"(OFF) : "memory");
  else if constexpr (SC1) asm volatile("buffer_load_dwordx4 %0, %1, %2, %3 offen offset:%4 sc1" : "=v"(d) : "v"(voff), "s"(rs), "s"(soff), "i"(OFF) : "memory");
  else asm volatile("buffer_load_dwordx4 %0, %1, %2, %3 offen offset:%4" : "=v"(d) : "v"(voff), "s"(rs), "s"(soff), "i"(OFF) : "memory");
  return ++vm.n;
}
template <int OFF> DI int ld8c(rsrc_t rs, VM& vm, u32x2& d, u32 voff, u32 soff) {
  asm volatile("buffer_load_dwordx2 %0, %1, %2, %3 offen offset:%4 sc1" : "=v"(d) : "v"(voff), "s"(rs), "s"(soff), "i"(OFF) : "memory");
  return ++vm.n;
}
template <bool FIRST, int SA, int SBB> DI void mma(f32x4& d, const u32x4& a, const u32x4& b) {
#define MMA_(D_, C_) \
  if constexpr (SA >= 0 && SBB >= 0) asm volatile("v_mfma_f32_16x16x32_bf16 %0, a[%c1:%c2], a[%c3:%c4], " C_ : D_(d) : "i"(4 * SA), "i"(4 * SA + 3), "i"(4 * SBB), "i"(4 * SBB + 3)); \
  else if constexpr (SA >= 0) asm volatile("v_mfma_f32_16x16x32_bf16 %0, a[%c1:%c2], %3, " C_ : D_(d) : "i"(4 * SA), "i"(4 * SA + 3), "v"(b)); \
  else if constexpr (SBB >= 0) asm volatile("v_mfma_f32_16x16x32_bf16 %0, %3, a[%c1:%c2], " C_ : D_(d) : "i"(4 * SBB), "i"(4 * SBB + 3), "v"(a)); \
  else asm volatile("v_mfma_f32_16x16x32_bf16 %0, %1, %2, " C_ : D_(d) : "v"(a), "v"(b));
  if constexpr (FIRST) { MMA_("=&v", "0") } else { MMA_("+v", "%0") }
#undef MMA_
}
DI void st16c(const Ctx& c, VM& vm, u32 voff, u32x4 v) { ++vm.n; __builtin_amdgcn_raw_buffer_store_b128(v, c.ra, voff, 0, 16); }
DI void st8c(const Ctx& c, VM& vm, u32 voff, u32x2 v) { ++vm.n; __builtin_amdgcn_raw_buffer_store_b64(v, c.ra, voff, 0, 16); }
DI void st4c(const Ctx& c, VM& vm, u32 voff, u32 v) { ++vm.n; __builtin_amdgcn_raw_buffer_store_b32(v, c.ra, voff, 0, 16); }
template <u32 LOFF, u32 SOFF> DI void glds_head(const Ctx& c, VM& vm, u32& cur, u32 m0b, u32 sb, u32 voff) {
  ++vm.n;
  asm volatile("s_add_u32 m0, %1, %2\n\ts_add_u32 %0, %3, %4\n\tbuffer_load_dwordx4 %5, %6, %0 offen sc1 lds"
               : "=&s"(cur) : "s"(m0b), "i"(LOFF), "s"(sb), "i"(SOFF), "v"(voff), "s"(c.ra) : "memory", "scc");
}
template <int OFF> DI void glds_next(const Ctx& c, VM& vm, u32 cur, u32 voff) {
  ++vm.n;
  asm volatile("buffer_load_dwordx4 %0, %1, %2 offen offset:%3 sc1 lds" :: "v"(voff), "s"(c.ra), "s"(cur), "i"(OFF) : "memory");
}
#define BARL() asm volatile("s_waitcnt lgkmcnt(0)\n\ts_barrier" ::: "memory")
#define BAR0() asm volatile("s_barrier" ::: "memory")
extern "C" __device__ void carry_count_too_large() __attribute__((error("carry count too large")));

#define STAMP(k) do {} while (0)
#define TS(t, k) STAMP(1 + ((10 * l + (t)) * 5 + (k)))

struct Poll { u32 off, need; int mark, mark2; };
DI void poll_issue(const Ctx& c, VM& vm, Poll& p, u32 off, u32 need) {
  p.off = off; p.need = need;
  asm volatile("buffer_load_dword a252, %0, %1, 0 offen sc1" :: "v"(p.off), "s"(c.rs) : "memory");
  p.mark = ++vm.n;
}
DI void poll_again(const Ctx& c, VM& vm, Poll& p) {
  asm volatile("buffer_load_dword a253, %0, %1, 0 offen sc1" :: "v"(p.off), "s"(c.rs) : "memory");
  p.mark2 = ++vm.n;
}
template <bool TWO> DI void poll_check(const Ctx& c, VM& vm, Poll& p) {
  need(vm, p.mark);
  u32 v; asm volatile("v_accvgpr_read_b32 %0, a252" : "=v"(v));
  if (__builtin_expect(!__all(v - p.need < 32u), 0)) {
    asm volatile("buffer_load_dword a254, %1, %2, 0 offen sc1\n\ts_waitcnt vmcnt(%3) ; SECOND\n\tv_accvgpr_read_b32 %0, a253" : "=v"(v) : "v"(p.off), "s"(c.rs), "i"(vm.n - p.mark2 + 1) : "memory");
    bool ok = __all(v - p.need < 32u);
#pragma unroll 1
    for (u32 spins = 0; !ok; ++spins) {
      asm volatile("buffer_load_dword a255, %1, %2, 0 offen sc1\n\ts_waitcnt vmcnt(1)\n\tv_accvgpr_read_b32 %0, a254" : "=v"(v) : "v"(p.off), "s"(c.rs) : "memory");
      if (__all(v - p.need < 32u)) break;
      asm volatile("buffer_load_dword a254, %1, %2, 0 offen sc1\n\ts_waitcnt vmcnt(1)\n\tv_accvgpr_read_b32 %0, a255" : "=v"(v) : "v"(p.off), "s"(c.rs) : "memory");
      ok = __all(v - p.need < 32u);
      if (spins > (1u << 16)) { __hip_atomic_store((GAS u32*)(c.ws + OFF_TMO), 1u, RLX_AGENT); break; }
    }
  }
  asm volatile("" ::: "memory");
}
DI void flag_store(const Ctx& c, VM& vm, u32 val) { ++vm.n; __builtin_amdgcn_raw_buffer_store_b32(val, c.rs, c.foff, 0, 16); }
DI void publish(const Ctx& c, VM& vm, int mark, u32 val) { need(vm, mark); BAR0(); flag_store(c, vm, val); }
DI u32 lo16(u32 v) { return vop(v) & 0xFFFF; }
DI u32 hi16(u32 v) { return vop(v) >> 16; }

template <int ID> constexpr int slotB(int j) { return ID == 0 ? (j < 10 ? -1 : 15 + j) : ID == 1 ? j : ID == 2 ? (j < 38 ? 25 + j : -1) : j; }
template <int ID_, int N> struct BF { static constexpr int ID = ID_; u32x4 f[N]; int m[N]; u32 base, cur; };
template <int J, int ID, int N> DI void ldB(const Ctx& c, VM& vm, BF<ID, N>& B) {
  if constexpr ((J & 3) == 0) B.cur = sop(B.base) + J * KB;
  B.m[J] = ld16<slotB<ID>(J), true, (J & 3) * 1024>(c.rs, vm, B.f[J], c.l16, B.cur);
}
template <int LO, int HI, class BT> DI void loadB(const Ctx& c, VM& vm, BT& B) { sfor<LO, HI>(LAMBDA(J) { ldB<J.v>(c, vm, B); }); }
template <int CTRL> DI float dpp_add(float v) { return v + __int_as_float(__builtin_amdgcn_update_dpp(0, __float_as_int(v), CTRL, 0xF, 0xF, true)); }

#define STAT0 4
template <int HALF> constexpr u32 img_off(int g) { return HALF == 0 ? g * 16 * KB : (g < 4 ? L_Y + g * 16 * KB : 0); }
struct Stage { u32x4 sv; u32 src, cur; int mg[5]; };
template <int HALF, int K> DI void stage(const Ctx& c, VM& vm, Stage& st, u32 xoff, u32 tf) {
  if constexpr (K == 0) {
    const u32 t = sop(tf), l = vop(c.l16);
    st.src = xoff + HALF * 64 * 20480 + t * 20480;
    ld16<-1, true, 0>(c.ra, vm, st.sv, ((l << 2) & 0x3C0) + ((l >> 4) & 0x30), xoff + (A_SA - A_XA) + HALF * 65536 + t * 1024);
  } else {
    constexpr int g = (K - 1) >> 2, j = (K - 1) & 3;
    if constexpr (j == 0) glds_head<img_off<HALF>(g), g * 4 * KB>(c, vm, st.cur, c.m0x, st.src, c.l16); else glds_next<j * 1024>(c, vm, st.cur, c.l16);
    st.mg[g] = vm.n;
  }
}
template <int HALF, int LO, int HI> DI void stages(const Ctx& c, VM& vm, Stage& st, u32 xoff, u32 tf) { sfor<LO, HI>(LAMBDA(K) { stage<HALF, K.v>(c, vm, st, xoff, tf); }); }
template <int HALF, int NF, int SYNC, int PG, class BT, class HL, class HA> DI void gemm_lds(const Ctx& c, VM& vm, Stage& st, bool use_stats, int mst, u32 fv, f32x4 (&acc)[NF][4], BT& B, HL hl, HA ha) {
  const u32 b0 = vop(c.lb + img_off<HALF>(0) + c.l16), b1 = vop(c.lb + img_off<HALF>(4) + c.l16);
  auto rd = [&](int s, int tf) __attribute__((always_inline)) { return *LP(const u32x4, (s < 16 ? b0 + (s >> 2) * 16 * KB : b1) + tf * 4 * KB + (s & 3) * KB); };
  prof<true>(vm);
  auto cover = [](int g) constexpr { int e = g; while (e < 4 && !((SYNC >> (e + 1)) & 1)) ++e; return e; };
  need(vm, st.mg[cover(0)]); BAR0(); pin(st.sv);
  u32x4 a[2][4]; float s1, s2;
#pragma unroll
  for (int tf = 0; tf < 4; ++tf) a[0][tf] = rd(0, tf);
  SB();
  sfor<0, 20>(LAMBDA(S) {
    constexpr int s = S.v;
    if constexpr ((s & 3) == 3 && s < 19 && ((SYNC >> ((s + 1) >> 2)) & 1)) {
      need(vm, st.mg[cover((s + 1) >> 2)]); if ((s + 1) >> 2 == PG) need(vm, mst);
      BAR0();
    }
    need(vm, B.m[s * NF + NF - 1]);
    __builtin_amdgcn_s_waitcnt(0xC07F);
    prof<false>(vm);
    SB();
    sfor<0, 4 * NF>(LAMBDA(J) {
      constexpr int nf = J.v >> 2, tf = J.v & 3;
      mma<s == 0, slotB<BT::ID>(s * NF + nf), -1>(acc[nf][tf], B.f[s * NF + nf], a[s & 1][tf]);
      if constexpr (s < 19 && nf == 0) { a[(s + 1) & 1][tf] = rd(s + 1, tf); SB(); }
      if constexpr (J.v >= 4 && (J.v - 4) % NF == 0) { SB(); hl(IC<s * NF + (J.v - 4) / NF>{}); SB(); }
      if constexpr (J.v == 0 && s == 4 * PG - 1) { flag_store(c, vm, fv); SB(); }
      if constexpr (J.v == 5) { SB(); ha(S); SB(); }
      if constexpr (J.v == 7 - (NF == 3) && s >= STAT0 && s < STAT0 + 6) {
        SB();
        if constexpr (s == STAT0) { s1 = __uint_as_float(st.sv.x) + __uint_as_float(st.sv.z); s2 = __uint_as_float(st.sv.y) + __uint_as_float(st.sv.w); pin(s1); pin(s2); }
        if constexpr (s == STAT0 + 1) { auto t = __builtin_amdgcn_permlane16_swap(__float_as_uint(s1), __float_as_uint(s2), false, false); s1 = __uint_as_float(t[0]) + __uint_as_float(t[1]); s2 = s1; pin(s1); pin(s2); }
        if constexpr (s == STAT0 + 2) { auto t = __builtin_amdgcn_permlane32_swap(__float_as_uint(s1), __float_as_uint(s2), false, false); s1 = __uint_as_float(t[0]) + __uint_as_float(t[1]); s2 = s1; pin(s1); pin(s2); }
        if constexpr (s == STAT0 + 3) { auto t = __builtin_amdgcn_permlane16_swap(__float_as_uint(s1), __float_as_uint(s2), false, false); s1 = __uint_as_float(t[0]) * (-1.0f / D); s2 = __uint_as_float(t[1]) * (1.0f / D); pin(s1); pin(s2); }
        if constexpr (s == STAT0 + 4) { s2 = __builtin_fmaxf(s2 - s1 * s1, 0.f) + 1e-5f; pin(s2); }
        if constexpr (s == STAT0 + 5) { float rstd = __builtin_amdgcn_rsqf(s2); if (!use_stats) { s1 = 0.f; rstd = 1.f; }
          *LP(f32x2, c.lb + L_STAT + HALF * 512 + c.w * 128 + ((c.l16 >> 1) & 0x78)) = f32x2{s1, rstd}; }
        SB();
      }
    });
    SB();
  });
  asm volatile("s_nop 7");
  prof<true>(vm);
  SB();
}
struct EpQ { f32x4 cs[2]; f32x2 st[4]; u32 vo; float qs; };
struct EpG { f32x4 cs[3]; f32x2 st[4]; u32 vo; };
template <int HALF> DI void pre_q(const Ctx& c, EpQ& e) {
  const u32 l = vop(c.l16), cg = sop(c.cg);
  const u32 pc = c.lb + L_CS + c.w * 128 + ((l >> 3) & 0x60), ps = c.lb + L_STAT + HALF * 512 + ((l >> 1) & 0x78);
  e.cs[0] = *LP(const f32x4, pc); e.cs[1] = *LP(const f32x4, pc + 16);
#pragma unroll
  for (int tf = 0; tf < 4; ++tf) e.st[tf] = *LP(const f32x2, ps + tf * 128);
  e.qs = cg < 5 ? 0.22360679774997896f * 1.4426950408889634f : 1.0f;
  const u32 base = cg < 15 ? A_QKV + ((16 * cg + 4 * c.w) * 2048 + c.sq + HALF * 1024) * 16 : OOR;
  e.vo = base + ((l >> 8) << 15) + (l & 0xF0);
}
DI void epi_q(const Ctx& c, VM& vm, const EpQ& e, const f32x4 (&acc)[2][4]) {
#pragma unroll
  for (int tf = 0; tf < 4; ++tf) {
    const float rs = e.st[tf].y * e.qs;
    const f32x4 a = (acc[0][tf] + e.st[tf].x * e.cs[0]) * rs, b = (acc[1][tf] + e.st[tf].x * e.cs[1]) * rs;
    st16c(c, vm, e.vo + tf * 256, u32x4{pk2(a.x, a.y), pk2(a.z, a.w), pk2(b.x, b.y), pk2(b.z, b.w)});
  }
}
template <int HALF> DI void pre_g(const Ctx& c, EpG& e) {
  const u32 l = vop(c.l16), cg = sop(c.cg);
  const u32 pc = c.lb + L_CS + 512 + c.w * 192 + ((l >> 4) & 0x30), ps = c.lb + L_STAT + HALF * 512 + ((l >> 1) & 0x78);
#pragma unroll
  for (int nf = 0; nf < 3; ++nf) e.cs[nf] = *LP(const f32x4, pc + nf * 64);
#pragma unroll
  for (int tf = 0; tf < 4; ++tf) e.st[tf] = *LP(const f32x2, ps + tf * 128);
  e.vo = A_HF + (((c.sq + HALF * 1024) >> 4) * 192 + 12 * cg + 3 * c.w) * 256 + (l >> 8) * (192 * 256) + (l & 0xF0);
}
DI void epi_g(const Ctx& c, VM& vm, const EpG& e, const f32x4 (&acc)[3][4]) {
#pragma unroll
  for (int nf = 0; nf < 3; ++nf) {
    u32 R[4];
#pragma unroll
    for (int tf = 0; tf < 4; ++tf) { const f32x4 a = acc[nf][tf] + e.st[tf].x * e.cs[nf]; const float r2 = e.st[tf].y * e.st[tf].y; R[tf] = pk2(a.x * r2 * a.z, a.y * r2 * a.w); }
    { auto t = __builtin_amdgcn_permlane32_swap(R[0], R[2], false, false); R[0] = t[0]; R[2] = t[1]; }
    { auto t = __builtin_amdgcn_permlane32_swap(R[1], R[3], false, false); R[1] = t[0]; R[3] = t[1]; }
    { auto t = __builtin_amdgcn_permlane16_swap(R[0], R[1], false, false); R[0] = t[0]; R[1] = t[1]; }
    { auto t = __builtin_amdgcn_permlane16_swap(R[2], R[3], false, false); R[2] = t[0]; R[3] = t[1]; }
    st16c(c, vm, e.vo + nf * 256, u32x4{R[0], R[1], R[2], R[3]});
  }
}

struct AW { u32x4 f[2][5]; u32 s0; int m[2][5]; static constexpr int slot(int, int) { return -1; } };
template <bool X> struct AD { u32x4 f[2][12]; u32 cur; int m[2][12]; static constexpr int slot(int tf, int s) { return X && tf == 1 && s >= 9 ? 51 + s : -1; } };
template <int HALF, int K> DI void ld_w(const Ctx& c, VM& vm, AW& a) {
  constexpr int tf = K / 5, j = K % 5;
  if constexpr (j == 0 || j == 4) a.s0 = sop(c.bof) + (HALF * 64 + tf) * 20480 + j * KB;
  a.m[tf][j] = ld16<-1, true, (j & 3) * 1024>(c.ra, vm, a.f[tf][j], c.l16, a.s0);
}
template <int NS, int HALF> constexpr int kord(int p) { return NS == 12 && HALF == 1 ? (p + 5) % 12 : p; }
template <int HALF, int K, bool X> DI void ld_d(const Ctx& c, VM& vm, AD<X>& a) {
  constexpr int tf = K / 12, s = kord<12, HALF>(K % 12);
  if constexpr (K % 12 == 0 || (s & 3) == 0) a.cur = sop(c.bhf) + (HALF * 64 + tf) * 192 * 256 + (s & ~3) * KB;
  a.m[tf][s] = ld16<AD<X>::slot(tf, s), true, (s & 3) * 1024>(c.ra, vm, a.f[tf][s], c.l16, a.cur);
}
template <int TF, int S> DI u32x4 get_a(VM& vm, AW& a) { need(vm, a.m[TF][S]); return a.f[TF][S]; }
template <int TF, int S, bool X> DI u32x4 get_a(VM& vm, AD<X>& a) { need(vm, a.m[TF][S]); if constexpr (AD<X>::slot(TF, S) < 0) return a.f[TF][S]; else return u32x4{}; }
template <int HALF, int NS, class BT, class AT, class HL, class HA> DI void gemm_reg(const Ctx& c, VM& vm, BT& B, AT& a, HL hl, HA ha) {
  f32x4 acc[10];
  const u32 p0 = vop(c.rw) + (HALF ? L_HY - L_HX : 0), p1 = p0 ^ 16;
  auto wr = [&](int j) __attribute__((always_inline)) { *LP(f32x4, (((j % 5) & 1) ? p1 : p0) + j * KB) = acc[j]; SB(); };
  sfor<0, 2 * NS>(LAMBDA(P) {
    constexpr int tf = P.v / NS, s = kord<NS, HALF>(P.v % NS);
    if constexpr ((P.v & 1) == 0) prof<true, false>(vm);
    need(vm, B.m[s * 5 + 4]);
    const u32x4 o = get_a<tf, s>(vm, a);
    SB();
    sfor<0, 5>(LAMBDA(J) { constexpr int nf = J.v, g = P.v * 5 + nf;
      mma<P.v % NS == 0, slotB<BT::ID>(s * 5 + nf), AT::slot(tf, s)>(acc[tf * 5 + nf], B.f[s * 5 + nf], o);
      if constexpr (g % 4 == 1) { SB(); hl(IC<g / 4>{}); SB(); }
      if constexpr (g % 10 == 4) { SB(); ha(IC<g / 10>{}); SB(); }
      if constexpr (g >= NS * 5 + 3 && g < NS * 5 + 23 && (g - NS * 5 - 3) % 4 == 0) { SB(); wr((g - NS * 5 - 3) / 4); } });
    SB();
  });
  prof<true>(vm);
  asm volatile("s_nop 3"); SB();
  sfor<5, 10>(LAMBDA(J) { wr(J.v); });
  prof<true>(vm);
}
struct XR { u32x4 m; u32 t; };
struct Red { f32x4 a, b; f32x2 t; u32 pa, pb, pt, vo, vt, vs; };
template <int HALF> DI void red_pre(const Ctx& c, Red& r, const XR& xr, u32 xoff) {
  r.a = f32x4{bf_lo(xr.m.x), bf_hi(xr.m.x), bf_lo(xr.m.y), bf_hi(xr.m.y)}; r.b = f32x4{bf_lo(xr.m.z), bf_hi(xr.m.z), bf_lo(xr.m.w), bf_hi(xr.m.w)}; r.t = f32x2{bf_lo(xr.t), bf_hi(xr.t)};
  r.pa = vop(c.rpa) + (HALF ? L_HY - L_HX : 0); r.pb = r.pa ^ 16; r.pt = vop(c.rpt) + (HALF ? L_HY - L_HX : 0);
  r.vo = vop(c.rvo) + (xoff + HALF * 64 * 20480); r.vt = vop(c.rvt) + (xoff + HALF * 64 * 20480); r.vs = vop(c.rvs) + (xoff + HALF * 65536);
  pin(r.a); pin(r.b); pin(r.t); pin(r.pa); pin(r.pb); pin(r.pt); pin(r.vo); pin(r.vt); pin(r.vs);
}
template <int HALF> DI void red_out(const Ctx& c, VM& vm, Red& r, XR& xr, u16* out) {
  prof<true>(vm);
  f32x4 pa[4], pb[4]; f32x2 pt[4];
#pragma unroll
  for (int ww = 0; ww < 4; ++ww) { pa[ww] = *LP(const f32x4, r.pa + ww * 10 * KB); pb[ww] = *LP(const f32x4, r.pb + ww * 10 * KB); }
#pragma unroll
  for (int ww = 0; ww < 4; ++ww) pt[ww] = *LP(const f32x2, r.pt + ww * 10 * KB);
  SB();
  const f32x4 a = ((r.a + pa[0]) + pa[1]) + (pa[2] + pa[3]), b = ((r.b + pb[0]) + pb[1]) + (pb[2] + pb[3]);
  xr.m = u32x4{pk2(a.x, a.y), pk2(a.z, a.w), pk2(b.x, b.y), pk2(b.z, b.w)};
  st16c(c, vm, r.vo, xr.m);
  const f32x2 t = ((r.t + pt[0]) + pt[1]) + (pt[2] + pt[3]);
  xr.t = pk2(t.x, t.y);
  st4c(c, vm, r.vt, xr.t);
  const f32x2 sp = (a.xy + a.zw) + (b.xy + b.zw) + t;
  const f32x2 qp = a.xy * a.xy + (a.zw * a.zw + (b.xy * b.xy + (b.zw * b.zw + t * t)));
  float s1 = sp.x + sp.y, s2 = qp.x + qp.y;
  s1 = dpp_add<0xB1>(s1); s2 = dpp_add<0xB1>(s2); s1 = dpp_add<0x4E>(s1); s2 = dpp_add<0x4E>(s2); s1 = dpp_add<0x141>(s1); s2 = dpp_add<0x141>(s2);
  st8c(c, vm, r.vs, u32x2{__float_as_uint(s1), __float_as_uint(s2)});
  if (out) {
    const u32 lane = vop(c.l16) >> 4;
    GAS u16* o = (GAS u16*)out + (c.sw + HALF * 1024 + 8 * c.w + (lane >> 3)) * D + 80 * ((c.bid >> 3) & 7);
    *(GAS u32x4*)(o + 8 * (lane & 7)) = xr.m; *(GAS u32*)(o + 64 + 2 * (lane & 7)) = xr.t;
  }
  prof<true>(vm);
}

struct AIn { u32x4 Q0; u32x2 qc; u32 v0, v1, v2, cur; int r0, mark; };
DI u32 wg_qg(u32 RG, u32 cg) { return 2 * (RG >> 2) + (cg >> 3) + 8 * ((RG & 3) * 8 + (cg & 7)); }
DI u32 a_head(u32 bid) { return 4 * (bid & 7) + ((bid >> 3) & 3); }
DI u32 a_dep(const Ctx& c, int half) {
  const int h = a_head(c.bid), gp = half * 8 + (c.bid >> 5), clo = 5 * (h >> 1) + 2 * (h & 1), lane = c.l16 >> 4;
  const int j = lane < 36 ? lane : 0, sec = j / 12, lh = (j / 6) & 1, pi = (j >> 1) % 3, rg = j & 1;
  int g2 = gp - (sec == 0 ? 0 : pi); if (g2 < 0) g2 = gp;
  return (wg_qg(2 * (g2 & 7) + rg, (80 * sec + clo + 2 * lh) >> 4) << 7) | (g2 >> 3);
}
DI void a_poll(const Ctx& c, VM& vm, Poll& p, u32 fl, u32 dep) { poll_issue(c, vm, p, dep & ~1u, fl + 1 + (dep & 1)); }
template <int HALF, int K> DI void a_stage(const Ctx& c, VM& vm, AIn& in) {
  if constexpr (K == 0) {
    const u32 bid = sop(c.bid), h = a_head(bid), odd = h & 1, cA = 5 * (h >> 1) + 3 * odd, cC = 5 * (h >> 1) + 2, l = vop(c.l16), r16 = l & 0x1F0; const bool hh = l >= 512;
    in.r0 = (HALF * 8 + (bid >> 5)) * 128;
    in.v0 = (hh ? 81 + cA : 80 + cA) * 32768 + r16;
    ld16<-1, true, 0>(c.ra, vm, in.Q0, in.v0, A_QKV - 80 * 32768 + (in.r0 + 32 * c.w) * 16);
    in.v1 = (hh ? 160 + cA : 80 + cC) * 32768 + r16 - 1024;
    in.v2 = (hh ? 160 + cC : 161 + cA) * 32768 + (r16 ^ (hh ? 128 : 64)) - 2048;
    in.cur = A_QKV - 80 * 32768 + (in.r0 + 32 * c.w) * 16 + odd * 8;
  } else if constexpr (K == 1) {
    ld8c<1024>(c.ra, vm, in.qc, in.v1, in.cur);
    const u32 po = c.m0a + (HALF ? L_HY : L_HX) + 3072 + (vop(c.l16) >> 1);
#pragma unroll
    for (int u = 0; u < 3; ++u) *LP(u32x2, po + u * TP) = u32x2{0x3F803F80u, 0x3F803F80u};
  } else {
    constexpr int u = (K - 2) / 3, j = (K - 2) % 3;
    if constexpr (j == 0) {
      int krow = in.r0 - 256 + 96 * (int)c.w + 32 * u; if (HALF == 0 && krow < 0) krow = in.r0;
      glds_head<(HALF ? L_HY : L_HX) + u * TP, A_QKV>(c, vm, in.cur, c.m0a, krow * 16, in.v0);
    } else glds_next<j * 1024>(c, vm, in.cur, j == 1 ? in.v1 : in.v2);
    in.mark = vm.n;
  }
}
DI float mx3(float a, float b, float c) { float r; asm("v_max3_f32 %0, %1, %2, %3" : "=v"(r) : "v"(a), "v"(b), "v"(c)); return r; }
DI void smfma0(f32x16& d, u32x4 a, u32x4 b) { asm volatile("v_mfma_f32_32x32x16_bf16 %0, %1, %2, 0" : "=&v"(d) : "v"(a), "v"(b)); }
DI void smfmac(f32x16& d, u32x4 a, u32x4 b, const f32x16& cc) { asm volatile("v_mfma_f32_32x32x16_bf16 %0, %1, %2, %3" : "=&v"(d) : "v"(a), "v"(b), "v"(cc)); }
DI void smfma1(f32x16& d, u32x2 a, u32x2 b) { asm volatile("v_mfma_f32_32x32x8_bf16 %0, %1, %2, %0" : "+v"(d) : "v"(a), "v"(b)); }
DI void omfma(f32x16& d, u32x4 a, u32x4 b) { asm volatile("v_mfma_f32_32x32x16_bf16 %0, %1, %2, %0" : "+v"(d) : "v"(a), "v"(b)); }
#define MX3(a, b, c) __builtin_fmaxf(__builtin_fmaxf((a), (b)), (c))
template <int HALF, class HL, class HA> DI void attn(const Ctx& c, VM& vm, AIn& in, HL hl, HA ha) {
  const u32 bid = sop(c.bid), h = a_head(bid), odd = h & 1, l = vop(c.l16), r16 = l & 0x1F0; const bool hh = l >= 512;
  const int q0 = (HALF * 8 + (bid >> 5)) * 128 + 32 * c.w;
  const u32 img = c.lb + (HALF ? L_HY : L_HX) + c.w * TP;
  const u32 kp0 = vop(img + l), kp1 = vop(img + 1024 + odd * 8 + r16), mp = vop(c.lb + L_MASK + l);
  const u32 lane = l >> 4, pc4 = lane & 3, db = (lane >> 4) & 1, kq = ((lane >> 3) & 4) + ((lane >> 2) & 3);
  const u32 vpl = db ? (pc4 ? 3 : 2) : pc4 & 1;
  const u32 vlo = vop(img + 1536 + vpl * 512 + (db ? odd : pc4 >> 1) * 8 + ((kq ^ (4 * vpl)) << 4)), vhi = vop(vlo ^ 128);
  const int t0 = (256 - q0) >> 5;
  auto vfrag = [&](int t, int s) __attribute__((always_inline)) { const u32x2 lo = trread(vlo + t * TP + s * 256), hi = trread(vhi + t * TP + s * 256); return u32x4{lo.x, lo.y, hi.x, hi.y}; };
  u32x4 sel[4];
#pragma unroll
  for (int j = 0; j < 4; ++j) sel[j] = *LP(const u32x4, mp + j * KB);
  const u32x4 mkA = *LP(const u32x4, mp + 4 * KB), mkB = *LP(const u32x4, mp + 5 * KB);
  prof<true>(vm);
  need(vm, in.mark); BARL(); pin(in.Q0); pin(in.qc);
  prof<true>(vm);
  u32x4 K0[9]; u32x2 K1[9];
  auto kread = [&](int t) __attribute__((always_inline)) { K0[t] = *LP(const u32x4, kp0 + t * TP); K1[t] = *LP(const u32x2, kp1 + t * TP); };
  auto kfix = [&](int t) __attribute__((always_inline)) { if (HALF == 0) { K1[t].x = hh ? (t < t0 ? 0xF149u : 0u) : K1[t].x; pin(K1[t]); } };
  kread(0); kread(8);
  u32x4 Q0 = in.Q0; u32x2 Q1 = hh ? u32x2{HALF == 0 ? 0x3F80u : 0u, 0} : in.qc;
  pin(Q1);
  SB(); kread(1); kread(2); SB();
  f32x16 S[2], M;
  kfix(0); kfix(8);
  SB(); asm volatile("s_nop 0"); smfma0(M, K0[0], Q0);
  SB(); kread(3); kread(4); SB();
  smfma1(M, K1[0], Q1);
  SB(); hl(IC<0>{}); kread(5); SB();
  smfma0(S[0], K0[8], Q0);
  SB(); kread(6); kread(7); SB();
  smfma1(S[0], K1[8], Q1);
  SB(); hl(IC<1>{});
  const u32x4 V0a = vfrag(0, 0), V8a = vfrag(8, 0), V0b = vfrag(0, 1), V8b = vfrag(8, 1);
  SB(); hl(IC<2>{});
#pragma unroll
  for (int t = 1; t < 8; ++t) kfix(t);
  asm volatile("s_nop 1" : "+v"(M), "+v"(S[0]));
  prof<true>(vm);
  SB();
#pragma unroll
  for (int g = 0; g < 16; ++g) M[g] = __uint_as_float((sel[g >> 2][g & 3] & __float_as_uint(S[0][g])) | (~sel[g >> 2][g & 3] & __float_as_uint(M[g])));
  float m = mx3(mx3(mx3(M[0], M[1], M[2]), mx3(M[3], M[4], M[5]), mx3(M[6], M[7], M[8])), mx3(mx3(M[9], M[10], M[11]), mx3(M[12], M[13], M[14]), M[15]), M[15]);
  { auto t = __builtin_amdgcn_permlane32_swap(__float_as_uint(m), __float_as_uint(m), false, false); m = mx3(__uint_as_float(t[0]), __uint_as_float(t[1]), __uint_as_float(t[1])); }
  f32x16 negm, O;
#pragma unroll
  for (int g = 0; g < 16; ++g) negm[g] = -m;
  pin(negm);
  prof<true>(vm);
  SB(); ha(IC<0>{}); hl(IC<3>{});
  prof<true>(vm);
  SB();
#define EM(i) __builtin_amdgcn_exp2f(M[i] - m)
  smfmac(S[1], K0[1], Q0, negm);
  u32x4 P; P.x = vop(pk2(EM(0), EM(1))); P.y = vop(pk2(EM(2), EM(3)));
  SB(); smfma1(S[1], K1[1], Q1);
  P.z = vop(pk2(EM(4), EM(5))); P.w = vop(pk2(EM(6), EM(7)));
  u32x4 A8 = P & mkA, A0 = P & ~mkA; pin(A8); pin(A0);
  P.x = vop(pk2(EM(8), EM(9)));
  SB(); hl(IC<4>{}); SB(); smfma0(O, V0a, A0);
  P.y = vop(pk2(EM(10), EM(11))); P.z = vop(pk2(EM(12), EM(13)));
  SB(); hl(IC<5>{}); ha(IC<9>{}); SB(); omfma(O, V8a, A8);
  P.w = vop(pk2(EM(14), EM(15)));
  u32x4 Pp = P & mkB, B0 = P & ~mkB, Vp = V8b; pin(Pp); pin(B0);
  SB(); hl(IC<6>{}); SB();
#define EX(i) __builtin_amdgcn_exp2f(S[t & 1][i])
  sfor<1, 8>(LAMBDA(T) {
    constexpr int t = T.v;
    prof<true>(vm);
    const u32x4 Va = vfrag(t, 0), Vb = vfrag(t, 1);
    u32x4 Pa, Pb;
    SB();
    if constexpr (t == 1) omfma(O, V0b, B0); else omfma(O, Vp, Pp);
    Pa.x = vop(pk2(EX(0), EX(1))); Pa.y = vop(pk2(EX(2), EX(3)));
    SB(); hl(IC<4 + 3 * t>{}); SB();
    if constexpr (t < 7) smfmac(S[(t + 1) & 1], K0[t + 1], Q0, negm);
    Pa.z = vop(pk2(EX(4), EX(5))); Pa.w = vop(pk2(EX(6), EX(7)));
    SB(); hl(IC<5 + 3 * t>{}); SB();
    if constexpr (t < 7) smfma1(S[(t + 1) & 1], K1[t + 1], Q1);
    Pb.x = vop(pk2(EX(8), EX(9))); Pb.y = vop(pk2(EX(10), EX(11)));
    SB();
    if constexpr (t == 1) { omfma(O, Vp, Pp); SB(); }
    ha(T);
    SB(); hl(IC<6 + 3 * t>{});
    __builtin_amdgcn_s_waitcnt(0xC07F);
    SB();
    omfma(O, Va, Pa);
    Pb.z = vop(pk2(EX(12), EX(13))); Pb.w = vop(pk2(EX(14), EX(15)));
    Pp = Pb; Vp = Vb;
    SB();
  });
  const u32 vb = A_OF + ((q0 >> 4) * 80 + 5 * (h >> 1)) * 256 + ((l >> 8) & 1) * 20480 + (l & 0xF0);
  const u32 vo = vb + (3 * odd + (hh ? 1 : 0)) * 256, vt = hh ? OOR : vb + 512 + odd * 8;
  asm volatile("s_nop 1" :: "v"(negm));
  omfma(O, Vp, Pp);
  prof<true>(vm);
  ha(IC<8>{});
  asm volatile("s_nop 11" : "+v"(O) : "v"(vo), "v"(vt));
  if (__builtin_expect(!__all(O[12] < 1.0995116e12f), 0)) {
    f32x16 X;
    auto score = [&](int t) __attribute__((always_inline)) {
      u32x4 k0 = *LP(const u32x4, kp0 + t * TP); u32x2 k1 = *LP(const u32x2, kp1 + t * TP);
      if (HALF == 0) k1.x = hh ? (t < t0 ? 0xF149u : 0u) : k1.x;
      asm volatile("s_nop 1\n\tv_mfma_f32_32x32x16_bf16 %0, %1, %2, 0\n\tv_mfma_f32_32x32x8_bf16 %0, %3, %4, %0\n\ts_nop 11" : "=&v"(X) : "v"(k0), "v"(Q0), "v"(k1), "v"(Q1));
      const u32 x0 = t == 0 ? ~0u : 0u, x1 = (t & 7) ? ~0u : 0u;
#pragma unroll
      for (int j = 0; j < 4; ++j) { const u32x4 ok = (*LP(const u32x4, mp + j * KB) ^ x0) | x1;
#pragma unroll
        for (int e = 0; e < 4; ++e) X[4 * j + e] = __uint_as_float((ok[e] & __float_as_uint(X[4 * j + e])) | (~ok[e] & 0xFF800000u)); }
    };
    float mt = -INFINITY;
#pragma unroll 1
    for (int t = 0; t < 9; ++t) { score(t);
#pragma unroll
      for (int g = 0; g < 16; ++g) mt = __builtin_fmaxf(mt, X[g]); }
    { auto t = __builtin_amdgcn_permlane32_swap(__float_as_uint(mt), __float_as_uint(mt), false, false); mt = __builtin_fmaxf(__uint_as_float(t[0]), __uint_as_float(t[1])); }
#pragma unroll
    for (int g = 0; g < 16; ++g) O[g] = 0.f;
#pragma unroll 1
    for (int t = 0; t < 9; ++t) { score(t);
      u32x4 pa, pb;
#pragma unroll
      for (int g = 0; g < 4; ++g) { pa[g] = pk2(__builtin_amdgcn_exp2f(X[2 * g] - mt), __builtin_amdgcn_exp2f(X[2 * g + 1] - mt)); pb[g] = pk2(__builtin_amdgcn_exp2f(X[8 + 2 * g] - mt), __builtin_amdgcn_exp2f(X[9 + 2 * g] - mt)); }
      const u32x4 va = vfrag(t, 0), vb = vfrag(t, 1);
      asm volatile("s_nop 1\n\tv_mfma_f32_32x32x16_bf16 %0, %1, %2, %0\n\tv_mfma_f32_32x32x16_bf16 %0, %3, %4, %0\n\ts_nop 11" : "+v"(O) : "v"(va), "v"(pa), "v"(vb), "v"(pb)); }
  }
  const float inv = __builtin_amdgcn_rcpf(O[12]);
  st16c(c, vm, vo, u32x4{pk2(O[0] * inv, O[1] * inv), pk2(O[2] * inv, O[3] * inv), pk2(O[4] * inv, O[5] * inv), pk2(O[6] * inv, O[7] * inv)});
  st8c(c, vm, vt, u32x2{pk2(O[8] * inv, O[9] * inv), pk2(O[10] * inv, O[11] * inv)});
}

struct Job { u32x2 a[4], b[4]; u32 kind, dst, gn0, m, magic, stride, cs; };
DI void job_load(const Params& p, Job& j, u32 id, u32 w, u32 lane) {
  const GAS unsigned char* A; const GAS unsigned char* B; u32 ld, cb, s;
  if (id < 150) { cb = (id * 205) >> 10; s = 4 * (id - 5 * cb) + w; A = (const GAS unsigned char*)p.wqkv; B = A + 8; ld = 3840; j.kind = 0; j.dst = OFF_BQ; j.m = 2; j.magic = 32768; j.stride = 40; j.cs = OFF_CS + s * CS_ROW + cb * 256; }
  else if (id < 390) { id -= 150; cb = (id * 205) >> 10; s = 4 * (id - 5 * cb) + w; A = (const GAS unsigned char*)p.wg; B = (const GAS unsigned char*)p.wu; ld = 3072; j.kind = 1; j.dst = OFF_BG; j.m = 3; j.magic = 21846; j.stride = 60; j.cs = OFF_CS + s * CS_ROW + 7680 + cb * 256; }
  else { j.kind = 2; ld = 1280; j.m = 5; j.magic = 13108; j.cs = OOR;
    if (id < 440) { id -= 390; cb = (id * 205) >> 10; s = 4 * (id - 5 * cb) + w; A = (const GAS unsigned char*)p.wo; j.dst = OFF_BW; j.stride = 100; }
    else { const bool none = id >= 560; id = none ? 0 : id - 440; cb = (id * 171) >> 11; s = 4 * (id - 12 * cb) + w; A = (const GAS unsigned char*)p.wd; j.dst = none ? OOR : OFF_BD; j.stride = 240; }
    B = A + 8; }
  j.dst += s * j.m * KB; j.gn0 = 4 * cb;
  const u32 so = 32 * s * ld + (cb << (j.kind == 1 ? 6 : 7)), vo = (lane >> 3) * ld + ((lane & 7) << (j.kind == 1 ? 3 : 4));
#pragma unroll
  for (int q = 0; q < 4; ++q) { j.a[q] = *(const GAS u32x2*)(A + so + q * 8 * ld + vo); j.b[q] = *(const GAS u32x2*)(B + so + q * 8 * ld + vo); }
}
DI void job_run(const Ctx& c, const Job& j, u32 lane) {
  const u32 L = c.lb + 16 * KB + c.w * 4608, g = j.kind == 1 ? ~0u : 0u;
#pragma unroll
  for (int q = 0; q < 4; ++q) {
    const u32 t = (j.a[q].y ^ j.b[q].x) & g;
    *LP(u32x4, L + (8 * q + (lane >> 3)) * 144 + (lane & 7) * 16) = u32x4{j.a[q].x, j.a[q].y ^ t, j.b[q].x ^ t, j.b[q].y};
  }
  const u32 ra = L + (8 * (lane >> 4) + ((lane & 15) >> 2)) * 144 + ((lane & 3) << (j.kind == 0 ? 4 : 3)), rb = ra + (j.kind == 0 ? 8 : 32);
  u32x4 v[4]; f32x4 cs[4];
#pragma unroll
  for (int f = 0; f < 4; ++f) { const u32 a = ((f & 1) ? rb : ra) + (f >> 1) * 64; const u32x2 lo = trread(a), hi = trread(a + 4 * 144); v[f] = u32x4{lo.x, lo.y, hi.x, hi.y}; }
  const u32x4 ones = {0x3F803F80u, 0x3F803F80u, 0x3F803F80u, 0x3F803F80u};
  asm volatile("s_nop 1\n\tv_mfma_f32_16x16x32_bf16 %0, %4, %8, 0\n\tv_mfma_f32_16x16x32_bf16 %1, %5, %8, 0\n\tv_mfma_f32_16x16x32_bf16 %2, %6, %8, 0\n\tv_mfma_f32_16x16x32_bf16 %3, %7, %8, 0"
               : "=&v"(cs[0]), "=&v"(cs[1]), "=&v"(cs[2]), "=&v"(cs[3]) : "v"(v[0]), "v"(v[1]), "v"(v[2]), "v"(v[3]), "v"(ones));
#pragma unroll
  for (int f = 0; f < 4; ++f) { const u32 gn = j.gn0 + f, q = (gn * j.magic) >> 16; __builtin_amdgcn_raw_buffer_store_b128(v[f], c.rs, j.dst + (q * j.stride + gn - q * j.m) * KB + c.l16, 0, 16); }
  asm volatile("s_nop 7" : "+v"(cs[0]), "+v"(cs[1]), "+v"(cs[2]), "+v"(cs[3]));
  const u32 fsel = lane & 15;
  const f32x4 o = fsel == 0 ? cs[0] : fsel == 1 ? cs[1] : fsel == 2 ? cs[2] : cs[3];
  const u32 oo = j.kind == 0 ? (fsel >> 1) * 128 + (lane >> 4) * 32 + (fsel & 1) * 16 : fsel * 64 + (lane >> 4) * 16;
  __builtin_amdgcn_raw_buffer_store_b128(__builtin_bit_cast(u32x4, o), c.rs, fsel < 4 ? j.cs + oo : OOR, 0, 16);
}
DI float red32(float v) {
  v = dpp_add<0x140>(dpp_add<0x141>(dpp_add<0x4E>(dpp_add<0xB1>(v))));
  auto t = __builtin_amdgcn_permlane16_swap(__float_as_uint(v), __float_as_uint(v), false, false); return __uint_as_float(t[0]) + __uint_as_float(t[1]);
}
DI void ln1(const Ctx& c, VM& vm, const u32x4 (&v)[3], u32 lane) {
  const u32 row = c.bid * 8 + c.w * 2 + (lane >> 5), l32 = lane & 31;
  const float e = l32 < 16 ? 1.f : 0.f;
  float f[24], s = 0, s2 = 0;
#pragma unroll
  for (int j = 0; j < 12; ++j) { f[2 * j] = bf_lo(v[j >> 2][j & 3]); f[2 * j + 1] = bf_hi(v[j >> 2][j & 3]); if (j < 8) s += f[2 * j] + f[2 * j + 1]; else s2 += f[2 * j] + f[2 * j + 1]; }
  const float mean = red32(s + e * s2) * (1.0f / D);
  float q = 0, q2 = 0;
#pragma unroll
  for (int j = 0; j < 24; ++j) { const float d = f[j] - mean; if (j < 16) q += d * d; else q2 += d * d; }
  const float mb = rbf(mean), sd = rbf(sqrtf(rbf(rbf(red32(q + e * q2) * (1.0f / D)) + 1e-5f))), rs = 1.0f / sd;
  const u32 wa = c.lb + l32 * 144 + (row & 7) * 16;
#pragma unroll
  for (int k = 0; k < 3; ++k) {
    u32x4 o;
#pragma unroll
    for (int j = 0; j < 4; ++j) { const u32 t = pk2(f[8 * k + 2 * j] - mb, f[8 * k + 2 * j + 1] - mb); o[j] = pk2(bf_lo(t) * rs, bf_hi(t) * rs); }
    *LP(u32x4, wa + k * 32 * 144) = o;
  }
}
DI void ln1_store(const Ctx& c, VM& vm) {
  const u32 t = threadIdx.x, ra = c.lb + (t >> 3) * 144 + (t & 7) * 16, dst = A_XA + (c.bid >> 1) * (80 * 256) + (c.bid & 1) * 128 + (t >> 3) * 256 + (t & 7) * 16;
#pragma unroll
  for (int k = 0; k < 3; ++k) st16c(c, vm, (k < 2 || t < 128) ? dst + k * 32 * 256 : OOR, *LP(const u32x4, ra + k * 32 * 144));
}

constexpr int CARRY0 = 8, CARRY1 = 4, CARRY2 = 0, CARRYST = 4;
#define G decltype(Gp)::v
#define QSYNC 0x0B
#define QPG 1
#define QP1 8
#define QP2 11
#define QC 14
#define AP1 1
#define AP2 3
#define AC 4
#define DPUB 4
#define DP1 7
#define DP2 10

__global__ void __launch_bounds__(256, 1) mega_kernel(Params p) {
  extern __shared__ __attribute__((aligned(16))) unsigned char smem[];
  {
    unsigned long long pc; u32 len;
    asm volatile("s_getpc_b64 %0\n.Lmk_begin:\n\ts_mov_b32 %1, .Lmk_end-.Lmk_begin" : "=s"(pc), "=s"(len));
    const rsrc_t rc = __builtin_amdgcn_make_buffer_rsrc((void*)pc, 0, (int)len, 0x00020000);
    const rsrc_t rw = __builtin_amdgcn_make_buffer_rsrc(p.ws, 0, (int)OFF_DBG, 0x00020000);
    asm volatile("s_nop 4\n\tbuffer_load_dwordx2 a[248:249], %0, %1, 0 offen\n\tbuffer_load_dword a250, %2, %3, 0 offen sc1"
                 :: "v"(((blockIdx.x >> 3) * 256 + threadIdx.x) * 8), "s"(rc), "v"(threadIdx.x < 64 ? (threadIdx.x << 20) + 40 * KB : OOR), "s"(rw) : "memory");
  }
  Ctx c;
  c.ws = p.ws; c.rs = __builtin_amdgcn_make_buffer_rsrc(p.ws, 0, (int)OFF_ACT, 0x00020000);
  c.ra = __builtin_amdgcn_make_buffer_rsrc(p.ws + OFF_ACT, 0, (int)(2 * LB), 0x00020000);
  c.lb = (u32)(size_t)(LAS unsigned char*)smem;
  c.l16 = (threadIdx.x & 63) * 16; c.w = __builtin_amdgcn_readfirstlane(threadIdx.x >> 6); c.bid = blockIdx.x;
  const u32 x = c.bid & 7, i = c.bid >> 3;
  c.sq = 64 * ((x >> 1) * 4 + (i >> 3)); c.cg = (x & 1) * 8 + (i & 7); c.sw = 128 * x + 32 * (i >> 3);
  const u32 cgq = c.cg < 15 ? c.cg : 14;
  c.tq = (c.sq >> 4) + c.w;
  c.bof = A_OF + ((c.sw >> 4) * 80 + 20 * c.w) * 256; c.bhf = A_HF + ((c.sw >> 4) * 192 + 48 * c.w) * 256;
  c.m0x = c.lb + c.w * 4 * KB; c.m0a = c.lb + 3 * c.w * TP;
  { const int l = 0; (void)l; STAMP(0); }
  asm volatile("" ::: "a0", "a255");
  BF<0, 40> Bq; BF<1, 25> Bw; BF<2, 60> Bg; BF<3, 60> Bd;
  Bq.base = OFF_BQ + (cgq * 4 + c.w) * 40 * KB; Bw.base = OFF_BW + ((i & 7) * 4 + c.w) * 25 * KB;
  Bg.base = OFF_BG + (c.cg * 4 + c.w) * 60 * KB; Bd.base = OFF_BD + ((i & 7) * 4 + c.w) * 60 * KB;
  XR xr[2];
  Stage sx, sy; Poll pl; AIn ain; VM vm = {}; int mst = 0;
  const u32 lane = threadIdx.x & 63;
  u32 ep = __hip_atomic_load((const GAS u32*)(p.ws + OFF_EPOCH), RLX_AGENT);
  u32x4 lv[3];
  { const GAS u32x4* src = (const GAS u32x4*)((const GAS u16*)p.x + (c.bid * 8 + c.w * 2 + (lane >> 5)) * D);
    lv[0] = src[lane & 31]; lv[1] = src[32 + (lane & 31)]; lv[2] = src[64 + (lane & 15)]; }
  Job job[3];
  sfor<0, 3>(LAMBDA(J) { job_load(p, job[J.v], c.bid + 256 * J.v, c.w, lane); SB(); });
  {
    const u32 cc = lane & 7;
#pragma unroll
    for (int hf = 0; hf < 2; ++hf) {
      const GAS u16* src = (const GAS u16*)p.x + (c.sw + hf * 1024 + 8 * c.w + (lane >> 3)) * D + 80 * (i & 7);
      xr[hf].m = *(const GAS u32x4*)(src + 8 * cc); xr[hf].t = *(const GAS u32*)(src + 64 + 2 * cc);
    }
    if (threadIdx.x < 80) *LP(u32x4, c.lb + L_CS + threadIdx.x * 16) = u32x4{0, 0, 0, 0};
    const int z = (int)(lane & 31) - 4 * (int)(lane >> 5) - 8 * (int)c.w;
    *LP(u32x4, c.lb + L_MASK + c.w * KB + c.l16) = u32x4{z >= 0 ? ~0u : 0u, z >= 1 ? ~0u : 0u, z >= 2 ? ~0u : 0u, z >= 3 ? ~0u : 0u};
    *LP(u32x2, c.lb + L_MASK + (4 + (c.w >> 1)) * KB + c.l16 + (c.w & 1) * 8) = u32x2{(z >= 0 ? 0xFFFFu : 0u) | (z >= 1 ? 0xFFFF0000u : 0u), (z >= 2 ? 0xFFFFu : 0u) | (z >= 3 ? 0xFFFF0000u : 0u)};
    const u32 t16 = (8 * c.w + (lane >> 3)) & 15, g = (t16 >> 1) & 3, cg = i & 7, h = 8 * c.w + cc;
    c.dxx = ((c.tq >> 3) + 8 * (((c.tq >> 1) & 3) * 8 + cc)) << 7;
    c.doh = (((h >> 2) + 8 * ((h & 3) + 4 * x)) << 7) | (wg_qg(c.sw >> 6, 4 * c.w + (lane & 3)) << 23);
    c.foff = threadIdx.x == 0 ? OFF_FLAGS + c.bid * 128 : OOR;
    c.daa = a_dep(c, 0) | (a_dep(c, 1) << 16);
    c.rw = c.lb + L_HX + c.w * 10 * KB + (lane & 15) * 64 + (((lane >> 4) ^ (lane >> 1)) & 3) * 16;
    const u32 base = c.lb + L_HX + (c.w >> 1) * 5 * KB + t16 * 64, row = c.sw + 8 * c.w + (lane >> 3);
    c.rpa = base + (cc >> 1) * KB + (((2 * (cc & 1)) ^ g ^ ((cc >> 1) & 1)) << 4);
    c.rpt = base + 4 * KB + (((cc >> 1) ^ g) << 4) + (cc & 1) * 8;
    c.rvo = ((row >> 4) * 80 + 10 * cg + cc) * 256 + t16 * 16;
    c.rvt = ((row >> 4) * 80 + 10 * cg + 8 + (cc >> 2)) * 256 + t16 * 16 + (cc & 3) * 4;
    c.rvs = cc == 0 ? (A_SA - A_XA) + row * 64 + cg * 8 : OOR;
  }
  STAMP(104);
  ep = __builtin_amdgcn_readfirstlane(ep);
  STAMP(105);
  flag_store(c, vm, ep);
  ln1(c, vm, lv, lane);
  BARL();
  ln1_store(c, vm);
  STAMP(106);
  job_run(c, job[0], lane);
  STAMP(107);
  asm volatile("s_waitcnt vmcnt(0)\n\ts_barrier" : "+v"(xr[0].m), "+v"(xr[0].t), "+v"(xr[1].m), "+v"(xr[1].t) :: "memory");
  STAMP(108);
  flag_store(c, vm, ep + 1);
  job_run(c, job[1], lane);
  STAMP(109);
  vm.n = 0; vm.done = 0;
  poll_issue(c, vm, pl, (lane < 4 ? 2 * (c.tq + (lane >> 1) * 64) + (lane & 1) : 5 * ((cgq * 4 + c.w) >> 1) + (lane - 4) % 5) << 7, ep + 1);
  if (c.bid < 48) job_run(c, job[2], lane);
  poll_again(c, vm, pl);
  STAMP(110);
  poll_check<true>(c, vm, pl);
  STAMP(111);
  loadB<0, 24>(c, vm, Bq);
  BARL();
  stages<0, 0, 13>(c, vm, sx, A_XA, c.tq);
  STAMP(112);
  need(vm, Bq.m[9]);
  STAMP(113);
  u32 fl = ep + 2, dq = threadIdx.x << 7;
#pragma unroll 1
  for (int l = 0; l < 2; ++l) {
    vm.n = 0; vm.done = -1000; sx.mg[0] = -CARRY0; sx.mg[1] = -CARRY1; sx.mg[2] = -CARRY2; mst = -CARRYST;
    sfor<0, 24>(LAMBDA(J) { Bq.m[J.v] = -CARRY0; });
    asm volatile("; MARK QX");
    { f32x4 acc[2][4]; EpQ e;
      TS(0, 0);
      gemm_lds<0, 2, QSYNC, QPG>(c, vm, sx, l != 0, mst, fl, acc, Bq, LAMBDA(Gp) {
        at<G, 0, 1, 8>(LAMBDA(K) { stage<0, 13 + K.v>(c, vm, sx, 0, 0); });
        at<G, 8, 1, 16>(LAMBDA(K) { ldB<24 + K.v>(c, vm, Bq); });
        at<G, 2 * QC + 1, 1, 13>(LAMBDA(K) { stage<1, K.v>(c, vm, sy, A_XA, c.tq); });
      }, LAMBDA(S) { constexpr int s = S.v;
        if (s == QP1) poll_issue(c, vm, pl, dq, fl);
        if (s == QP2) poll_again(c, vm, pl);
        if (s == QC) { TS(0, 1); poll_check<true>(c, vm, pl); TS(0, 2); }
        if (s == 17) pre_q<0>(c, e);
      });
      TS(0, 3);
      epi_q(c, vm, e, acc); mst = vm.n; stages<1, 39 - 2 * QC, 13>(c, vm, sy, 0, 0); TS(0, 4); }
    asm volatile("; MARK QY");
    { f32x4 acc[2][4]; EpQ e;
      TS(1, 0);
      gemm_lds<1, 2, QSYNC, QPG>(c, vm, sy, l != 0, mst, fl + 1, acc, Bq, LAMBDA(Gp) {
        at<G, 0, 1, 8>(LAMBDA(K) { stage<1, 13 + K.v>(c, vm, sy, 0, 0); });
        at<G, 8, 1, 2 * QC - 7>(LAMBDA(K) { ldB<K.v>(c, vm, Bw); });
        at<G, 2 * QC + 1, 1, 11>(LAMBDA(K) { a_stage<0, K.v>(c, vm, ain); });
      }, LAMBDA(S) { constexpr int s = S.v;
        if (s == QP1) a_poll(c, vm, pl, fl, lo16(c.daa));
        if (s == QP2) poll_again(c, vm, pl);
        if (s == QC) { TS(1, 1); poll_check<true>(c, vm, pl); TS(1, 2); }
        if (s == 17) pre_q<1>(c, e);
      });
      TS(1, 3);
      sfor<39 - 2 * QC, 11>(LAMBDA(K) { a_stage<0, K.v>(c, vm, ain); });
      epi_q(c, vm, e, acc); mst = vm.n; TS(1, 4); }
    asm volatile("; MARK AX");
    AIn ain2;
    constexpr int BWR = 32 - 2 * QC;
    TS(2, 0);
    attn<0>(c, vm, ain, LAMBDA(Gp) {
      at<G, 0, 1, BWR>(LAMBDA(K) { ldB<25 - BWR + K.v>(c, vm, Bw); });
      at<G, BWR, 1, 6 + 3 * AC - BWR>(LAMBDA(K) { ldB<K.v>(c, vm, Bg); });
      at<G, 6 + 3 * AC, 1, 11>(LAMBDA(K) { a_stage<1, K.v>(c, vm, ain2); });
    }, LAMBDA(S) { constexpr int s = S.v;
      if (s == 0) publish(c, vm, mst, fl + 2);
      if (s == AP1) a_poll(c, vm, pl, fl, hi16(c.daa));
      if (s == AP2) poll_again(c, vm, pl);
      if (s == AC) { TS(2, 1); poll_check<true>(c, vm, pl); TS(2, 2); }
      if (s == 8) { sfor<22 - 3 * AC, 11>(LAMBDA(K) { a_stage<1, K.v>(c, vm, ain2); }); TS(2, 3); }
    });
    mst = vm.n; TS(2, 4);
    asm volatile("; MARK AY");
    AW aw;
    TS(3, 0);
    attn<1>(c, vm, ain2, LAMBDA(Gp) {
      at<G, 0, 1, 6 + 3 * AC>(LAMBDA(K) { if constexpr (6 + 3 * AC - BWR + K.v < 38) ldB<6 + 3 * AC - BWR + K.v>(c, vm, Bg); });
      at<G, 6 + 3 * AC, 1, 10>(LAMBDA(K) { ld_w<0, K.v>(c, vm, aw); });
    }, LAMBDA(S) { constexpr int s = S.v;
      if (s == 0) publish(c, vm, mst, fl + 3);
      if (s == AP1) poll_issue(c, vm, pl, lo16(c.doh), fl + 3);
      if (s == AP2) poll_again(c, vm, pl);
      if (s == AC) { TS(3, 1); poll_check<true>(c, vm, pl); TS(3, 2); }
      if (s == 8) { sfor<22 - 3 * AC, 10>(LAMBDA(K) { ld_w<0, K.v>(c, vm, aw); }); TS(3, 3); }
    });
    mst = vm.n; TS(3, 4);
    asm volatile("; MARK WX");
    constexpr int BG0 = 12 + 6 * AC - BWR < 38 ? 12 + 6 * AC - BWR : 38;
    static_assert(44 - BG0 <= 13, "");
    { Red r;
      TS(4, 0);
      gemm_reg<0, 5>(c, vm, Bw, aw, LAMBDA(Gp) { at<G, 0, 1, 12>(LAMBDA(K) { ldB<BG0 + K.v>(c, vm, Bg); }); }, LAMBDA(S) { if (S.v == 1) red_pre<0>(c, r, xr[0], A_XB); if (S.v == 4) publish(c, vm, mst, fl + 4); });
      TS(4, 1);
      BARL();
      poll_issue(c, vm, pl, lo16(c.doh), fl + 4);
      TS(4, 2);
      red_out<0>(c, vm, r, xr[0], nullptr); mst = vm.n;
      TS(4, 3);
      poll_again(c, vm, pl);
      TS(4, 4); }
    asm volatile("; MARK WY");
    if (l == 0 && threadIdx.x < 80) {
      const u32 t = threadIdx.x, o = OFF_CS + (t < 32 ? cgq * 512 + t * 16 : 7680 + c.cg * 768 + (t - 32) * 16);
      f32x4 v[20];
#pragma unroll
      for (int j = 0; j < 20; ++j) v[j] = __builtin_bit_cast(f32x4, __builtin_amdgcn_raw_buffer_load_b128(c.rs, o, j * CS_ROW, 16));
#pragma unroll
      for (int j = 1; j < 20; ++j) v[0] += v[j];
      *LP(f32x4, c.lb + L_CS + t * 16) = v[0];
    }
    { Red r;
      poll_check<true>(c, vm, pl);
      TS(5, 0);
      sfor<0, 10>(LAMBDA(K) { ld_w<1, K.v>(c, vm, aw); });
      loadB<BG0 + 12, BG0 + 16>(c, vm, Bg);
      red_pre<1>(c, r, xr[1], A_XB);

      publish(c, vm, mst, fl + 5);
      gemm_reg<1, 5>(c, vm, Bw, aw, LAMBDA(Gp) {
        at<G, 0, 1, 44 - BG0>(LAMBDA(K) { ldB<BG0 + 16 + K.v>(c, vm, Bg); });
      }, LAMBDA(S) { constexpr int s = S.v; if (s == 1) poll_issue(c, vm, pl, c.dxx, fl + 5); if (s == 4) poll_again(c, vm, pl); });
      TS(5, 1); poll_check<true>(c, vm, pl); TS(5, 2);
      stages<0, 0, 9>(c, vm, sx, A_XB, c.tq);
      BARL();
      TS(5, 3);
      red_out<1>(c, vm, r, xr[1], nullptr); mst = vm.n;
      stages<0, 9, 13>(c, vm, sx, 0, 0);

      TS(5, 4); }
    asm volatile("; MARK GX");
    { f32x4 acc[3][4]; EpG e;
      TS(6, 0);
      gemm_lds<0, 3, 0x0B, 1>(c, vm, sx, true, mst, fl + 6, acc, Bg, LAMBDA(Gp) {
        at<G, 0, 1, 8>(LAMBDA(K) { stage<0, 13 + K.v>(c, vm, sx, 0, 0); });
        at<G, 8, 1, 25>(LAMBDA(K) { ldB<K.v>(c, vm, Bd); });
        at<G, 37, 1, 21>(LAMBDA(K) { stage<1, K.v>(c, vm, sy, A_XB, c.tq); });
      }, LAMBDA(S) { constexpr int s = S.v;
        if (s == 7) poll_issue(c, vm, pl, c.dxx, fl + 6);
        if (s == 10) poll_again(c, vm, pl);
        if (s == 12) { TS(6, 1); poll_check<true>(c, vm, pl); TS(6, 2); }
        if (s == 18) pre_g<0>(c, e);
      });
      TS(6, 3);
      epi_g(c, vm, e, acc); mst = vm.n; TS(6, 4); }
    asm volatile("; MARK GY");
    AD<true> ad;
    { f32x4 acc[3][4]; EpG e;
      TS(7, 0);
      gemm_lds<1, 3, 0x13, 1>(c, vm, sy, true, mst, fl + 7, acc, Bg, LAMBDA(Gp) {
        at<G, 3, 1, 31>(LAMBDA(K) { ldB<25 + K.v>(c, vm, Bd); });
        at<G, 37, 1, 23>(LAMBDA(K) { ld_d<0, K.v>(c, vm, ad); });
      }, LAMBDA(S) { constexpr int s = S.v;
        if (s == 7) poll_issue(c, vm, pl, hi16(c.doh), fl + 7);
        if (s == 10) poll_again(c, vm, pl);
        if (s == 12) { TS(7, 1); poll_check<true>(c, vm, pl); TS(7, 2); }
        if (s == 18) pre_g<1>(c, e);
      });
      TS(7, 3);
      epi_g(c, vm, e, acc); mst = vm.n; loadB<56, 60>(c, vm, Bd); TS(7, 4); }
    asm volatile("; MARK DX");
    if (l) Bq.base = OOR;
    AD<false> ad2;
    { Red r;
      TS(8, 0);
      gemm_reg<0, 12>(c, vm, Bd, ad, LAMBDA(Gp) {
        at<G, 0, 1, 1>(LAMBDA(K) { ld_d<0, 23>(c, vm, ad); });
        at<G, 1, 1, 10>(LAMBDA(K) { ldB<K.v>(c, vm, Bq); });
      }, LAMBDA(S) { constexpr int s = S.v;
        if (s == DPUB) publish(c, vm, mst, fl + 8);
        if (s == DP1) poll_issue(c, vm, pl, hi16(c.doh), fl + 8);
        if (s == 9) red_pre<0>(c, r, xr[0], LB + A_XA);
        if (s == DP2) poll_again(c, vm, pl);
      });
      TS(8, 1);
      poll_check<true>(c, vm, pl);
      TS(8, 2);
      sfor<0, 6>(LAMBDA(K) { ld_d<1, K.v>(c, vm, ad2); });
      BARL();
      TS(8, 3);
      red_out<0>(c, vm, r, xr[0], l ? p.out : nullptr); mst = vm.n;
      sfor<6, 12>(LAMBDA(K) { ld_d<1, K.v>(c, vm, ad2); });

      TS(8, 4); }
    asm volatile("; MARK DY");
    { Red r;
      TS(9, 0);
      gemm_reg<1, 12>(c, vm, Bd, ad2, LAMBDA(Gp) {
        at<G, 0, 1, 12>(LAMBDA(K) { ld_d<1, 12 + K.v>(c, vm, ad2); });
        at<G, 17, 1, 11>(LAMBDA(K) { ldB<10 + K.v>(c, vm, Bq); });
      }, LAMBDA(S) { constexpr int s = S.v;
        if (s == DPUB) publish(c, vm, mst, fl + 9);
        if (s == DP1) poll_issue(c, vm, pl, c.dxx, l ? fl : fl + 9);
        if (s == 9) red_pre<1>(c, r, xr[1], LB + A_XA);
        if (s == 11) { loadB<21, 24>(c, vm, Bq); poll_again(c, vm, pl); }
      });
      TS(9, 1);
      poll_check<true>(c, vm, pl);
      TS(9, 2);
      stages<0, 0, 9>(c, vm, sx, LB + A_XA, c.tq);
      BARL();
      TS(9, 3);
      red_out<1>(c, vm, r, xr[1], l ? p.out : nullptr); mst = vm.n;
      stages<0, 9, 13>(c, vm, sx, 0, 0);

      TS(9, 4); }
    if (vm.n - sx.mg[0] < CARRY0 || vm.n - sx.mg[1] < CARRY1 || vm.n - sx.mg[2] < CARRY2 || vm.n - mst < CARRYST || vm.done < Bq.m[9] || Bq.m[23] > sx.mg[0]) carry_count_too_large();
    c.ra = __builtin_amdgcn_make_buffer_rsrc(p.ws + OFF_ACT + LB, 0, (int)LB, 0x00020000);
    sx.src -= LB; fl += 10; dq = c.dxx;
    prof<true>(vm); prof<true>(vm);
    asm volatile("; MARK END");
  }
  asm volatile(".Lmk_end:");
  if (blockIdx.x == 0 && threadIdx.x == 0) __hip_atomic_store((GAS u32*)(p.ws + OFF_EPOCH), ep + 64, RLX_AGENT);
  asm volatile("s_waitcnt vmcnt(0)" ::: "memory");
}

extern "C" void kernel_launch(void* const* d_in, const int* in_sizes, int n_in, void* d_out, int out_size, void* d_ws, size_t ws_size, hipStream_t stream) {
  Params p;
  p.x = (const u16*)d_in[0]; p.wqkv = (const u16*)d_in[1]; p.wo = (const u16*)d_in[2];
  p.wg = (const u16*)d_in[4]; p.wu = (const u16*)d_in[5]; p.wd = (const u16*)d_in[6];
  p.out = (u16*)d_out; p.ws = (unsigned char*)d_ws;
  (void)hipFuncSetAttribute((const void*)mega_kernel, hipFuncAttributeMaxDynamicSharedMemorySize, LDS_BYTES);
  mega_kernel<<<256, 256, LDS_BYTES, stream>>>(p);
}
```

```cpp
#include <hip/hip_runtime.h>
#include <stdint.h>

typedef unsigned short u16;
typedef unsigned u32;
typedef u32 u32x2 __attribute__((ext_vector_type(2)));
typedef u32 u32x4 __attribute__((ext_vector_type(4)));
typedef float f32x2 __attribute__((ext_vector_type(2)));
typedef float f32x4 __attribute__((ext_vector_type(4)));
typedef float f32x16 __attribute__((ext_vector_type(16)));
typedef short bf16x8 __attribute__((ext_vector_type(8)));
typedef short s16x4 __attribute__((ext_vector_type(4)));
typedef __bf16 bf16x2_t __attribute__((ext_vector_type(2)));
#define GAS __attribute__((address_space(1)))
#define LAS __attribute__((address_space(3)))
#define DI __device__ __forceinline__
#define RLX_AGENT __ATOMIC_RELAXED, __HIP_MEMORY_SCOPE_AGENT

constexpr int S = 2048, D = 640, F = 1536;
constexpr u32 KB = 1024;
constexpr u32 OFF_FLAGS = 0;
constexpr u32 OFF_TMO = 32 * KB;
constexpr u32 OFF_EPOCH = 33 * KB;
constexpr u32 OFF_CS = 64 * KB;
constexpr u32 CS_ROW = 3072 * 4;
constexpr u32 OFF_BQ = 512 * KB;
constexpr u32 OFF_BW = OFF_BQ + 2400 * KB;
constexpr u32 OFF_BG = OFF_BW + 800 * KB;
constexpr u32 OFF_BD = OFF_BG + 3840 * KB;
constexpr u32 A_XA = 0, A_SA = 2560 * KB, A_QKV = A_SA + 128 * KB, A_OF = A_QKV + 7680 * KB, A_XB = A_OF + 2560 * KB,
              A_SB = A_XB + 2560 * KB, A_HF = A_SB + 128 * KB, LB = A_HF + 6144 * KB;
constexpr u32 OFF_ACT = OFF_BD + 1920 * KB;
constexpr u32 OFF_DBG = OFF_ACT + 2 * LB;
constexpr u32 OOR = 0x80000000u;
constexpr u32 L_Y = 80 * KB, L_HX = 16 * KB, L_HY = 80 * KB, L_MISC = 144 * KB;
constexpr u32 TP = 3584;
constexpr u32 L_STAT = L_MISC;
constexpr u32 L_CS = L_STAT + 1024;
constexpr u32 L_MASK = L_CS + 1280;
constexpr u32 L_DBG = L_MASK + 6 * KB;
constexpr u32 LDS_BYTES = L_DBG + 1024;
static_assert(LDS_BYTES <= 160 * KB, "");

struct Params { const u16* x; const u16* wqkv; const u16* wo; const u16* wg; const u16* wu; const u16* wd; u16* out; unsigned char* ws; };

DI float bf_lo(u32 v) { return __uint_as_float(v << 16); }
DI float bf_hi(u32 v) { return __uint_as_float(v & 0xffff0000u); }
DI u32 pk2(float a, float b) { f32x2 f = {a, b}; bf16x2_t h = __builtin_convertvector(f, bf16x2_t); return __builtin_bit_cast(u32, h); }
DI float rbf(float a) { return bf_lo(pk2(a, a)); }

DI u32x2 trread(u32 a) { return __builtin_bit_cast(u32x2, __builtin_amdgcn_ds_read_tr16_b64_v4i16((LAS s16x4*)(size_t)a)); }

typedef __amdgpu_buffer_rsrc_t rsrc_t;
#define SB() __builtin_amdgcn_sched_barrier(0)
template <int I> struct IC { static constexpr int v = I; };
template <int B, int E, class Fn> DI void sfor(Fn&& f) { if constexpr (B < E) { f(IC<B>{}); sfor<B + 1, E>(f); } }
#define LAMBDA(S) [&](auto S) __attribute__((always_inline))
template <class T> DI void pin(T& d) { asm volatile("" : "+v"(d)); }
DI u32 sop(u32 v) { asm volatile("" : "+s"(v)); return v; }
DI u32 vop(u32 v) { asm volatile("" : "+v"(v)); return v; }
struct Ctx {
  rsrc_t rs, ra;
  u32 lb; unsigned char* ws;
  u32 l16, w, bid;
  u32 sq, sw, cg;
  u32 tq;
  u32 bof, bhf;
  u32 m0x, m0a;
  u32 foff, eq, eg, es;
  u32 av0, av1, av2, ak1, avl, aob;
  float qs;
  u32 dxx, doh, daa;
  u32 rw, rpa, rpt, rvo, rvt, rvs;
};
template <int G, int START, int STRIDE, int N, class Fn> DI void at(Fn&& f) {
  if constexpr (G >= START && (G - START) % STRIDE == 0 && (G - START) / STRIDE < N) f(IC<(G - START) / STRIDE>{});
}
template <int G, int START, int N, class Fn> DI void at56(Fn&& f) {
  if constexpr (G >= START && (G - START) % 6 != 5 && (G - START) - (G - START) / 6 < N) f(IC<(G - START) - (G - START) / 6>{});
}
#define LP(T, a) ((LAS T*)(size_t)(a))

struct VM { int n, done;
};
template <bool W, bool W2 = W> DI void prof(VM& vm) {
}
DI void need(VM& vm, int mark) {
  if (mark > vm.done) { const int k = vm.n - mark; if (k < 63) asm volatile("s_waitcnt vmcnt(%0)" :: "i"(k) : "memory"); vm.done = mark; }
}
template <int SLOT, bool SC1, int OFF> DI int ld16(rsrc_t rs, VM& vm, u32x4& d, u32 voff, u32 soff) {
  if constexpr (SLOT >= 0 && SC1) asm volatile("buffer_load_dwordx4 a[%c0:%c1], %2, %3, %4 offen offset:%5 sc1" :: "i"(4 * SLOT), "i"(4 * SLOT + 3), "v"(voff), "s"(rs), "s"(soff), "i"(OFF) : "memory");
  else if constexpr (SLOT >= 0) asm volatile("buffer_load_dwordx4 a[%c0:%c1], %2, %3, %4 offen offset:%5" :: "i"(4 * SLOT), "i"(4 * SLOT + 3), "v"(voff), "s"(rs), "s"(soff), "i"(OFF) : "memory");
  else if constexpr (SC1) asm volatile("buffer_load_dwordx4 %0, %1, %2, %3 offen offset:%4 sc1" : "=v"(d) : "v"(voff), "s"(rs), "s"(soff), "i"(OFF) : "memory");
  else asm volatile("buffer_load_dwordx4 %0, %1, %2, %3 offen offset:%4" : "=v"(d) : "v"(voff), "s"(rs), "s"(soff), "i"(OFF) : "memory");
  return ++vm.n;
}
template <int OFF> DI int ld8c(rsrc_t rs, VM& vm, u32x2& d, u32 voff, u32 soff) {
  asm volatile("buffer_load_dwordx2 %0, %1, %2, %3 offen offset:%4 sc1" : "=v"(d) : "v"(voff), "s"(rs), "s"(soff), "i"(OFF) : "memory");
  return ++vm.n;
}
template <bool FIRST, int SA, int SBB> DI void mma(f32x4& d, const u32x4& a, const u32x4& b) {
#define MMA_(D_, C_) \
  if constexpr (SA >= 0 && SBB >= 0) asm volatile("v_mfma_f32_16x16x32_bf16 %0, a[%c1:%c2], a[%c3:%c4], " C_ : D_(d) : "i"(4 * SA), "i"(4 * SA + 3), "i"(4 * SBB), "i"(4 * SBB + 3)); \
  else if constexpr (SA >= 0) asm volatile("v_mfma_f32_16x16x32_bf16 %0, a[%c1:%c2], %3, " C_ : D_(d) : "i"(4 * SA), "i"(4 * SA + 3), "v"(b)); \
  else if constexpr (SBB >= 0) asm volatile("v_mfma_f32_16x16x32_bf16 %0, %3, a[%c1:%c2], " C_ : D_(d) : "i"(4 * SBB), "i"(4 * SBB + 3), "v"(a)); \
  else asm volatile("v_mfma_f32_16x16x32_bf16 %0, %1, %2, " C_ : D_(d) : "v"(a), "v"(b));
  if constexpr (FIRST) { MMA_("=&v", "0") } else { MMA_("+v", "%0") }
#undef MMA_
}
DI void st16c(const Ctx& c, VM& vm, u32 voff, u32x4 v) { ++vm.n; __builtin_amdgcn_raw_buffer_store_b128(v, c.ra, voff, 0, 16); }
DI void st8c(const Ctx& c, VM& vm, u32 voff, u32x2 v) { ++vm.n; __builtin_amdgcn_raw_buffer_store_b64(v, c.ra, voff, 0, 16); }
DI void st4c(const Ctx& c, VM& vm, u32 voff, u32 v) { ++vm.n; __builtin_amdgcn_raw_buffer_store_b32(v, c.ra, voff, 0, 16); }
template <u32 LOFF, u32 SOFF> DI void glds_head(const Ctx& c, VM& vm, u32& cur, u32 m0b, u32 sb, u32 voff) {
  ++vm.n;
  asm volatile("s_add_u32 m0, %1, %2\n\ts_add_u32 %0, %3, %4\n\tbuffer_load_dwordx4 %5, %6, %0 offen sc1 lds"
               : "=&s"(cur) : "s"(m0b), "i"(LOFF), "s"(sb), "i"(SOFF), "v"(voff), "s"(c.ra) : "memory", "scc");
}
template <u32 LOFF, u32 SOFF> DI int glds_raw(rsrc_t rs, VM& vm, u32 m0b, u32 sb, u32 voff) {
  u32 cur;
  asm volatile("s_add_u32 m0, %1, %2\n\ts_add_u32 %0, %3, %4\n\tbuffer_load_dwordx4 %5, %6, %0 offen lds"
               : "=&s"(cur) : "s"(m0b), "i"(LOFF), "s"(sb), "i"(SOFF), "v"(voff), "s"(rs) : "memory", "scc");
  return ++vm.n;
}
template <int OFF> DI void glds_next(const Ctx& c, VM& vm, u32 cur, u32 voff) {
  ++vm.n;
  asm volatile("buffer_load_dwordx4 %0, %1, %2 offen offset:%3 sc1 lds" :: "v"(voff), "s"(c.ra), "s"(cur), "i"(OFF) : "memory");
}
#define BARL() asm volatile("s_waitcnt lgkmcnt(0)\n\ts_barrier" ::: "memory")
#define BAR0() asm volatile("s_barrier" ::: "memory")
extern "C" __device__ void carry_count_too_large() __attribute__((error("carry count too large")));

#define STAMP(k) do {} while (0)
#define TS(t, k) STAMP(1 + ((10 * l + (t)) * 5 + (k)))

struct Poll { u32 off, need; int mark, mark2; };
DI void poll_issue(const Ctx& c, VM& vm, Poll& p, u32 off, u32 need) {
  p.off = off; p.need = need;
  asm volatile("buffer_load_dword a252, %0, %1, 0 offen sc1" :: "v"(p.off), "s"(c.rs) : "memory");
  p.mark = ++vm.n;
}
DI void poll_again(const Ctx& c, VM& vm, Poll& p) {
  asm volatile("buffer_load_dword a253, %0, %1, 0 offen sc1" :: "v"(p.off), "s"(c.rs) : "memory");
  p.mark2 = ++vm.n;
}
template <bool TWO> DI void poll_check(const Ctx& c, VM& vm, Poll& p) {
  need(vm, p.mark);
  u32 v; asm volatile("v_accvgpr_read_b32 %0, a252" : "=v"(v));
  if (__builtin_expect(!__all(v - p.need < 32u), 0)) {
    asm volatile("buffer_load_dword a254, %1, %2, 0 offen sc1\n\ts_waitcnt vmcnt(%3) ; SECOND\n\tv_accvgpr_read_b32 %0, a253" : "=v"(v) : "v"(p.off), "s"(c.rs), "i"(vm.n - p.mark2 + 1) : "memory");
    bool ok = __all(v - p.need < 32u);
#pragma unroll 1
    for (u32 spins = 0; !ok; ++spins) {
      asm volatile("buffer_load_dword a255, %1, %2, 0 offen sc1\n\ts_waitcnt vmcnt(1)\n\tv_accvgpr_read_b32 %0, a254" : "=v"(v) : "v"(p.off), "s"(c.rs) : "memory");
      if (__all(v - p.need < 32u)) break;
      asm volatile("buffer_load_dword a254, %1, %2, 0 offen sc1\n\ts_waitcnt vmcnt(1)\n\tv_accvgpr_read_b32 %0, a255" : "=v"(v) : "v"(p.off), "s"(c.rs) : "memory");
      ok = __all(v - p.need < 32u);
      if (spins > (1u << 16)) { __hip_atomic_store((GAS u32*)(c.ws + OFF_TMO), 1u, RLX_AGENT); break; }
    }
  }
  asm volatile("" ::: "memory");
}
DI void flag_store(const Ctx& c, VM& vm, u32 val) { ++vm.n; __builtin_amdgcn_raw_buffer_store_b32(val, c.rs, c.foff, 0, 16); }
DI void publish(const Ctx& c, VM& vm, int mark, u32 val) { need(vm, mark); BAR0(); flag_store(c, vm, val); }
DI u32 lo16(u32 v) { return vop(v) & 0xFFFF; }
DI u32 hi16(u32 v) { return vop(v) >> 16; }

template <int ID> constexpr int slotB(int j) { return ID == 0 ? (j < 10 ? -1 : 15 + j) : ID == 1 ? j : ID == 2 ? (j < 38 ? 25 + j : -1) : j; }
template <int ID_, int N> struct BF { static constexpr int ID = ID_; u32x4 f[N]; int m[N]; u32 base, cur; };
template <int J, int ID, int N> DI void ldB(const Ctx& c, VM& vm, BF<ID, N>& B) {
  if constexpr ((J & 3) == 0) B.cur = sop(B.base) + J * KB;
  B.m[J] = ld16<slotB<ID>(J), true, (J & 3) * 1024>(c.rs, vm, B.f[J], c.l16, B.cur);
}
template <int LO, int HI, class BT> DI void loadB(const Ctx& c, VM& vm, BT& B) { sfor<LO, HI>(LAMBDA(J) { ldB<J.v>(c, vm, B); }); }
template <int CTRL> DI float dpp_add(float v) { return v + __int_as_float(__builtin_amdgcn_update_dpp(0, __float_as_int(v), CTRL, 0xF, 0xF, true)); }

#define STAT0 4
template <int HALF> constexpr u32 img_off(int g) { return HALF == 0 ? g * 16 * KB : (g < 4 ? L_Y + g * 16 * KB : 0); }
DI u32 raw_lane(const Ctx& c) { const u32 l = c.l16 >> 4; return (l & 15) * 128 + (((l >> 4) ^ l) & 7) * 16; }
struct Stage { u32x4 sv; u32 src, cur; int mg[5]; };
template <int HALF, int K> DI void stage(const Ctx& c, VM& vm, Stage& st, u32 xoff, u32 tf) {
  if constexpr (K == 0) {
    const u32 t = sop(tf), l = vop(c.l16);
    st.src = xoff + HALF * 64 * 20480 + t * 20480;
    ld16<-1, true, 0>(c.ra, vm, st.sv, ((l << 2) & 0x3C0) + ((l >> 4) & 0x30), xoff + (A_SA - A_XA) + HALF * 65536 + t * 1024);
  } else {
    constexpr int g = (K - 1) >> 2, j = (K - 1) & 3;
    if constexpr (j == 0) glds_head<img_off<HALF>(g), g * 4 * KB>(c, vm, st.cur, c.m0x, st.src, c.l16); else glds_next<j * 1024>(c, vm, st.cur, c.l16);
    st.mg[g] = vm.n;
  }
}
template <int HALF, int LO, int HI> DI void stages(const Ctx& c, VM& vm, Stage& st, u32 xoff, u32 tf) { sfor<LO, HI>(LAMBDA(K) { stage<HALF, K.v>(c, vm, st, xoff, tf); }); }
template <int HALF, int NF, int SYNC, int PG, bool STATS, class BT, class HL, class HA> DI void gemm_lds(const Ctx& c, VM& vm, Stage& st, int mst, u32 fv, f32x4 (&acc)[NF][4], BT& B, HL hl, HA ha) {
  const u32 lo = STATS ? c.l16 : raw_lane(c);
  const u32 b0 = vop(c.lb + img_off<HALF>(0) + lo), b1 = vop(c.lb + img_off<HALF>(4) + lo);
  const u32 b2 = STATS ? b0 : vop(b0 ^ 64), b3 = STATS ? b1 : vop(b1 ^ 64);
  auto rd = [&](int s, int tf) __attribute__((always_inline)) { return *LP(const u32x4, (s < 16 ? ((s & 1) ? b2 : b0) + (s >> 2) * 16 * KB : ((s & 1) ? b3 : b1)) + tf * 4 * KB + (s & (STATS ? 3 : 2)) * KB); };
  prof<true>(vm);
  auto cover = [](int g) constexpr { int e = g; while (e < 4 && !((SYNC >> (e + 1)) & 1)) ++e; return e; };
  need(vm, st.mg[cover(0)]); BAR0(); if (STATS) pin(st.sv);
  u32x4 a[2][4]; float s1, s2;
#pragma unroll
  for (int tf = 0; tf < 4; ++tf) a[0][tf] = rd(0, tf);
  SB();
  sfor<0, 20>(LAMBDA(S) {
    constexpr int s = S.v;
    if constexpr ((s & 3) == 3 && s < 19 && ((SYNC >> ((s + 1) >> 2)) & 1)) {
      need(vm, st.mg[cover((s + 1) >> 2)]); if ((s + 1) >> 2 == PG) need(vm, mst);
      BAR0();
    }
    need(vm, B.m[s * NF + NF - 1]);
    __builtin_amdgcn_s_waitcnt(0xC07F);
    prof<false>(vm);
    SB();
    sfor<0, 4 * NF>(LAMBDA(J) {
      constexpr int nf = J.v >> 2, tf = J.v & 3;
      mma<s == 0, slotB<BT::ID>(s * NF + nf), -1>(acc[nf][tf], B.f[s * NF + nf], a[s & 1][tf]);
      if constexpr (s < 19 && nf == 0) { a[(s + 1) & 1][tf] = rd(s + 1, tf); SB(); }
      if constexpr (J.v >= 4 && (J.v - 4) % NF == 0) { SB(); hl(IC<s * NF + (J.v - 4) / NF>{}); SB(); }
      if constexpr (J.v == 0 && s == 4 * PG - 1) { flag_store(c, vm, fv); SB(); }
      if constexpr (J.v == 5) { SB(); ha(S); SB(); }
      if constexpr (STATS && J.v == 7 - (NF == 3) && s >= STAT0 && s < STAT0 + 6) {
        SB();
        if constexpr (s == STAT0) { s1 = __uint_as_float(st.sv.x) + __uint_as_float(st.sv.z); s2 = __uint_as_float(st.sv.y) + __uint_as_float(st.sv.w); pin(s1); pin(s2); }
        if constexpr (s == STAT0 + 1) { auto t = __builtin_amdgcn_permlane16_swap(__float_as_uint(s1), __float_as_uint(s2), false, false); s1 = __uint_as_float(t[0]) + __uint_as_float(t[1]); s2 = s1; pin(s1); pin(s2); }
        if constexpr (s == STAT0 + 2) { auto t = __builtin_amdgcn_permlane32_swap(__float_as_uint(s1), __float_as_uint(s2), false, false); s1 = __uint_as_float(t[0]) + __uint_as_float(t[1]); s2 = s1; pin(s1); pin(s2); }
        if constexpr (s == STAT0 + 3) { auto t = __builtin_amdgcn_permlane16_swap(__float_as_uint(s1), __float_as_uint(s2), false, false); s1 = __uint_as_float(t[0]) * (-1.0f / D); s2 = __uint_as_float(t[1]) * (1.0f / D); pin(s1); pin(s2); }
        if constexpr (s == STAT0 + 4) { s2 = __builtin_fmaxf(s2 - s1 * s1, 0.f) + 1e-5f; pin(s2); }
        if constexpr (s == STAT0 + 5) { float rstd = __builtin_amdgcn_rsqf(s2);
          *LP(f32x2, vop(c.es) + c.w * 128 + HALF * 512) = f32x2{s1, rstd}; }
        SB();
      }
    });
    SB();
  });
  asm volatile("s_nop 7");
  prof<true>(vm);
  SB();
}
#define JPUB 15
#define JR1 5
#define JR2 9
struct Raw { rsrc_t rx, rw; u32 vx, sx, vw, sw, ring, ra; int m[20]; };
template <int HALF, int S> DI void raw_x(const Ctx& c, VM& vm, Raw& r, Stage& st) { st.mg[S >> 2] = glds_raw<img_off<HALF>(S >> 2) + (S & 3) * KB, HALF * 1024 * 1280 + (S >> 1) * 128 + (S & 1) * 8 * 1280>(r.rx, vm, c.m0x, r.sx, r.vx); }
template <int K> DI void raw_w(VM& vm, Raw& r) { r.m[K >> 1] = glds_raw<((K >> 1) % 6) * 2 * KB + (K & 1) * KB, K * 16 * 3840>(r.rw, vm, r.ring, r.sw, r.vw); }
template <int J, int H, class BT> DI void raw_rd(Raw& r, BT& B, u32x2 (&t)[2][2]) {
  constexpr int off = ((J >> 1) % 6) * 2048 + (J & 1) * 8 + H * 256, sl = slotB<0>(J);
  if constexpr (sl >= 0) asm volatile("ds_read_b64_tr_b16 a[%c0:%c1], %2 offset:%3" :: "i"(4 * sl + 2 * H), "i"(4 * sl + 2 * H + 1), "v"(r.ra), "i"(off) : "memory");
  else { t[J & 1][H] = trread(r.ra + off); if constexpr (H == 1) B.f[J] = u32x4{t[J & 1][0].x, t[J & 1][0].y, t[J & 1][1].x, t[J & 1][1].y}; }
}
template <class BT, class HL, class HA> DI void gemm_q0(const Ctx& c, VM& vm, Stage& st, Raw& r, int mst0, int& mst, u32 fv, f32x4 (&acc)[2][4], f32x4 (&cs)[2], BT& B, HL hl, HA ha) {
  const u32 lo = raw_lane(c), b0 = vop(c.lb + lo), b1 = vop(c.lb + img_off<0>(4) + lo), b2 = vop(b0 ^ 64), b3 = vop(b1 ^ 64);
  auto rd = [&](int s, int tf) __attribute__((always_inline)) { return *LP(const u32x4, (s < 16 ? ((s & 1) ? b2 : b0) + (s >> 2) * 16 * KB : ((s & 1) ? b3 : b1)) + tf * 4 * KB + (s & 2) * KB); };
  u32x4 ones = {0x3F803F80u, 0x3F803F80u, 0x3F803F80u, 0x3F803F80u}; pin(ones);
  u32x4 a[2][4]; u32x2 t[2][2];
  need(vm, st.mg[0] > r.m[0] ? st.mg[0] : r.m[0]); BAR0();
#pragma unroll
  for (int tf = 0; tf < 4; ++tf) a[0][tf] = rd(0, tf);
  sfor<0, 4>(LAMBDA(K) { raw_rd<(K.v >> 1), (K.v & 1)>(r, B, t); });
  SB();
  sfor<0, 20>(LAMBDA(S) {
    constexpr int s = S.v;
    if constexpr ((s & 3) == 3 && s < 19) { need(vm, st.mg[(s + 1) >> 2]); if (s == 3) need(vm, mst0); if (s == JPUB) need(vm, mst); BAR0(); }
    if constexpr (s < 19) need(vm, r.m[s + 1]);
    asm volatile("s_waitcnt lgkmcnt(0)" ::: "memory"); __builtin_amdgcn_s_waitcnt(0xC07F);
    prof<false>(vm);
    SB();
    sfor<0, 10>(LAMBDA(J) {
      constexpr int nf = J.v < 8 ? J.v >> 2 : J.v - 8, tf = J.v & 3;
      if constexpr (J.v < 8) mma<s == 0, slotB<0>(s * 2 + nf), -1>(acc[nf][tf], B.f[s * 2 + nf], a[s & 1][tf]);
      else mma<s == 0, slotB<0>(s * 2 + nf), -1>(cs[nf], B.f[s * 2 + nf], ones);
      if constexpr (J.v == 0 && s == 3) flag_store(c, vm, fv - 1);
      if constexpr (J.v == 0 && s == JPUB) flag_store(c, vm, fv);
      if constexpr (s < 19 && J.v < 4) a[(s + 1) & 1][tf] = rd(s + 1, tf);
      if constexpr (s < 19 && J.v >= 4 && J.v < 8) raw_rd<2 * (s + 1) + (tf >> 1), (tf & 1)>(r, B, t);
      SB();
      if constexpr (J.v == 1 || J.v == 5 || J.v == 8) { hl(IC<s * 3 + (J.v >> 2)>{}); SB(); }
      if constexpr (J.v == 9) { ha(S); SB(); }
    });
  });
  asm volatile("s_nop 7");
  SB();
}
struct EpQ { f32x4 cs[2]; f32x2 st[4]; u32 vo; };
struct EpG { f32x4 cs[3]; f32x2 st[4]; u32 vo; };
template <int HALF, int PART> DI void pre_q(const Ctx& c, EpQ& e) {
  if constexpr (PART == 0) {
    const u32 pc = c.lb + L_CS + c.w * 128 + ((vop(c.l16) >> 3) & 0x60);
    e.cs[0] = *LP(const f32x4, pc); e.cs[1] = *LP(const f32x4, pc + 16);
  } else {
    const u32 ps = vop(c.es);
#pragma unroll
    for (int tf = 0; tf < 4; ++tf) e.st[tf] = *LP(const f32x2, ps + HALF * 512 + tf * 128);
    e.vo = vop(c.eq) + HALF * 1024 * 16;
  }
}
DI void epi_q(const Ctx& c, VM& vm, const EpQ& e, const f32x4 (&acc)[2][4]) {
#pragma unroll
  for (int tf = 0; tf < 4; ++tf) {
    const float rs = e.st[tf].y * c.qs;
    const f32x4 a = (acc[0][tf] + e.st[tf].x * e.cs[0]) * rs, b = (acc[1][tf] + e.st[tf].x * e.cs[1]) * rs;
    st16c(c, vm, e.vo + tf * 256, u32x4{pk2(a.x, a.y), pk2(a.z, a.w), pk2(b.x, b.y), pk2(b.z, b.w)});
  }
}
template <int HALF, int PART> DI void pre_g(const Ctx& c, EpG& e) {
  if constexpr (PART == 0) {
    const u32 pc = c.lb + L_CS + 512 + c.w * 192 + ((vop(c.l16) >> 4) & 0x30);
#pragma unroll
    for (int nf = 0; nf < 3; ++nf) e.cs[nf] = *LP(const f32x4, pc + nf * 64);
  } else {
    const u32 ps = vop(c.es);
#pragma unroll
    for (int tf = 0; tf < 4; ++tf) e.st[tf] = *LP(const f32x2, ps + HALF * 512 + tf * 128);
    e.vo = vop(c.eg) + HALF * 64 * 192 * 256;
  }
}
DI void epi_g(const Ctx& c, VM& vm, const EpG& e, const f32x4 (&acc)[3][4]) {
#pragma unroll
  for (int nf = 0; nf < 3; ++nf) {
    u32 R[4];
#pragma unroll
    for (int tf = 0; tf < 4; ++tf) { const f32x4 a = acc[nf][tf] + e.st[tf].x * e.cs[nf]; const float r2 = e.st[tf].y * e.st[tf].y; R[tf] = pk2(a.x * r2 * a.z, a.y * r2 * a.w); }
    { auto t = __builtin_amdgcn_permlane32_swap(R[0], R[2], false, false); R[0] = t[0]; R[2] = t[1]; }
    { auto t = __builtin_amdgcn_permlane32_swap(R[1], R[3], false, false); R[1] = t[0]; R[3] = t[1]; }
    { auto t = __builtin_amdgcn_permlane16_swap(R[0], R[1], false, false); R[0] = t[0]; R[1] = t[1]; }
    { auto t = __builtin_amdgcn_permlane16_swap(R[2], R[3], false, false); R[2] = t[0]; R[3] = t[1]; }
    st16c(c, vm, e.vo + nf * 256, u32x4{R[0], R[1], R[2], R[3]});
  }
}

struct AW { u32x4 f[2][5]; u32 s0; int m[2][5]; static constexpr int slot(int, int) { return -1; } };
template <bool X> struct AD { u32x4 f[2][12]; u32 cur; int m[2][12]; static constexpr int slot(int tf, int s) { return X && tf == 1 && s >= 9 ? 51 + s : -1; } };
template <int HALF, int K> DI void ld_w(const Ctx& c, VM& vm, AW& a) {
  constexpr int tf = K / 5, j = K % 5;
  if constexpr (j == 0 || j == 4) a.s0 = sop(c.bof) + (HALF * 64 + tf) * 20480 + j * KB;
  a.m[tf][j] = ld16<-1, true, (j & 3) * 1024>(c.ra, vm, a.f[tf][j], c.l16, a.s0);
}
template <int NS, int HALF> constexpr int kord(int p) { return NS == 12 && HALF == 1 ? (p + 5) % 12 : p; }
template <int HALF, int K, bool X> DI void ld_d(const Ctx& c, VM& vm, AD<X>& a) {
  constexpr int tf = K / 12, s = kord<12, HALF>(K % 12);
  if constexpr (K % 12 == 0 || (s & 3) == 0) a.cur = sop(c.bhf) + (HALF * 64 + tf) * 192 * 256 + (s & ~3) * KB;
  a.m[tf][s] = ld16<AD<X>::slot(tf, s), true, (s & 3) * 1024>(c.ra, vm, a.f[tf][s], c.l16, a.cur);
}
template <int TF, int S> DI u32x4 get_a(VM& vm, AW& a) { need(vm, a.m[TF][S]); return a.f[TF][S]; }
template <int TF, int S, bool X> DI u32x4 get_a(VM& vm, AD<X>& a) { need(vm, a.m[TF][S]); if constexpr (AD<X>::slot(TF, S) < 0) return a.f[TF][S]; else return u32x4{}; }
template <int HALF, int NS, class BT, class AT, class HL, class HA> DI void gemm_reg(const Ctx& c, VM& vm, BT& B, AT& a, HL hl, HA ha) {
  f32x4 acc[10];
  const u32 p0 = vop(c.rw) + (HALF ? L_HY - L_HX : 0), p1 = p0 ^ 16;
  auto wr = [&](int j) __attribute__((always_inline)) { *LP(f32x4, (((j % 5) & 1) ? p1 : p0) + j * KB) = acc[j]; SB(); };
  sfor<0, 2 * NS>(LAMBDA(P) {
    constexpr int tf = P.v / NS, s = kord<NS, HALF>(P.v % NS);
    if constexpr ((P.v & 1) == 0) prof<true, false>(vm);
    need(vm, B.m[s * 5 + 4]);
    const u32x4 o = get_a<tf, s>(vm, a);
    SB();
    sfor<0, 5>(LAMBDA(J) { constexpr int nf = J.v, g = P.v * 5 + nf;
      mma<P.v % NS == 0, slotB<BT::ID>(s * 5 + nf), AT::slot(tf, s)>(acc[tf * 5 + nf], B.f[s * 5 + nf], o);
      if constexpr (g % 4 == 1) { SB(); hl(IC<g / 4>{}); SB(); }
      if constexpr (g % 10 == 4) { SB(); ha(IC<g / 10>{}); SB(); }
      if constexpr (g >= NS * 5 + 3 && g < NS * 5 + 23 && (g - NS * 5 - 3) % 4 == 0) { SB(); wr((g - NS * 5 - 3) / 4); } });
    SB();
  });
  prof<true>(vm);
  asm volatile("s_nop 3"); SB();
  sfor<5, 10>(LAMBDA(J) { wr(J.v); });
  prof<true>(vm);
}
struct XR { u32x4 m; u32 t; };
struct Red { f32x4 a, b; f32x2 t; u32 pa, pb, pt, vo, vt, vs; };
template <int HALF> DI void red_pre(const Ctx& c, Red& r, const XR& xr, u32 xoff) {
  r.a = f32x4{bf_lo(xr.m.x), bf_hi(xr.m.x), bf_lo(xr.m.y), bf_hi(xr.m.y)}; r.b = f32x4{bf_lo(xr.m.z), bf_hi(xr.m.z), bf_lo(xr.m.w), bf_hi(xr.m.w)}; r.t = f32x2{bf_lo(xr.t), bf_hi(xr.t)};
  r.pa = vop(c.rpa) + (HALF ? L_HY - L_HX : 0); r.pb = r.pa ^ 16; r.pt = vop(c.rpt) + (HALF ? L_HY - L_HX : 0);
  r.vo = vop(c.rvo) + (xoff + HALF * 64 * 20480); r.vt = vop(c.rvt) + (xoff + HALF * 64 * 20480); r.vs = vop(c.rvs) + (xoff + HALF * 65536);
  pin(r.a); pin(r.b); pin(r.t); pin(r.pa); pin(r.pb); pin(r.pt); pin(r.vo); pin(r.vt); pin(r.vs);
}
template <int HALF> DI void red_out(const Ctx& c, VM& vm, Red& r, XR& xr, u16* out) {
  prof<true>(vm);
  f32x4 pa[4], pb[4]; f32x2 pt[4];
#pragma unroll
  for (int ww = 0; ww < 4; ++ww) { pa[ww] = *LP(const f32x4, r.pa + ww * 10 * KB); pb[ww] = *LP(const f32x4, r.pb + ww * 10 * KB); }
#pragma unroll
  for (int ww = 0; ww < 4; ++ww) pt[ww] = *LP(const f32x2, r.pt + ww * 10 * KB);
  SB();
  const f32x4 a = ((r.a + pa[0]) + pa[1]) + (pa[2] + pa[3]), b = ((r.b + pb[0]) + pb[1]) + (pb[2] + pb[3]);
  xr.m = u32x4{pk2(a.x, a.y), pk2(a.z, a.w), pk2(b.x, b.y), pk2(b.z, b.w)};
  st16c(c, vm, r.vo, xr.m);
  const f32x2 t = ((r.t + pt[0]) + pt[1]) + (pt[2] + pt[3]);
  xr.t = pk2(t.x, t.y);
  st4c(c, vm, r.vt, xr.t);
  const f32x2 sp = (a.xy + a.zw) + (b.xy + b.zw) + t;
  const f32x2 qp = a.xy * a.xy + (a.zw * a.zw + (b.xy * b.xy + (b.zw * b.zw + t * t)));
  float s1 = sp.x + sp.y, s2 = qp.x + qp.y;
  s1 = dpp_add<0xB1>(s1); s2 = dpp_add<0xB1>(s2); s1 = dpp_add<0x4E>(s1); s2 = dpp_add<0x4E>(s2); s1 = dpp_add<0x141>(s1); s2 = dpp_add<0x141>(s2);
  st8c(c, vm, r.vs, u32x2{__float_as_uint(s1), __float_as_uint(s2)});
  if (out) {
    const u32 lane = vop(c.l16) >> 4;
    GAS u16* o = (GAS u16*)out + (c.sw + HALF * 1024 + 8 * c.w + (lane >> 3)) * D + 80 * ((c.bid >> 3) & 7);
    *(GAS u32x4*)(o + 8 * (lane & 7)) = xr.m; *(GAS u32*)(o + 64 + 2 * (lane & 7)) = xr.t;
  }
  prof<true>(vm);
}

struct AIn { u32x4 Q0; u32x2 qc; u32 cur; int r0, mark; };
DI u32 wg_qg(u32 RG, u32 cg) { return 2 * (RG >> 2) + (cg >> 3) + 8 * ((RG & 3) * 8 + (cg & 7)); }
DI u32 a_head(u32 bid) { return 4 * (bid & 7) + ((bid >> 3) & 3); }
DI u32 a_dep(const Ctx& c, int half) {
  const int h = a_head(c.bid), gp = half * 8 + (c.bid >> 5), clo = 5 * (h >> 1) + 2 * (h & 1), lane = c.l16 >> 4;
  const int j = lane < 36 ? lane : 0, sec = j / 12, lh = (j / 6) & 1, pi = (j >> 1) % 3, rg = j & 1;
  int g2 = gp - (sec == 0 ? 0 : pi); if (g2 < 0) g2 = gp;
  return (wg_qg(2 * (g2 & 7) + rg, (80 * sec + clo + 2 * lh) >> 4) << 7) | (g2 >> 3);
}
DI void a_poll(const Ctx& c, VM& vm, Poll& p, u32 fl, u32 dep) { poll_issue(c, vm, p, dep & ~1u, fl + 1 + (dep & 1)); }
template <int HALF, int K> DI void a_stage(const Ctx& c, VM& vm, AIn& in) {
  if constexpr (K == 0) {
    in.r0 = (HALF * 8 + (sop(c.bid) >> 5)) * 128;
    in.cur = A_QKV - 80 * 32768 + (in.r0 + 32 * c.w) * 16;
    ld16<-1, true, 0>(c.ra, vm, in.Q0, c.av0, in.cur);
  } else if constexpr (K == 1) {
    ld8c<1024>(c.ra, vm, in.qc, c.av1, in.cur + (c.bid & 8));
    const u32 po = c.m0a + (HALF ? L_HY : L_HX) + 3072 + (vop(c.l16) >> 1);
#pragma unroll
    for (int u = 0; u < 3; ++u) *LP(u32x2, po + u * TP) = u32x2{0x3F803F80u, 0x3F803F80u};
  } else {
    constexpr int u = (K - 2) / 3, j = (K - 2) % 3;
    if constexpr (j == 0) {
      int krow = in.r0 - 256 + 96 * (int)c.w + 32 * u; if (HALF == 0 && krow < 0) krow = in.r0;
      glds_head<(HALF ? L_HY : L_HX) + u * TP, A_QKV>(c, vm, in.cur, c.m0a, krow * 16, c.av0);
    } else glds_next<j * 1024>(c, vm, in.cur, j == 1 ? c.av1 : c.av2);
    in.mark = vm.n;
  }
}
DI float mx3(float a, float b, float c) { float r; asm("v_max3_f32 %0, %1, %2, %3" : "=v"(r) : "v"(a), "v"(b), "v"(c)); return r; }
DI void smfma0(f32x16& d, u32x4 a, u32x4 b) { asm volatile("v_mfma_f32_32x32x16_bf16 %0, %1, %2, 0" : "=&v"(d) : "v"(a), "v"(b)); }
DI void smfmac(f32x16& d, u32x4 a, u32x4 b, const f32x16& cc) { asm volatile("v_mfma_f32_32x32x16_bf16 %0, %1, %2, %3" : "=&v"(d) : "v"(a), "v"(b), "v"(cc)); }
DI void smfma1(f32x16& d, u32x2 a, u32x2 b) { asm volatile("v_mfma_f32_32x32x8_bf16 %0, %1, %2, %0" : "+v"(d) : "v"(a), "v"(b)); }
DI void omfma(f32x16& d, u32x4 a, u32x4 b) { asm volatile("v_mfma_f32_32x32x16_bf16 %0, %1, %2, %0" : "+v"(d) : "v"(a), "v"(b)); }
#define MX3(a, b, c) __builtin_fmaxf(__builtin_fmaxf((a), (b)), (c))
template <int HALF, class HL, class HA> DI void attn(const Ctx& c, VM& vm, AIn& in, HL hl, HA ha) {
  const u32 l = vop(c.l16); const bool hh = l >= 512;
  const int q0 = (HALF * 8 + (sop(c.bid) >> 5)) * 128 + 32 * c.w;
  const u32 img = c.lb + (HALF ? L_HY : L_HX) + c.w * TP;
  const u32 kp0 = vop(img + l), kp1 = vop(img + c.ak1), mp = vop(c.lb + L_MASK + l);
  const u32 vlo = vop(img + c.avl), vhi = vop(vlo ^ 128);
  const int t0 = (256 - q0) >> 5;
  auto vfrag = [&](int t, int s) __attribute__((always_inline)) { const u32x2 lo = trread(vlo + t * TP + s * 256), hi = trread(vhi + t * TP + s * 256); return u32x4{lo.x, lo.y, hi.x, hi.y}; };
  u32x4 sel[4];
#pragma unroll
  for (int j = 0; j < 4; ++j) sel[j] = *LP(const u32x4, mp + j * KB);
  const u32x4 mkA = *LP(const u32x4, mp + 4 * KB), mkB = *LP(const u32x4, mp + 5 * KB);
  prof<true>(vm);
  need(vm, in.mark); BARL(); pin(in.Q0); pin(in.qc);
  prof<true>(vm);
  u32x4 K0[9]; u32x2 K1[9];
  auto kread = [&](int t) __attribute__((always_inline)) { K0[t] = *LP(const u32x4, kp0 + t * TP); K1[t] = *LP(const u32x2, kp1 + t * TP); };
  auto kfix = [&](int t) __attribute__((always_inline)) { if (HALF == 0) { K1[t].x = hh ? (t < t0 ? 0xF149u : 0u) : K1[t].x; pin(K1[t]); } };
  kread(0); kread(8);
  u32x4 Q0 = in.Q0; u32x2 Q1 = hh ? u32x2{HALF == 0 ? 0x3F80u : 0u, 0} : in.qc;
  pin(Q1);
  SB(); kread(1); kread(2); SB();
  f32x16 S[2], M;
  kfix(0); kfix(8);
  SB(); asm volatile("s_nop 0"); smfma0(M, K0[0], Q0);
  SB(); kread(3); kread(4); SB();
  smfma1(M, K1[0], Q1);
  SB(); hl(IC<0>{}); kread(5); SB();
  smfma0(S[0], K0[8], Q0);
  SB(); kread(6); kread(7); SB();
  smfma1(S[0], K1[8], Q1);
  SB(); hl(IC<1>{});
  const u32x4 V0a = vfrag(0, 0), V8a = vfrag(8, 0), V0b = vfrag(0, 1), V8b = vfrag(8, 1);
  SB(); hl(IC<2>{});
#pragma unroll
  for (int t = 1; t < 8; ++t) kfix(t);
  asm volatile("s_nop 1" : "+v"(M), "+v"(S[0]));
  prof<true>(vm);
  SB();
#pragma unroll
  for (int g = 0; g < 16; ++g) M[g] = __uint_as_float((sel[g >> 2][g & 3] & __float_as_uint(S[0][g])) | (~sel[g >> 2][g & 3] & __float_as_uint(M[g])));
  float m = mx3(mx3(mx3(M[0], M[1], M[2]), mx3(M[3], M[4], M[5]), mx3(M[6], M[7], M[8])), mx3(mx3(M[9], M[10], M[11]), mx3(M[12], M[13], M[14]), M[15]), M[15]);
  { auto t = __builtin_amdgcn_permlane32_swap(__float_as_uint(m), __float_as_uint(m), false, false); m = mx3(__uint_as_float(t[0]), __uint_as_float(t[1]), __uint_as_float(t[1])); }
  f32x16 negm, O;
#pragma unroll
  for (int g = 0; g < 16; ++g) negm[g] = -m;
  pin(negm);
  prof<true>(vm);
  SB(); ha(IC<0>{}); hl(IC<3>{});
  prof<true>(vm);
  SB();
#define EM(i) __builtin_amdgcn_exp2f(M[i] - m)
  smfmac(S[1], K0[1], Q0, negm);
  u32x4 P; P.x = vop(pk2(EM(0), EM(1))); P.y = vop(pk2(EM(2), EM(3)));
  SB(); smfma1(S[1], K1[1], Q1);
  P.z = vop(pk2(EM(4), EM(5))); P.w = vop(pk2(EM(6), EM(7)));
  u32x4 A8 = P & mkA, A0 = P & ~mkA; pin(A8); pin(A0);
  P.x = vop(pk2(EM(8), EM(9)));
  SB(); hl(IC<4>{}); SB(); smfma0(O, V0a, A0);
  P.y = vop(pk2(EM(10), EM(11))); P.z = vop(pk2(EM(12), EM(13)));
  SB(); hl(IC<5>{}); ha(IC<9>{}); SB(); omfma(O, V8a, A8);
  P.w = vop(pk2(EM(14), EM(15)));
  u32x4 Pp = P & mkB, B0 = P & ~mkB, Vp = V8b; pin(Pp); pin(B0);
  SB(); hl(IC<6>{}); SB();
#define EX(i) __builtin_amdgcn_exp2f(S[t & 1][i])
  sfor<1, 8>(LAMBDA(T) {
    constexpr int t = T.v;
    prof<true>(vm);
    const u32x4 Va = vfrag(t, 0), Vb = vfrag(t, 1);
    u32x4 Pa, Pb;
    SB();
    if constexpr (t == 1) omfma(O, V0b, B0); else omfma(O, Vp, Pp);
    Pa.x = vop(pk2(EX(0), EX(1))); Pa.y = vop(pk2(EX(2), EX(3)));
    SB(); hl(IC<4 + 3 * t>{}); SB();
    if constexpr (t < 7) smfmac(S[(t + 1) & 1], K0[t + 1], Q0, negm);
    Pa.z = vop(pk2(EX(4), EX(5))); Pa.w = vop(pk2(EX(6), EX(7)));
    SB(); hl(IC<5 + 3 * t>{}); SB();
    if constexpr (t < 7) smfma1(S[(t + 1) & 1], K1[t + 1], Q1);
    Pb.x = vop(pk2(EX(8), EX(9))); Pb.y = vop(pk2(EX(10), EX(11)));
    SB();
    if constexpr (t == 1) { omfma(O, Vp, Pp); SB(); }
    ha(T);
    SB(); hl(IC<6 + 3 * t>{});
    __builtin_amdgcn_s_waitcnt(0xC07F);
    SB();
    omfma(O, Va, Pa);
    Pb.z = vop(pk2(EX(12), EX(13))); Pb.w = vop(pk2(EX(14), EX(15)));
    Pp = Pb; Vp = Vb;
    SB();
  });
  const u32 vo = vop(c.aob) + HALF * 64 * 20480, vt = hh ? OOR : vo + ((c.bid & 8) ? 8 - 256 : 512);
  asm volatile("s_nop 1" :: "v"(negm));
  omfma(O, Vp, Pp);
  prof<true>(vm);
  ha(IC<8>{});
  asm volatile("s_nop 11" : "+v"(O) : "v"(vo), "v"(vt));
  if (__builtin_expect(!__all(O[12] < 1.0995116e12f), 0)) {
    f32x16 X;
    auto score = [&](int t) __attribute__((always_inline)) {
      u32x4 k0 = *LP(const u32x4, kp0 + t * TP); u32x2 k1 = *LP(const u32x2, kp1 + t * TP);
      if (HALF == 0) k1.x = hh ? (t < t0 ? 0xF149u : 0u) : k1.x;
      asm volatile("s_nop 1\n\tv_mfma_f32_32x32x16_bf16 %0, %1, %2, 0\n\tv_mfma_f32_32x32x8_bf16 %0, %3, %4, %0\n\ts_nop 11" : "=&v"(X) : "v"(k0), "v"(Q0), "v"(k1), "v"(Q1));
      const u32 x0 = t == 0 ? ~0u : 0u, x1 = (t & 7) ? ~0u : 0u;
#pragma unroll
      for (int j = 0; j < 4; ++j) { const u32x4 ok = (*LP(const u32x4, mp + j * KB) ^ x0) | x1;
#pragma unroll
        for (int e = 0; e < 4; ++e) X[4 * j + e] = __uint_as_float((ok[e] & __float_as_uint(X[4 * j + e])) | (~ok[e] & 0xFF800000u)); }
    };
    float mt = -INFINITY;
#pragma unroll 1
    for (int t = 0; t < 9; ++t) { score(t);
#pragma unroll
      for (int g = 0; g < 16; ++g) mt = __builtin_fmaxf(mt, X[g]); }
    { auto t = __builtin_amdgcn_permlane32_swap(__float_as_uint(mt), __float_as_uint(mt), false, false); mt = __builtin_fmaxf(__uint_as_float(t[0]), __uint_as_float(t[1])); }
#pragma unroll
    for (int g = 0; g < 16; ++g) O[g] = 0.f;
#pragma unroll 1
    for (int t = 0; t < 9; ++t) { score(t);
      u32x4 pa, pb;
#pragma unroll
      for (int g = 0; g < 4; ++g) { pa[g] = pk2(__builtin_amdgcn_exp2f(X[2 * g] - mt), __builtin_amdgcn_exp2f(X[2 * g + 1] - mt)); pb[g] = pk2(__builtin_amdgcn_exp2f(X[8 + 2 * g] - mt), __builtin_amdgcn_exp2f(X[9 + 2 * g] - mt)); }
      const u32x4 va = vfrag(t, 0), vb = vfrag(t, 1);
      asm volatile("s_nop 1\n\tv_mfma_f32_32x32x16_bf16 %0, %1, %2, %0\n\tv_mfma_f32_32x32x16_bf16 %0, %3, %4, %0\n\ts_nop 11" : "+v"(O) : "v"(va), "v"(pa), "v"(vb), "v"(pb)); }
  }
  const float inv = __builtin_amdgcn_rcpf(O[12]);
  st16c(c, vm, vo, u32x4{pk2(O[0] * inv, O[1] * inv), pk2(O[2] * inv, O[3] * inv), pk2(O[4] * inv, O[5] * inv), pk2(O[6] * inv, O[7] * inv)});
  st8c(c, vm, vt, u32x2{pk2(O[8] * inv, O[9] * inv), pk2(O[10] * inv, O[11] * inv)});
}

struct Job { u32x4 v[4]; u32x2 a[4], b[4]; u32 dst, cb, stride, s; int mark; };
template <class T> DI void gld(VM& vm, T& d, const void* base, u32 voff) {
  if constexpr (sizeof(T) == 16) asm volatile("global_load_dwordx4 %0, %1, %2" : "=v"(d) : "v"(voff), "s"(base) : "memory");
  else if constexpr (sizeof(T) == 8) asm volatile("global_load_dwordx2 %0, %1, %2" : "=v"(d) : "v"(voff), "s"(base) : "memory");
  else asm volatile("global_load_dword %0, %1, %2" : "=v"(d) : "v"(voff), "s"(base) : "memory");
  ++vm.n;
}
template <int KIND> DI void job_load(const Params& p, VM& vm, Job& j, u32 id, bool valid, u32 w, u32 lane) {
  if (!valid) id = 0;
  const bool dn = KIND == 2 && id >= 50;
  id -= dn ? 50 : 0;
  j.cb = (id * (dn ? 171 : 410)) >> 11; j.s = 4 * (id - (dn ? 12 : 5) * j.cb) + w;
  constexpr u32 ld = KIND == 0 ? 3840 : KIND == 1 ? 3072 : 1280;
  j.dst = (valid ? (KIND == 0 ? OFF_BQ : KIND == 1 ? OFF_BG : dn ? OFF_BD : OFF_BW) : OOR) + j.s * (KIND == 0 ? 2 : KIND == 1 ? 3 : 5) * KB;
  j.stride = KIND == 0 ? 40 : KIND == 1 ? 60 : dn ? 240 : 100;
  const u32 vo = 32 * j.s * ld + j.cb * (KIND == 1 ? 64 : 128) + (lane >> 3) * ld + (lane & 7) * (KIND == 1 ? 8 : 16);
  const void* base = KIND == 0 ? p.wqkv : KIND == 1 ? p.wg : dn ? p.wd : p.wo;
#pragma unroll
  for (int q = 0; q < 4; ++q) {
    if constexpr (KIND == 1) { gld(vm, j.a[q], base, vo + q * 8 * ld); gld(vm, j.b[q], p.wu, vo + q * 8 * ld); } else gld(vm, j.v[q], base, vo + q * 8 * ld);
  }
  j.mark = vm.n;
}
template <int KIND> DI void job_run(const Ctx& c, VM& vm, Job& j, u32 lane) {
  const u32 L = c.lb + 80 * KB + c.w * 4096;
  need(vm, j.mark);
#pragma unroll
  for (int q = 0; q < 4; ++q) {
    if constexpr (KIND == 1) { pin(j.a[q]); pin(j.b[q]); j.v[q] = u32x4{j.a[q].x, j.b[q].x, j.a[q].y, j.b[q].y}; } else pin(j.v[q]);
    *LP(u32x4, L + (8 * q + (lane >> 3)) * 128 + (((lane & 7) ^ (2 * q)) * 16)) = j.v[q];
  }
  const u32 ra = L + (8 * (lane >> 4) + ((lane & 15) >> 2)) * 128, rc = (lane & 3) * (KIND == 0 ? 16 : 8);
  u32x4 v[4]; f32x4 cs[4];
#pragma unroll
  for (int f = 0; f < 4; ++f) { const u32 a = ra + ((rc + (f & 1) * (KIND == 0 ? 8 : 32) + (f >> 1) * 64) ^ ((lane >> 4) * 32)); const u32x2 lo = trread(a), hi = trread(a + 4 * 128); v[f] = u32x4{lo.x, lo.y, hi.x, hi.y}; }
  if constexpr (KIND == 1) {
    const u32x4 ones = {0x3F803F80u, 0x3F803F80u, 0x3F803F80u, 0x3F803F80u};
    asm volatile("s_nop 1\n\tv_mfma_f32_16x16x32_bf16 %0, %4, %8, 0\n\tv_mfma_f32_16x16x32_bf16 %1, %5, %8, 0\n\tv_mfma_f32_16x16x32_bf16 %2, %6, %8, 0\n\tv_mfma_f32_16x16x32_bf16 %3, %7, %8, 0"
                 : "=&v"(cs[0]), "=&v"(cs[1]), "=&v"(cs[2]), "=&v"(cs[3]) : "v"(v[0]), "v"(v[1]), "v"(v[2]), "v"(v[3]), "v"(ones));
  }
#pragma unroll
  for (int f = 0; f < 4; ++f) { constexpr u32 m = KIND == 0 ? 2 : KIND == 1 ? 3 : 5; const u32 gn = 4 * j.cb + f, q = gn / m;
    ++vm.n; __builtin_amdgcn_raw_buffer_store_b128(v[f], c.rs, j.dst + (q * j.stride + gn - q * m) * KB + c.l16, 0, 16); }
  if constexpr (KIND == 1) {
    asm volatile("s_nop 7" : "+v"(cs[0]), "+v"(cs[1]), "+v"(cs[2]), "+v"(cs[3]));
    const u32 fsel = lane & 15;
    u32x4 o;
#pragma unroll
    for (int e = 0; e < 4; ++e) { const float lo = (fsel & 1) ? cs[1][e] : cs[0][e], hi = (fsel & 1) ? cs[3][e] : cs[2][e]; o[e] = __float_as_uint((fsel & 2) ? hi : lo); }
    ++vm.n; __builtin_amdgcn_raw_buffer_store_b128(o, c.rs, (fsel < 4 ? OFF_CS + j.s * CS_ROW + j.cb * 256 + fsel * 64 + (lane >> 4) * 16 : OOR) | (j.dst & OOR), 0, 16);
  }
}
DI float red32(float v) {
  v = dpp_add<0x140>(dpp_add<0x141>(dpp_add<0x4E>(dpp_add<0xB1>(v))));
  auto t = __builtin_amdgcn_permlane16_swap(__float_as_uint(v), __float_as_uint(v), false, false); return __uint_as_float(t[0]) + __uint_as_float(t[1]);
}
DI void ln1_stats(const Ctx& c, VM& vm, const u32x4 (&v)[3], u32 lane) {
  const float e = (lane & 16) ? 0.f : 1.f;
  float f[24], s = 0, s2 = 0;
#pragma unroll
  for (int j = 0; j < 12; ++j) { f[2 * j] = bf_lo(v[j >> 2][j & 3]); f[2 * j + 1] = bf_hi(v[j >> 2][j & 3]); if (j < 8) s += f[2 * j] + f[2 * j + 1]; else s2 += f[2 * j] + f[2 * j + 1]; }
  const float mean = red32(s + e * s2) * (1.0f / D);
  float q = 0, q2 = 0;
#pragma unroll
  for (int j = 0; j < 24; ++j) { const float d = f[j] - mean; if (j < 16) q += d * d; else q2 += d * d; }
  const float sd = rbf(sqrtf(rbf(rbf(red32(q + e * q2) * (1.0f / D)) + 1e-5f)));
  st8c(c, vm, (lane & 24) ? OOR : A_SA + (c.bid * 4 + c.w) * 128 + (lane >> 5) * 64 + (lane & 7) * 8, u32x2{__float_as_uint(-rbf(mean)), __float_as_uint(1.0f / sd)});
}

constexpr int CARRYA = 4;
#define G decltype(Gp)::v
#define PW prof<true>(vm)
#define QSYNC 0x17
#define QPG 1
#define QP1 8
#define QP2 11
#define QC 14
#define AP1 1
#define AP2 3
#define AC 4
#define DPUB 4
#define DP1 7
#define DC 12
#define DP2 10

__global__ void __launch_bounds__(256, 1) mega_kernel(Params p) {
  extern __shared__ __attribute__((aligned(16))) unsigned char smem[];
  Ctx c;
  c.ws = p.ws; c.rs = __builtin_amdgcn_make_buffer_rsrc(p.ws, 0, (int)OFF_ACT, 0x00020000);
  c.ra = __builtin_amdgcn_make_buffer_rsrc(p.ws + OFF_ACT, 0, (int)(2 * LB), 0x00020000);
  c.lb = (u32)(size_t)(LAS unsigned char*)smem;
  c.l16 = (threadIdx.x & 63) * 16; c.w = __builtin_amdgcn_readfirstlane(threadIdx.x >> 6); c.bid = blockIdx.x;
  const u32 x = c.bid & 7, i = c.bid >> 3;
  c.sq = 64 * ((x >> 1) * 4 + (i >> 3)); c.cg = (x & 1) * 8 + (i & 7); c.sw = 128 * x + 32 * (i >> 3);
  const u32 cgq = c.cg < 15 ? c.cg : 14;
  c.tq = (c.sq >> 4) + c.w;
  c.bof = A_OF + ((c.sw >> 4) * 80 + 20 * c.w) * 256; c.bhf = A_HF + ((c.sw >> 4) * 192 + 48 * c.w) * 256;
  c.m0x = c.lb + c.w * 4 * KB; c.m0a = c.lb + 3 * c.w * TP;
  { const int l = 0; (void)l; STAMP(0); }
  asm volatile("" ::: "a0", "a255");
  BF<0, 40> Bq; BF<1, 25> Bw; BF<2, 60> Bg; BF<3, 60> Bd;
  Bq.base = OFF_BQ + (cgq * 4 + c.w) * 40 * KB; Bw.base = OFF_BW + ((i & 7) * 4 + c.w) * 25 * KB;
  Bg.base = OFF_BG + (c.cg * 4 + c.w) * 60 * KB; Bd.base = OFF_BD + ((i & 7) * 4 + c.w) * 60 * KB;
  XR xr[2];
  Stage sx, sy; Poll pl; AIn ain; VM vm = {}; int mst = 0;
  const u32 lane = threadIdx.x & 63;
  u32 epv; u32x4 lv[3]; Job jg, jn; Raw raw;
  asm volatile("buffer_load_dword %0, off, %1, %2 sc1" : "=v"(epv) : "s"(c.rs), "s"(OFF_EPOCH) : "memory"); ++vm.n;
  { const u32 o = (c.bid * 8 + c.w * 2 + (lane >> 5)) * 1280;
    gld(vm, lv[0], p.x, o + (lane & 31) * 16); gld(vm, lv[1], p.x, o + 512 + (lane & 31) * 16); gld(vm, lv[2], p.x, o + 1024 + (lane & 15) * 16); }
  const int mlv = vm.n;
  STAMP(109);
  STAMP(110);
  STAMP(111);
  raw.rx = __builtin_amdgcn_make_buffer_rsrc((void*)p.x, 0, S * D * 2, 0x00020000); raw.rw = __builtin_amdgcn_make_buffer_rsrc((void*)p.wqkv, 0, D * 3 * D * 2, 0x00020000);
  raw.vx = (lane >> 3) * 1280 + (((lane >> 3) ^ lane) & 7) * 16; raw.sx = c.tq * 16 * 1280;
  raw.vw = (lane >> 2) * 3840 + (lane & 3) * 16; raw.sw = (cgq * 4 + c.w) * 64;
  raw.ring = c.lb + 96 * KB + c.w * 12 * KB; raw.ra = raw.ring + (8 * (lane >> 4) + ((lane & 15) >> 2)) * 64 + (lane & 3) * 16;
  sfor<0, 2>(LAMBDA(K) { raw_x<0, K.v>(c, vm, raw, sx); raw_w<2 * K.v>(vm, raw); raw_w<2 * K.v + 1>(vm, raw); });
  STAMP(112);
  sfor<2, 4>(LAMBDA(K) { raw_x<0, K.v>(c, vm, raw, sx); raw_w<2 * K.v>(vm, raw); raw_w<2 * K.v + 1>(vm, raw); });
  STAMP(113);
  {
    const u32 cc = lane & 7;
    const int z = (int)(lane & 31) - 4 * (int)(lane >> 5) - 8 * (int)c.w;
    *LP(u32x4, c.lb + L_MASK + c.w * KB + c.l16) = u32x4{z >= 0 ? ~0u : 0u, z >= 1 ? ~0u : 0u, z >= 2 ? ~0u : 0u, z >= 3 ? ~0u : 0u};
    *LP(u32x2, c.lb + L_MASK + (4 + (c.w >> 1)) * KB + c.l16 + (c.w & 1) * 8) = u32x2{(z >= 0 ? 0xFFFFu : 0u) | (z >= 1 ? 0xFFFF0000u : 0u), (z >= 2 ? 0xFFFFu : 0u) | (z >= 3 ? 0xFFFF0000u : 0u)};
    const u32 t16 = (8 * c.w + (lane >> 3)) & 15, g = (t16 >> 1) & 3, cg = i & 7, h = 8 * c.w + cc;
    c.dxx = ((c.tq >> 3) + 8 * (((c.tq >> 1) & 3) * 8 + cc)) << 7;
    c.doh = (((h >> 2) + 8 * ((h & 3) + 4 * x)) << 7) | ((lane >= 4 && lane < 9 ? 5 * ((cgq * 4 + c.w) >> 1) + lane - 4 : wg_qg(c.sw >> 6, 4 * c.w + (lane & 3))) << 23);
    c.foff = threadIdx.x == 0 ? OFF_FLAGS + c.bid * 128 : OOR;
    c.daa = a_dep(c, 0) | (a_dep(c, 1) << 16);
    c.eq = (c.cg < 15 ? A_QKV + ((16 * c.cg + 4 * c.w) * 2048 + c.sq) * 16 : OOR) + ((lane >> 4) << 15) + (lane & 15) * 16;
    c.eg = A_HF + ((c.sq >> 4) * 192 + 12 * c.cg + 3 * c.w) * 256 + (lane >> 4) * (192 * 256) + (lane & 15) * 16;
    c.es = c.lb + L_STAT + (lane & 15) * 8;
    c.qs = c.cg < 5 ? 0.22360679774997896f * 1.4426950408889634f : 1.0f;
    { const u32 hd = a_head(c.bid), odd = hd & 1, cA = 5 * (hd >> 1) + 3 * odd, cC = 5 * (hd >> 1) + 2, r16 = (lane & 31) * 16, hh = lane >> 5;
      c.av0 = (80 + cA + hh) * 32768 + r16;
      c.av1 = (hh ? 160 + cA : 80 + cC) * 32768 + r16 - 1024;
      c.av2 = (hh ? 160 + cC : 161 + cA) * 32768 + (r16 ^ (hh ? 128 : 64)) - 2048;
      c.ak1 = 1024 + odd * 8 + r16;
      const u32 pc4 = lane & 3, db = (lane >> 4) & 1, kq = ((lane >> 3) & 4) + ((lane >> 2) & 3);
      const u32 vpl = db ? (pc4 ? 3 : 2) : pc4 & 1;
      c.avl = 1536 + vpl * 512 + (db ? odd : pc4 >> 1) * 8 + ((kq ^ (4 * vpl)) << 4);
      c.aob = A_OF + (((c.bid >> 5) * 8 + 2 * c.w + db) * 80 + cA + hh) * 256 + (lane & 15) * 16;
    }
    c.rw = c.lb + L_HX + c.w * 10 * KB + (lane & 15) * 64 + (((lane >> 4) ^ (lane >> 1)) & 3) * 16;
    const u32 base = c.lb + L_HX + (c.w >> 1) * 5 * KB + t16 * 64, row = c.sw + 8 * c.w + (lane >> 3);
    c.rpa = base + (cc >> 1) * KB + (((2 * (cc & 1)) ^ g ^ ((cc >> 1) & 1)) << 4);
    c.rpt = base + 4 * KB + (((cc >> 1) ^ g) << 4) + (cc & 1) * 8;
    c.rvo = ((row >> 4) * 80 + 10 * cg + cc) * 256 + t16 * 16;
    c.rvt = ((row >> 4) * 80 + 10 * cg + 8 + (cc >> 2)) * 256 + t16 * 16 + (cc & 3) * 4;
    c.rvs = cc == 0 ? (A_SA - A_XA) + row * 64 + cg * 8 : OOR;
  }
  STAMP(104);
  need(vm, mlv); pin(epv); pin(lv[0]); pin(lv[1]); pin(lv[2]);
  const u32 ep = __builtin_amdgcn_readfirstlane(epv);
  STAMP(105);
  flag_store(c, vm, ep);
  sfor<4, 6>(LAMBDA(K) { raw_x<0, K.v>(c, vm, raw, sx); raw_w<2 * K.v>(vm, raw); raw_w<2 * K.v + 1>(vm, raw); });
  sfor<6, 8>(LAMBDA(K) { raw_x<0, K.v>(c, vm, raw, sx); });
  ln1_stats(c, vm, lv, lane);
  const int mls = vm.n;
  STAMP(106);
  job_load<2>(p, vm, jn, c.bid - 86, c.bid >= 86, c.w, lane);
  STAMP(107);
  u32 fl = ep + 2;
  STAMP(108);
  { const int l = 0; (void)l;
    asm volatile("; MARK QX0");
    { f32x4 acc[2][4], cs[2]; EpQ e; u32x2 st0; int mst0 = 0;
      gemm_q0(c, vm, sx, raw, mls, mst, fl, acc, cs, Bq, LAMBDA(Gp) {
        if constexpr (G % 3 < 2 && G / 3 < 14) raw_w<12 + 2 * (G / 3) + G % 3>(vm, raw);
        if constexpr (G % 3 == 2 && G / 3 < 12) raw_x<0, 8 + G / 3>(c, vm, raw, sx);
        at<G, 44, 1, 4>(LAMBDA(K) { const u32 o = (c.sw + (K.v >> 1) * 1024 + 8 * c.w + (lane >> 3)) * 1280 + 160 * (i & 7);
          if constexpr (K.v & 1) gld(vm, xr[K.v >> 1].t, p.x, o + 128 + 4 * (lane & 7)); else gld(vm, xr[K.v >> 1].m, p.x, o + 16 * (lane & 7)); });
        at<G, 50, 1, 4>(LAMBDA(K) { raw_x<1, K.v>(c, vm, raw, sy); });
      }, LAMBDA(S) { constexpr int s = S.v;
        if (s == 0) job_load<1>(p, vm, jg, c.bid, c.bid < 240, c.w, lane);
        if (s == JR1) job_run<2>(c, vm, jn, lane);
        if (s == JR2) { job_run<1>(c, vm, jg, lane); mst = vm.n; }
        if (s == 7) poll_issue(c, vm, pl, (2 * (c.tq + ((lane >> 1) & 1) * 64) + (lane & 1)) << 7, fl - 1);
        if (s == 10) poll_again(c, vm, pl);
        if (s == 12) poll_check<true>(c, vm, pl);
        if (s == 18) poll_issue(c, vm, pl, threadIdx.x << 7, fl);
        if (s == 12) mst0 = ld8c<0>(c.ra, vm, st0, ((lane >> 4) & 1) * 65536 + ((lane >> 1) & 7) * 128 + (lane & 1) * 64, A_SA + c.tq * 1024);
        if (s == 17) { need(vm, mst0); pin(st0); *LP(u32x2, c.lb + L_STAT + ((lane >> 4) & 1) * 512 + c.w * 128 + (lane & 15) * 8) = st0; }
        if (s == 16) {
  {
    const rsrc_t rw = __builtin_amdgcn_make_buffer_rsrc(p.ws, 0, (int)OFF_DBG, 0x00020000);
    const u32 t = threadIdx.x, id = blockIdx.x * 16 + (t & 15);
    const u32 so = OFF_ACT + ((t >> 4) == 0 ? ((id >> 10) & 1) * LB + ((id >> 11) ? A_SB : A_SA) + (id & 1023) * 128
                                           : ((t >> 4) & 1) * LB + A_OF + ((id >> 5) * 80 + 5 * ((id >> 1) & 15) + 2) * 256 + (id & 1) * 128);
    asm volatile("buffer_load_dword a251, %0, %1, 0 offen sc1" :: "v"(t < 48 ? so : OOR), "s"(rw) : "memory");
  }
        }
      });
      *LP(f32x4, c.lb + L_CS + c.w * 128 + (lane >> 4) * 32) = cs[0]; *LP(f32x4, c.lb + L_CS + c.w * 128 + (lane >> 4) * 32 + 16) = cs[1];
      BARL();
      pre_q<0, 0>(c, e); pre_q<0, 1>(c, e);
      sfor<4, 8>(LAMBDA(K) { raw_x<1, K.v>(c, vm, raw, sy); });
      epi_q(c, vm, e, acc); mst = vm.n;
      sfor<8, 12>(LAMBDA(K) { raw_x<1, K.v>(c, vm, raw, sy); });
      TS(0, 4); }
    asm volatile("; MARK QY0");
    sfor<0, 40>(LAMBDA(J) { Bq.m[J.v] = 0; });
    { f32x4 acc[2][4]; EpQ e;
      gemm_lds<1, 2, 0x0F, QPG, false>(c, vm, sy, mst, fl + 1, acc, Bq, LAMBDA(Gp) {
        at<G, 0, 1, 8>(LAMBDA(K) { raw_x<1, 12 + K.v>(c, vm, raw, sy); });
        at<G, 8, 1, 2 * QC - 7>(LAMBDA(K) { ldB<K.v>(c, vm, Bw); });
        at<G, 2 * QC + 1, 1, 11>(LAMBDA(K) { a_stage<0, K.v>(c, vm, ain); });
      }, LAMBDA(S) { constexpr int s = S.v;
        if (s == 0) { pin(xr[0].m); pin(xr[0].t); pin(xr[1].m); pin(xr[1].t); poll_again(c, vm, pl); }
        if (s == 2) poll_check<true>(c, vm, pl);
        if (s == QP1) a_poll(c, vm, pl, fl, lo16(c.daa));
        if (s == QP2) poll_again(c, vm, pl);
        if (s == QC) poll_check<true>(c, vm, pl);
        if (s == 17) pre_q<1, 0>(c, e);
        if (s == 18) pre_q<1, 1>(c, e);
      });
      sfor<39 - 2 * QC, 11>(LAMBDA(K) { a_stage<0, K.v>(c, vm, ain); });
      epi_q(c, vm, e, acc);
      if (vm.n - ain.mark != CARRYA) carry_count_too_large();
      TS(1, 4); }
  }
#pragma unroll 1
  for (int l = 0;; ++l) {
    vm.n = 0; vm.done = -1000; ain.mark = -CARRYA; mst = 0;
    sfor<0, 2 * QC - 7>(LAMBDA(J) { Bw.m[J.v] = -CARRYA; });
    Bw.cur = sop(Bw.base) + ((2 * QC - 7) & ~3) * KB;
    asm volatile("; MARK AX");
    AIn ain2;
    constexpr int BWR = 32 - 2 * QC;
    TS(2, 0);
    attn<0>(c, vm, ain, LAMBDA(Gp) {
      at<G, 0, 1, BWR>(LAMBDA(K) { ldB<25 - BWR + K.v>(c, vm, Bw); });
      at<G, BWR, 1, 6 + 3 * AC - BWR>(LAMBDA(K) { ldB<K.v>(c, vm, Bg); });
      at<G, 6 + 3 * AC, 1, 11>(LAMBDA(K) { a_stage<1, K.v>(c, vm, ain2); });
    }, LAMBDA(S) { constexpr int s = S.v;
      if (s == 0) publish(c, vm, mst, fl + 2);
      if (s == AP1) a_poll(c, vm, pl, fl, hi16(c.daa));
      if (s == AP2) poll_again(c, vm, pl);
      if (s == AC) { TS(2, 1); poll_check<true>(c, vm, pl); TS(2, 2); }
      if (s == 8) { sfor<22 - 3 * AC, 11>(LAMBDA(K) { a_stage<1, K.v>(c, vm, ain2); }); TS(2, 3); }
    });
    mst = vm.n; TS(2, 4);
    asm volatile("; MARK AY");
    AW aw;
    TS(3, 0);
    attn<1>(c, vm, ain2, LAMBDA(Gp) {
      at<G, 0, 1, 6 + 3 * AC>(LAMBDA(K) { if constexpr (6 + 3 * AC - BWR + K.v < 38) ldB<6 + 3 * AC - BWR + K.v>(c, vm, Bg); });
      at<G, 6 + 3 * AC, 1, 10>(LAMBDA(K) { ld_w<0, K.v>(c, vm, aw); });
    }, LAMBDA(S) { constexpr int s = S.v;
      if (s == 0) publish(c, vm, mst, fl + 3);
      if (s == AP1) poll_issue(c, vm, pl, lo16(c.doh), fl + 3);
      if (s == AP2) poll_again(c, vm, pl);
      if (s == AC) { TS(3, 1); poll_check<true>(c, vm, pl); TS(3, 2); }
      if (s == 8) { sfor<22 - 3 * AC, 10>(LAMBDA(K) { ld_w<0, K.v>(c, vm, aw); }); TS(3, 3); }
    });
    mst = vm.n; TS(3, 4);
    asm volatile("; MARK WX");
    constexpr int BG0 = 12 + 6 * AC - BWR < 38 ? 12 + 6 * AC - BWR : 38;
    static_assert(44 - BG0 <= 13, "");
    { Red r;
      TS(4, 0);
      gemm_reg<0, 5>(c, vm, Bw, aw, LAMBDA(Gp) { at<G, 0, 1, 12>(LAMBDA(K) { ldB<BG0 + K.v>(c, vm, Bg); }); }, LAMBDA(S) { if (S.v == 1) red_pre<0>(c, r, xr[0], A_XB); if (S.v == 4) publish(c, vm, mst, fl + 4); });
      TS(4, 1);
      BARL();
      poll_issue(c, vm, pl, lo16(c.doh), fl + 4);
      TS(4, 2);
      red_out<0>(c, vm, r, xr[0], nullptr); mst = vm.n;
      TS(4, 3);
      poll_again(c, vm, pl);
      TS(4, 4); }
    asm volatile("; MARK WY");
    if (l == 0) {
      VM v2 = {}; Job jq; u32x4 t[5]; const u32 lane = vop(c.l16) >> 4;
      job_load<0>(p, v2, jq, c.bid, c.bid < 150, c.w, lane);
      const u32 o = lane < 48 ? OFF_CS + c.cg * 768 + c.w * 192 + (lane % 12) * 16 + (lane / 12) * 5 * CS_ROW : OOR;
      sfor<0, 5>(LAMBDA(J) { ld16<-1, true, 0>(c.rs, v2, t[J.v], o, sop(J.v * CS_ROW)); });
      const int mt = v2.n;
      job_run<0>(c, v2, jq, lane);
      need(v2, mt);
      f32x4 sum = {0, 0, 0, 0};
      sfor<0, 5>(LAMBDA(J) { pin(t[J.v]); sum += __builtin_bit_cast(f32x4, t[J.v]); });
      const u32 sc = c.lb + 80 * KB + c.w * 4096;
      *LP(f32x4, sc + c.l16) = sum;
      const u32 rb = sc + (lane % 12) * 16;
      *LP(f32x4, c.lb + L_CS + 512 + c.w * 192 + (lane % 12) * 16) = (*LP(const f32x4, rb) + *LP(const f32x4, rb + 192)) + (*LP(const f32x4, rb + 384) + *LP(const f32x4, rb + 576));
    }
    { Red r;
      PW;
      poll_check<true>(c, vm, pl);
      PW;
      TS(5, 0);
      sfor<0, 10>(LAMBDA(K) { ld_w<1, K.v>(c, vm, aw); });
      loadB<BG0 + 12, BG0 + 16>(c, vm, Bg);
      red_pre<1>(c, r, xr[1], A_XB);
      PW;
      need(vm, mst);
      PW;
      publish(c, vm, mst, fl + 5);
      gemm_reg<1, 5>(c, vm, Bw, aw, LAMBDA(Gp) {
        at<G, 0, 1, 44 - BG0>(LAMBDA(K) { ldB<BG0 + 16 + K.v>(c, vm, Bg); });
      }, LAMBDA(S) { constexpr int s = S.v; if (s == 1) poll_issue(c, vm, pl, c.dxx, fl + 5); if (s == 4) poll_again(c, vm, pl); });
      TS(5, 1); poll_check<true>(c, vm, pl); TS(5, 2);
      PW;
      stages<0, 0, 9>(c, vm, sx, A_XB, c.tq);
      PW;
      BARL();
      TS(5, 3);
      red_out<1>(c, vm, r, xr[1], nullptr); mst = vm.n;
      stages<0, 9, 13>(c, vm, sx, 0, 0);

      TS(5, 4); }
    asm volatile("; MARK GX");
    { f32x4 acc[3][4]; EpG e;
      TS(6, 0);
      gemm_lds<0, 3, 0x0B, 1, true>(c, vm, sx, mst, fl + 6, acc, Bg, LAMBDA(Gp) {
        at<G, 0, 1, 8>(LAMBDA(K) { stage<0, 13 + K.v>(c, vm, sx, 0, 0); });
        at56<G, 9, 25>(LAMBDA(K) { ldB<K.v>(c, vm, Bd); });
        at56<G, 40, 17>(LAMBDA(K) { stage<1, K.v>(c, vm, sy, A_XB, c.tq); });
      }, LAMBDA(S) { constexpr int s = S.v;
        if (s == 8) poll_issue(c, vm, pl, c.dxx, fl + 6);
        if (s == 10) poll_again(c, vm, pl);
        if (s == 13) { TS(6, 1); poll_check<true>(c, vm, pl); TS(6, 2); }
        if (s == 17) pre_g<0, 0>(c, e);
        if (s == 18) pre_g<0, 1>(c, e);
      });
      TS(6, 3);
      epi_g(c, vm, e, acc); mst = vm.n; TS(6, 4); }
    asm volatile("; MARK GY");
    AD<true> ad;
    { f32x4 acc[3][4]; EpG e;
      TS(7, 0);
      gemm_lds<1, 3, 0x13, 1, true>(c, vm, sy, mst, fl + 7, acc, Bg, LAMBDA(Gp) {
        at<G, 0, 1, 4>(LAMBDA(K) { stage<1, 17 + K.v>(c, vm, sy, 0, 0); });
        at56<G, 4, 35>(LAMBDA(K) { ldB<25 + K.v>(c, vm, Bd); });
        at56<G, 46, 12>(LAMBDA(K) { ld_d<0, K.v>(c, vm, ad); });
      }, LAMBDA(S) { constexpr int s = S.v;
        if (s == 9) poll_issue(c, vm, pl, hi16(c.doh), fl + 7);
        if (s == 11) poll_again(c, vm, pl);
        if (s == 14) { TS(7, 1); poll_check<true>(c, vm, pl); TS(7, 2); }
        if (s == 17) pre_g<1, 0>(c, e);
        if (s == 18) pre_g<1, 1>(c, e);
      });
      TS(7, 3);
      epi_g(c, vm, e, acc); mst = vm.n; sfor<12, 14>(LAMBDA(K) { ld_d<0, K.v>(c, vm, ad); }); TS(7, 4); }
    asm volatile("; MARK DX");
    if (l) Bq.base = OOR;
    AD<false> ad2;
    constexpr int DG = (DC * 10 + 4) / 4 + 1, DN = DC < 12 ? 30 - DG : 0;
    { Red r;
      TS(8, 0);
      gemm_reg<0, 12>(c, vm, Bd, ad, LAMBDA(Gp) {
        at<G, 0, 1, 10>(LAMBDA(K) { ld_d<0, 14 + K.v>(c, vm, ad); });
        at<G, 11, 1, 10>(LAMBDA(K) { ldB<K.v>(c, vm, Bq); });
        at<G, DG, 1, DN>(LAMBDA(K) { ld_d<1, K.v>(c, vm, ad2); });
      }, LAMBDA(S) { constexpr int s = S.v;
        if (s == DPUB) publish(c, vm, mst, fl + 8);
        if (s == DP1) poll_issue(c, vm, pl, hi16(c.doh), fl + 8);
        if (s == 2) red_pre<0>(c, r, xr[0], LB + A_XA);
        if (s == DP2) poll_again(c, vm, pl);
        if (s == DC) poll_check<true>(c, vm, pl);
      });
      TS(8, 1);
      if (DC == 12) poll_check<true>(c, vm, pl);
      TS(8, 2);
      sfor<DN, 6>(LAMBDA(K) { ld_d<1, K.v>(c, vm, ad2); });
      BARL();
      TS(8, 3);
      red_out<0>(c, vm, r, xr[0], l ? p.out : nullptr); mst = vm.n;
      sfor<(DN > 6 ? DN : 6), 12>(LAMBDA(K) { ld_d<1, K.v>(c, vm, ad2); });
      TS(8, 4); }
    asm volatile("; MARK DY");
    { Red r;
      TS(9, 0);
      gemm_reg<1, 12>(c, vm, Bd, ad2, LAMBDA(Gp) {
        at<G, 0, 1, 12>(LAMBDA(K) { ld_d<1, 12 + K.v>(c, vm, ad2); });
        at<G, 17, 1, 13 - DN>(LAMBDA(K) { ldB<10 + K.v>(c, vm, Bq); });
        at<G, DG, 1, DN>(LAMBDA(K) { stage<0, K.v>(c, vm, sx, LB + A_XA, c.tq); });
      }, LAMBDA(S) { constexpr int s = S.v;
        if (s == DPUB) publish(c, vm, mst, fl + 9);
        if (s == DP1) poll_issue(c, vm, pl, c.dxx, l ? fl : fl + 9);
        if (s == 2) red_pre<1>(c, r, xr[1], LB + A_XA);
        if (s == DP2) poll_again(c, vm, pl);
        if (s == DC) poll_check<true>(c, vm, pl);
      });
      TS(9, 1);
      if (DC == 12) poll_check<true>(c, vm, pl);
      TS(9, 2);
      stages<0, DN, 9>(c, vm, sx, LB + A_XA, c.tq);
      BARL();
      TS(9, 3);
      red_out<1>(c, vm, r, xr[1], l ? p.out : nullptr); mst = vm.n;
      stages<0, 9, 13>(c, vm, sx, 0, 0);
      TS(9, 4); }
    prof<true>(vm); prof<true>(vm);
    if (l) break;
    c.ra = __builtin_amdgcn_make_buffer_rsrc(p.ws + OFF_ACT + LB, 0, (int)LB, 0x00020000);
    sx.src -= LB; fl += 10;
    asm volatile("; MARK QX");
    { f32x4 acc[2][4]; EpQ e;
      TS(10, 0);
      gemm_lds<0, 2, QSYNC, QPG, true>(c, vm, sx, mst, fl, acc, Bq, LAMBDA(Gp) {
        at<G, 0, 1, 8>(LAMBDA(K) { stage<0, 13 + K.v>(c, vm, sx, 0, 0); });
        at<G, 8, 1, 17 + DN>(LAMBDA(K) { ldB<23 - DN + K.v>(c, vm, Bq); });
        at<G, 2 * QC + 1, 1, 13>(LAMBDA(K) { stage<1, K.v>(c, vm, sy, A_XA, c.tq); });
      }, LAMBDA(S) { constexpr int s = S.v;
        if (s == QP1) poll_issue(c, vm, pl, c.dxx, fl);
        if (s == QP2) poll_again(c, vm, pl);
        if (s == QC) { TS(10, 1); poll_check<true>(c, vm, pl); TS(10, 2); }
        if (s == 17) pre_q<0, 0>(c, e);
        if (s == 18) pre_q<0, 1>(c, e);
      });
      TS(10, 3);
      epi_q(c, vm, e, acc); mst = vm.n; stages<1, 39 - 2 * QC, 13>(c, vm, sy, 0, 0); TS(10, 4); }
    asm volatile("; MARK QY");
    { f32x4 acc[2][4]; EpQ e;
      TS(11, 0);
      gemm_lds<1, 2, QSYNC, QPG, true>(c, vm, sy, mst, fl + 1, acc, Bq, LAMBDA(Gp) {
        at<G, 0, 1, 8>(LAMBDA(K) { stage<1, 13 + K.v>(c, vm, sy, 0, 0); });
        at<G, 8, 1, 2 * QC - 7>(LAMBDA(K) { ldB<K.v>(c, vm, Bw); });
        at<G, 2 * QC + 1, 1, 11>(LAMBDA(K) { a_stage<0, K.v>(c, vm, ain); });
      }, LAMBDA(S) { constexpr int s = S.v;
        if (s == QP1) a_poll(c, vm, pl, fl, lo16(c.daa));
        if (s == QP2) poll_again(c, vm, pl);
        if (s == QC) { TS(11, 1); poll_check<true>(c, vm, pl); TS(11, 2); }
        if (s == 17) pre_q<1, 0>(c, e);
        if (s == 18) pre_q<1, 1>(c, e);
      });
      TS(11, 3);
      sfor<39 - 2 * QC, 11>(LAMBDA(K) { a_stage<0, K.v>(c, vm, ain); });
      epi_q(c, vm, e, acc); TS(11, 4); }
    if (vm.n - ain.mark != CARRYA) carry_count_too_large();
    asm volatile("; MARK END");
  }
  if (blockIdx.x == 0 && threadIdx.x == 0) __hip_atomic_store((GAS u32*)(p.ws + OFF_EPOCH), ep + 64, RLX_AGENT);
  asm volatile("s_waitcnt vmcnt(0)" ::: "memory");
}

extern "C" void kernel_launch(void* const* d_in, const int* in_sizes, int n_in, void* d_out, int out_size, void* d_ws, size_t ws_size, hipStream_t stream) {
  Params p;
  p.x = (const u16*)d_in[0]; p.wqkv = (const u16*)d_in[1]; p.wo = (const u16*)d_in[2];
  p.wg = (const u16*)d_in[4]; p.wu = (const u16*)d_in[5]; p.wd = (const u16*)d_in[6];
  p.out = (u16*)d_out; p.ws = (unsigned char*)d_ws;
  (void)hipFuncSetAttribute((const void*)mega_kernel, hipFuncAttributeMaxDynamicSharedMemorySize, LDS_BYTES);
  mega_kernel<<<256, 256, LDS_BYTES, stream>>>(p);
}
```

```cpp
#include <hip/hip_runtime.h>
#include <stdint.h>

typedef unsigned short u16;
typedef unsigned u32;
typedef u32 u32x2 __attribute__((ext_vector_type(2)));
typedef u32 u32x4 __attribute__((ext_vector_type(4)));
typedef float f32x2 __attribute__((ext_vector_type(2)));
typedef float f32x4 __attribute__((ext_vector_type(4)));
typedef float f32x16 __attribute__((ext_vector_type(16)));
typedef short bf16x8 __attribute__((ext_vector_type(8)));
typedef short s16x4 __attribute__((ext_vector_type(4)));
typedef __bf16 bf16x2_t __attribute__((ext_vector_type(2)));
#define GAS __attribute__((address_space(1)))
#define LAS __attribute__((address_space(3)))
#define DI __device__ __forceinline__
#define RLX_AGENT __ATOMIC_RELAXED, __HIP_MEMORY_SCOPE_AGENT

constexpr int S = 2048, D = 640, F = 1536;
constexpr u32 KB = 1024;
constexpr u32 OFF_FLAGS = 0;
constexpr u32 OFF_TMO = 32 * KB;
constexpr u32 OFF_EPOCH = 33 * KB;
constexpr u32 OFF_DENSE = 320 * KB;
constexpr u32 OFF_CS = 64 * KB;
constexpr u32 CS_ROW = 3072 * 4;
constexpr u32 OFF_BQ = 512 * KB;
constexpr u32 OFF_BW = OFF_BQ + 2400 * KB;
constexpr u32 OFF_BG = OFF_BW + 800 * KB;
constexpr u32 OFF_BD = OFF_BG + 3840 * KB;
constexpr u32 A_XA = 0, A_SA = 2560 * KB, A_QKV = A_SA + 128 * KB, A_OF = A_QKV + 7680 * KB, A_XB = A_OF + 2560 * KB,
              A_SB = A_XB + 2560 * KB, A_HF = A_SB + 128 * KB, LB = A_HF + 6144 * KB;
constexpr u32 OFF_ACT = OFF_BD + 1920 * KB;
constexpr u32 WS_END = OFF_ACT + 2 * LB;
constexpr u32 OOR = 0x80000000u;
constexpr u32 L_Y = 80 * KB, L_HX = 16 * KB, L_HY = 80 * KB, L_MISC = 144 * KB;
constexpr u32 TP = 3584;
constexpr u32 L_STAT = L_MISC;
constexpr u32 L_CS = L_STAT + 1024;
constexpr u32 L_MASK = L_CS + 1280;
constexpr u32 LDS_BYTES = L_MASK + 6 * KB;
static_assert(LDS_BYTES <= 160 * KB, "");

struct Params { const u16* x; const u16* wqkv; const u16* wo; const u16* wg; const u16* wu; const u16* wd; u16* out; unsigned char* ws; };

DI float bf_lo(u32 v) { return __uint_as_float(v << 16); }
DI float bf_hi(u32 v) { return __uint_as_float(v & 0xffff0000u); }
DI u32 pk2(float a, float b) { f32x2 f = {a, b}; bf16x2_t h = __builtin_convertvector(f, bf16x2_t); return __builtin_bit_cast(u32, h); }
DI float rbf(float a) { return bf_lo(pk2(a, a)); }

DI u32x2 trread(u32 a) { return __builtin_bit_cast(u32x2, __builtin_amdgcn_ds_read_tr16_b64_v4i16((LAS s16x4*)(size_t)a)); }

typedef __amdgpu_buffer_rsrc_t rsrc_t;
#define SB() __builtin_amdgcn_sched_barrier(0)
template <int I> struct IC { static constexpr int v = I; };
template <int B, int E, class Fn> DI void sfor(Fn&& f) { if constexpr (B < E) { f(IC<B>{}); sfor<B + 1, E>(f); } }
#define LAMBDA(S) [&](auto S) __attribute__((always_inline))
template <class T> DI void pin(T& d) { asm volatile("" : "+v"(d)); }
DI u32 sop(u32 v) { asm volatile("" : "+s"(v)); return v; }
DI u32 vop(u32 v) { asm volatile("" : "+v"(v)); return v; }
DI u32 vadd(u32 v, u32 k) { u32 r; asm volatile("v_add_u32 %0, %1, %2" : "=v"(r) : "i"(k), "v"(v)); return r; }
struct Ctx {
  rsrc_t rs, ra;
  u32 lb; unsigned char* ws;
  u32 l16, w, bid;
  u32 cw, cv; rsrc_t rc;
  u32 sq, sw, cg;
  u32 tq;
  u32 bof, bhf;
  u32 m0x, m0a;
  u32 foff, eq, eg, es;
  u32 av0, av1, av2, ak1, avl, aob;
  float qs;
  u32 dxx, doh, daa;
  u32 rw, rpa, rpt, rvo, rvt, rvs;
};
template <int G, int START, int STRIDE, int N, class Fn> DI void at(Fn&& f) {
  if constexpr (G >= START && (G - START) % STRIDE == 0 && (G - START) / STRIDE < N) f(IC<(G - START) / STRIDE>{});
}
template <int G, int START, int N, class Fn> DI void at56(Fn&& f) {
  if constexpr (G >= START && (G - START) % 6 != 5 && (G - START) - (G - START) / 6 < N) f(IC<(G - START) - (G - START) / 6>{});
}
template <int G, int START, int PER, int N, class Fn> DI void atn(Fn&& f) {
  if constexpr (G >= START && (G - START) * PER < N) sfor<(G - START) * PER, ((G - START + 1) * PER < N ? (G - START + 1) * PER : N)>(f);
}
#define LP(T, a) ((LAS T*)(size_t)(a))

struct VM { int n, done; };
DI void need(VM& vm, int mark) {
  if (mark > vm.done) { const int k = vm.n - mark; asm volatile("s_waitcnt vmcnt(%0)" :: "i"(k < 63 ? k : 63) : "memory"); vm.done = mark; }
}
template <int SLOT, bool SC1, int OFF> DI int ld16(rsrc_t rs, VM& vm, u32x4& d, u32 voff, u32 soff) {
  if constexpr (SLOT >= 0 && SC1) asm volatile("buffer_load_dwordx4 a[%c0:%c1], %2, %3, %4 offen offset:%5 sc1" :: "i"(4 * SLOT), "i"(4 * SLOT + 3), "v"(voff), "s"(rs), "s"(soff), "i"(OFF) : "memory");
  else if constexpr (SLOT >= 0) asm volatile("buffer_load_dwordx4 a[%c0:%c1], %2, %3, %4 offen offset:%5" :: "i"(4 * SLOT), "i"(4 * SLOT + 3), "v"(voff), "s"(rs), "s"(soff), "i"(OFF) : "memory");
  else if constexpr (SC1) asm volatile("buffer_load_dwordx4 %0, %1, %2, %3 offen offset:%4 sc1" : "=v"(d) : "v"(voff), "s"(rs), "s"(soff), "i"(OFF) : "memory");
  else asm volatile("buffer_load_dwordx4 %0, %1, %2, %3 offen offset:%4" : "=v"(d) : "v"(voff), "s"(rs), "s"(soff), "i"(OFF) : "memory");
  return ++vm.n;
}
template <int OFF> DI int ld8c(rsrc_t rs, VM& vm, u32x2& d, u32 voff, u32 soff) {
  asm volatile("buffer_load_dwordx2 %0, %1, %2, %3 offen offset:%4 sc1" : "=v"(d) : "v"(voff), "s"(rs), "s"(soff), "i"(OFF) : "memory");
  return ++vm.n;
}
template <bool FIRST, int SA, int SBB> DI void mma(f32x4& d, const u32x4& a, const u32x4& b) {
#define MMA_(D_, C_) \
  if constexpr (SA >= 0 && SBB >= 0) asm volatile("v_mfma_f32_16x16x32_bf16 %0, a[%c1:%c2], a[%c3:%c4], " C_ : D_(d) : "i"(4 * SA), "i"(4 * SA + 3), "i"(4 * SBB), "i"(4 * SBB + 3)); \
  else if constexpr (SA >= 0) asm volatile("v_mfma_f32_16x16x32_bf16 %0, a[%c1:%c2], %3, " C_ : D_(d) : "i"(4 * SA), "i"(4 * SA + 3), "v"(b)); \
  else if constexpr (SBB >= 0) asm volatile("v_mfma_f32_16x16x32_bf16 %0, %3, a[%c1:%c2], " C_ : D_(d) : "i"(4 * SBB), "i"(4 * SBB + 3), "v"(a)); \
  else asm volatile("v_mfma_f32_16x16x32_bf16 %0, %1, %2, " C_ : D_(d) : "v"(a), "v"(b));
  if constexpr (FIRST) { MMA_("=&v", "0") } else { MMA_("+v", "%0") }
#undef MMA_
}
DI void st16c(const Ctx& c, VM& vm, u32 voff, u32x4 v) { ++vm.n; __builtin_amdgcn_raw_buffer_store_b128(v, c.ra, voff, 0, 16); }
DI void st8c(const Ctx& c, VM& vm, u32 voff, u32x2 v) { ++vm.n; __builtin_amdgcn_raw_buffer_store_b64(v, c.ra, voff, 0, 16); }
DI void st4c(const Ctx& c, VM& vm, u32 voff, u32 v) { ++vm.n; __builtin_amdgcn_raw_buffer_store_b32(v, c.ra, voff, 0, 16); }
template <u32 LOFF, u32 SOFF> DI void glds_head(const Ctx& c, VM& vm, u32& cur, u32 m0b, u32 sb, u32 voff) {
  ++vm.n;
  asm volatile("s_add_u32 m0, %1, %2\n\ts_add_u32 %0, %3, %4\n\tbuffer_load_dwordx4 %5, %6, %0 offen sc1 lds"
               : "=&s"(cur) : "s"(m0b), "i"(LOFF), "s"(sb), "i"(SOFF), "v"(voff), "s"(c.ra) : "memory", "scc");
}
template <u32 LOFF, u32 SOFF> DI int glds_raw(rsrc_t rs, VM& vm, u32 m0b, u32 sb, u32 voff) {
  u32 cur;
  asm volatile("s_add_u32 m0, %1, %2\n\ts_add_u32 %0, %3, %4\n\tbuffer_load_dwordx4 %5, %6, %0 offen lds"
               : "=&s"(cur) : "s"(m0b), "i"(LOFF), "s"(sb), "i"(SOFF), "v"(voff), "s"(rs) : "memory", "scc");
  return ++vm.n;
}
template <int OFF> DI void glds_next(const Ctx& c, VM& vm, u32 cur, u32 voff) {
  ++vm.n;
  asm volatile("buffer_load_dwordx4 %0, %1, %2 offen offset:%3 sc1 lds" :: "v"(voff), "s"(c.ra), "s"(cur), "i"(OFF) : "memory");
}
#define BARL() asm volatile("s_waitcnt lgkmcnt(0)\n\ts_barrier" ::: "memory")
#define BAR0() asm volatile("s_barrier" ::: "memory")
extern "C" __device__ void carry_count_too_large() __attribute__((error("carry count too large")));

#define NAP0 1
#define NAP1 16
#define DSTR 4
#define DREP 8
struct Poll { u32 off, need; int mark, mark2; };
DI void poll_issue(const Ctx& c, VM& vm, Poll& p, u32 off, u32 need) {
  p.off = off; p.need = need;
  asm volatile("buffer_load_dword a252, %0, %1, 0 offen sc1" :: "v"(p.off), "s"(c.rs) : "memory");
  p.mark = ++vm.n;
}
DI void poll_again(const Ctx& c, VM& vm, Poll& p) {
  asm volatile("buffer_load_dword a253, %0, %1, 0 offen sc1" :: "v"(p.off), "s"(c.rs) : "memory");
  p.mark2 = ++vm.n;
}
template <bool TWO, int NAP = NAP0> DI void poll_check(const Ctx& c, VM& vm, Poll& p) {
  need(vm, p.mark);
  u32 v; asm volatile("v_accvgpr_read_b32 %0, a252" : "=v"(v));
  if (__builtin_expect(!__all(v - p.need < 32u), 0)) {
    asm volatile("buffer_load_dword a254, %1, %2, 0 offen sc1\n\ts_waitcnt vmcnt(%3) ; SECOND\n\tv_accvgpr_read_b32 %0, a253" : "=v"(v) : "v"(p.off), "s"(c.rs), "i"(vm.n - p.mark2 + 1) : "memory");
    bool ok = __all(v - p.need < 32u);
#pragma unroll 1
    for (u32 spins = 0; !ok; ++spins) {
      if (NAP) __builtin_amdgcn_s_sleep(NAP);
      asm volatile("buffer_load_dword a255, %1, %2, 0 offen sc1\n\ts_waitcnt vmcnt(1)\n\tv_accvgpr_read_b32 %0, a254" : "=v"(v) : "v"(p.off), "s"(c.rs) : "memory");
      if (__all(v - p.need < 32u)) break;
      if (NAP) __builtin_amdgcn_s_sleep(NAP);
      asm volatile("buffer_load_dword a254, %1, %2, 0 offen sc1\n\ts_waitcnt vmcnt(1)\n\tv_accvgpr_read_b32 %0, a255" : "=v"(v) : "v"(p.off), "s"(c.rs) : "memory");
      ok = __all(v - p.need < 32u);
      if (spins > (1u << 16)) { __hip_atomic_store((GAS u32*)(c.ws + OFF_TMO), 1u, RLX_AGENT); break; }
    }
  }
  asm volatile("" ::: "memory");
}
DI void flag_store(const Ctx& c, VM& vm, u32 val) { ++vm.n; __builtin_amdgcn_raw_buffer_store_b32(val, c.rs, c.foff, 0, 16); }
DI void publish(const Ctx& c, VM& vm, int mark, u32 val) { need(vm, mark); BAR0(); flag_store(c, vm, val); }
DI void code_touch(const Ctx& c, VM& vm) { asm volatile("buffer_load_dword a255, %0, %1, 0 offen" :: "v"(c.cv), "s"(c.rc) : "memory"); ++vm.n; }
#define CT 22
template <int K> DI void ctouch(const Ctx& c, VM& vm) { if constexpr ((CT >> K) & 1) code_touch(c, vm); }
DI u32 lo16(u32 v) { return vop(v) & 0xFFFF; }
DI u32 hi16(u32 v) { return vop(v) >> 16; }

template <int ID> constexpr int slotB(int j) { return ID == 0 ? (j < 10 ? -1 : 15 + j) : ID == 1 ? j : ID == 2 ? (j < 38 ? 25 + j : -1) : j; }
template <int ID_, int N> struct BF { static constexpr int ID = ID_; u32x4 f[N]; int m[N]; u32 base, cur; };
template <int J, int ID, int N> DI void ldB(const Ctx& c, VM& vm, BF<ID, N>& B) {
  if constexpr ((J & 3) == 0) B.cur = sop(B.base) + J * KB;
  B.m[J] = ld16<slotB<ID>(J), true, (J & 3) * 1024>(c.rs, vm, B.f[J], c.l16, B.cur);
}
template <int LO, int HI, class BT> DI void loadB(const Ctx& c, VM& vm, BT& B) { sfor<LO, HI>(LAMBDA(J) { ldB<J.v>(c, vm, B); }); }
template <int CTRL> DI float dpp_add(float v) { return v + __int_as_float(__builtin_amdgcn_update_dpp(0, __float_as_int(v), CTRL, 0xF, 0xF, true)); }

#define STAT0 4
template <int HALF> constexpr u32 img_off(int g) { return HALF == 0 ? g * 16 * KB : (g < 4 ? L_Y + g * 16 * KB : 0); }
DI u32 raw_lane(const Ctx& c) { const u32 l = c.l16 >> 4; return (l & 15) * 128 + (((l >> 4) ^ l) & 7) * 16; }
constexpr int cover(int sync, int g) { int e = g; while (e < 4 && !((sync >> (e + 1)) & 1)) ++e; return e; }
struct Stage { u32x4 sv; u32 src, cur; int mg[5]; };
template <int HALF, int K> DI void stage(const Ctx& c, VM& vm, Stage& st, u32 xoff, u32 tf) {
  if constexpr (K == 0) {
    const u32 t = sop(tf), l = vop(c.l16);
    st.src = xoff + HALF * 64 * 20480 + t * 20480;
    ld16<-1, true, 0>(c.ra, vm, st.sv, ((l << 2) & 0x3C0) + ((l >> 4) & 0x30), xoff + (A_SA - A_XA) + HALF * 65536 + t * 1024);
  } else {
    constexpr int g = (K - 1) >> 2, j = (K - 1) & 3;
    if constexpr (j == 0) glds_head<img_off<HALF>(g), g * 4 * KB>(c, vm, st.cur, c.m0x, st.src, c.l16); else glds_next<j * 1024>(c, vm, st.cur, c.l16);
    st.mg[g] = vm.n;
  }
}
template <int HALF, int LO, int HI> DI void stages(const Ctx& c, VM& vm, Stage& st, u32 xoff, u32 tf) { sfor<LO, HI>(LAMBDA(K) { stage<HALF, K.v>(c, vm, st, xoff, tf); }); }
template <int HALF, int NF, int SYNC, int PG, bool STATS, class BT, class HL, class HA> DI void gemm_lds(const Ctx& c, VM& vm, Stage& st, int mst, u32 fv, f32x4 (&acc)[NF][4], BT& B, HL hl, HA ha) {
  const u32 lo = STATS ? c.l16 : raw_lane(c);
  const u32 b0 = vop(c.lb + img_off<HALF>(0) + lo), b1 = vop(c.lb + img_off<HALF>(4) + lo);
  const u32 b2 = STATS ? b0 : vop(b0 ^ 64), b3 = STATS ? b1 : vop(b1 ^ 64);
  auto rd = [&](int s, int tf) __attribute__((always_inline)) { return *LP(const u32x4, (s < 16 ? ((s & 1) ? b2 : b0) + (s >> 2) * 16 * KB : ((s & 1) ? b3 : b1)) + tf * 4 * KB + (s & (STATS ? 3 : 2)) * KB); };
  need(vm, st.mg[IC<cover(SYNC, 0)>::v]); BAR0(); if (STATS) pin(st.sv);
  u32x4 a[2][4]; float s1, s2;
#pragma unroll
  for (int tf = 0; tf < 4; ++tf) a[0][tf] = rd(0, tf);
  SB();
  sfor<0, 20>(LAMBDA(S) {
    constexpr int s = S.v;
    if constexpr ((s & 3) == 3 && s < 19 && ((SYNC >> ((s + 1) >> 2)) & 1)) {
      need(vm, st.mg[IC<cover(SYNC, (s + 1) >> 2)>::v]); if ((s + 1) >> 2 == PG) need(vm, mst);
      BAR0();
    }
    if constexpr (PG >= 8 && s == PG - 8) { need(vm, mst); BAR0(); }
    need(vm, B.m[s * NF + NF - 1]);
    __builtin_amdgcn_s_waitcnt(0xC07F);
    SB();
    sfor<0, 4 * NF>(LAMBDA(J) {
      constexpr bool lq = NF == 2 && s == 19;
      constexpr int nf = lq ? J.v & 1 : J.v >> 2, tf = lq ? J.v >> 1 : J.v & 3;
      mma<s == 0, slotB<BT::ID>(s * NF + nf), -1>(acc[nf][tf], B.f[s * NF + nf], a[s & 1][tf]);
      if constexpr (s < 19 && nf == 0) { a[(s + 1) & 1][tf] = rd(s + 1, tf); SB(); }
      if constexpr (J.v >= 4 && (J.v - 4) % NF == 0) { SB(); hl(IC<s * NF + (J.v - 4) / NF>{}); SB(); }
      if constexpr (J.v == 0 && s == (PG >= 8 ? PG - 8 : 4 * PG - 1)) { flag_store(c, vm, fv); SB(); }
      if constexpr (J.v == 5) { SB(); ha(S); SB(); }
      if constexpr (STATS && J.v == 7 - (NF == 3) && s >= STAT0 && s < STAT0 + 6) {
        SB();
        if constexpr (s == STAT0) { s1 = __uint_as_float(st.sv.x) + __uint_as_float(st.sv.z); s2 = __uint_as_float(st.sv.y) + __uint_as_float(st.sv.w); pin(s1); pin(s2); }
        if constexpr (s == STAT0 + 1) { auto t = __builtin_amdgcn_permlane16_swap(__float_as_uint(s1), __float_as_uint(s2), false, false); s1 = __uint_as_float(t[0]) + __uint_as_float(t[1]); s2 = s1; pin(s1); pin(s2); }
        if constexpr (s == STAT0 + 2) { auto t = __builtin_amdgcn_permlane32_swap(__float_as_uint(s1), __float_as_uint(s2), false, false); s1 = __uint_as_float(t[0]) + __uint_as_float(t[1]); s2 = s1; pin(s1); pin(s2); }
        if constexpr (s == STAT0 + 3) { auto t = __builtin_amdgcn_permlane16_swap(__float_as_uint(s1), __float_as_uint(s2), false, false); s1 = __uint_as_float(t[0]) * (-1.0f / D); s2 = __uint_as_float(t[1]) * (1.0f / D); pin(s1); pin(s2); }
        if constexpr (s == STAT0 + 4) { s2 = __builtin_fmaxf(s2 - s1 * s1, 0.f) + 1e-5f; pin(s2); }
        if constexpr (s == STAT0 + 5) { float rstd = __builtin_amdgcn_rsqf(s2);
          *LP(f32x2, vop(c.es) + c.w * 128 + HALF * 512) = f32x2{s1, rstd}; }
        SB();
      }
    });
    SB();
  });
  if constexpr (NF == 2) asm volatile("s_nop 1");
  SB();
}
#define WARMS 16
#define JPUB 11
#define JR1 2
#define JR2 5
struct Raw { rsrc_t rx, rw; u32 vx, sx, vw, sw, ring, ra; int m[20]; };
template <int HALF, int S> DI void raw_x(const Ctx& c, VM& vm, Raw& r, Stage& st) { st.mg[S >> 2] = glds_raw<img_off<HALF>(S >> 2) + (S & 3) * KB, HALF * 1024 * 1280 + (S >> 1) * 128 + (S & 1) * 8 * 1280>(r.rx, vm, c.m0x, r.sx, r.vx); }
constexpr int ring_off(int s) { return (((s + 4) % 6) >> 1) * 16 * 1024 + (((s + 4) % 6) & 1) * 2048; }
template <int K> DI void raw_w(VM& vm, Raw& r) { r.m[K >> 1] = glds_raw<ring_off(K >> 1) + (K & 1) * KB, K * 16 * 3840>(r.rw, vm, r.ring, r.sw, r.vw); }
template <int J, int H, class BT> DI void raw_rd(Raw& r, BT& B, u32x2 (&t)[2][2]) {
  constexpr int off = ring_off(J >> 1) + (J & 1) * 8 + H * 256, sl = slotB<0>(J);
  if constexpr (sl >= 0) asm volatile("ds_read_b64_tr_b16 a[%c0:%c1], %2 offset:%3" :: "i"(4 * sl + 2 * H), "i"(4 * sl + 2 * H + 1), "v"(r.ra), "i"(off) : "memory");
  else { t[J & 1][H] = trread(r.ra + off); if constexpr (H == 1) B.f[J] = u32x4{t[J & 1][0].x, t[J & 1][0].y, t[J & 1][1].x, t[J & 1][1].y}; }
}
template <class BT, class HL, class HA> DI void gemm_q0(const Ctx& c, VM& vm, Stage& st, Raw& r, int mst0, int& mst, u32 fv, f32x4 (&acc)[2][4], f32x4 (&cs)[2], BT& B, HL hl, HA ha) {
  const u32 lo = raw_lane(c), b0 = vop(c.lb + lo), b1 = vop(c.lb + img_off<0>(4) + lo), b2 = vop(b0 ^ 64), b3 = vop(b1 ^ 64);
  auto rd = [&](int s, int tf) __attribute__((always_inline)) { return *LP(const u32x4, (s < 16 ? ((s & 1) ? b2 : b0) + (s >> 2) * 16 * KB : ((s & 1) ? b3 : b1)) + tf * 4 * KB + (s & 2) * KB); };
  u32x4 ones = {0x3F803F80u, 0x3F803F80u, 0x3F803F80u, 0x3F803F80u}; pin(ones);
  u32x4 a[2][4]; u32x2 t[2][2];
  need(vm, st.mg[0] > r.m[0] ? st.mg[0] : r.m[0]); BAR0();
#pragma unroll
  for (int tf = 0; tf < 4; ++tf) a[0][tf] = rd(0, tf);
  sfor<0, 4>(LAMBDA(K) { raw_rd<(K.v >> 1), (K.v & 1)>(r, B, t); });
  SB();
  sfor<0, 20>(LAMBDA(S) {
    constexpr int s = S.v;
    if constexpr ((s & 3) == 3 && s < 19) { need(vm, st.mg[(s + 1) >> 2]); if (s == 3) need(vm, mst0); if (s == JPUB) need(vm, mst); BAR0(); }
    if constexpr (s < 19) need(vm, r.m[s + 1]);
    asm volatile("s_waitcnt lgkmcnt(0)" ::: "memory"); __builtin_amdgcn_s_waitcnt(0xC07F);
    SB();
    sfor<0, 10>(LAMBDA(J) {
      constexpr int nf = J.v < 8 ? J.v >> 2 : J.v - 8, tf = J.v & 3;
      if constexpr (J.v < 8) mma<s == 0, slotB<0>(s * 2 + nf), -1>(acc[nf][tf], B.f[s * 2 + nf], a[s & 1][tf]);
      else mma<s == 0, slotB<0>(s * 2 + nf), -1>(cs[nf], B.f[s * 2 + nf], ones);
      if constexpr (J.v == 0 && s == 3) flag_store(c, vm, fv - 1);
      if constexpr (J.v == 0 && s == JPUB) { ++vm.n; __builtin_amdgcn_raw_buffer_store_b32(fv, c.rs, threadIdx.x < DREP ? OFF_DENSE + (threadIdx.x * 256 + c.bid) * DSTR : OOR, 0, 16); }
      if constexpr (s < 19 && J.v < 4) a[(s + 1) & 1][tf] = rd(s + 1, tf);
      if constexpr (s < 19 && J.v >= 4 && J.v < 8) raw_rd<2 * (s + 1) + (tf >> 1), (tf & 1)>(r, B, t);
      SB();
      if constexpr (J.v == 1 || J.v == 5 || J.v == 8) { hl(IC<s * 3 + (J.v >> 2)>{}); SB(); }
      if constexpr (J.v == 9) { ha(S); SB(); }
    });
  });
  asm volatile("s_nop 7");
  SB();
}
struct EpQ { f32x4 cs[2]; f32x2 st[4]; u32 vo; };
struct EpG { f32x4 cs[3]; f32x2 st[4]; u32 vo; };
template <int HALF, int PART> DI void pre_q(const Ctx& c, EpQ& e) {
  if constexpr (PART == 0) {
    const u32 pc = c.lb + L_CS + c.w * 128 + ((vop(c.l16) >> 3) & 0x60);
    e.cs[0] = *LP(const f32x4, pc); e.cs[1] = *LP(const f32x4, pc + 16);
  } else {
    const u32 ps = vop(c.es);
#pragma unroll
    for (int tf = 0; tf < 4; ++tf) e.st[tf] = *LP(const f32x2, ps + HALF * 512 + tf * 128);
    e.vo = vop(c.eq) + HALF * 1024 * 3840;
  }
}
DI void epi_q(const Ctx& c, VM& vm, const EpQ& e, const f32x4 (&acc)[2][4]) {
#pragma unroll
  for (int tf = 0; tf < 4; ++tf) {
    const float rs = e.st[tf].y * c.qs;
    const f32x4 a = (acc[0][tf] + e.st[tf].x * e.cs[0]) * rs, b = (acc[1][tf] + e.st[tf].x * e.cs[1]) * rs;
    st16c(c, vm, e.vo + (tf >> 1) * 122880 + (tf & 1) * 256, u32x4{pk2(a.x, a.y), pk2(a.z, a.w), pk2(b.x, b.y), pk2(b.z, b.w)});
    SB();
  }
}
template <int HALF, int PART> DI void pre_g(const Ctx& c, EpG& e) {
  if constexpr (PART == 0) {
    const u32 pc = c.lb + L_CS + 512 + c.w * 192 + ((vop(c.l16) >> 4) & 0x30);
#pragma unroll
    for (int nf = 0; nf < 3; ++nf) e.cs[nf] = *LP(const f32x4, pc + nf * 64);
  } else {
    const u32 ps = vop(c.es);
#pragma unroll
    for (int tf = 0; tf < 4; ++tf) e.st[tf] = *LP(const f32x2, ps + HALF * 512 + tf * 128);
    e.vo = vop(c.eg) + HALF * 64 * 192 * 256;
  }
}
DI void epi_g(const Ctx& c, VM& vm, const EpG& e, const f32x4 (&acc)[3][4]) {
#pragma unroll
  for (int nf = 0; nf < 3; ++nf) {
    u32 R[4];
#pragma unroll
    for (int tf = 0; tf < 4; ++tf) { const f32x4 a = acc[nf][tf] + e.st[tf].x * e.cs[nf]; const float r2 = e.st[tf].y * e.st[tf].y; R[tf] = pk2(a.x * r2 * a.z, a.y * r2 * a.w); }
    { auto t = __builtin_amdgcn_permlane32_swap(R[0], R[2], false, false); R[0] = t[0]; R[2] = t[1]; }
    { auto t = __builtin_amdgcn_permlane32_swap(R[1], R[3], false, false); R[1] = t[0]; R[3] = t[1]; }
    { auto t = __builtin_amdgcn_permlane16_swap(R[0], R[1], false, false); R[0] = t[0]; R[1] = t[1]; }
    { auto t = __builtin_amdgcn_permlane16_swap(R[2], R[3], false, false); R[2] = t[0]; R[3] = t[1]; }
    st16c(c, vm, e.vo + nf * 256, u32x4{R[0], R[1], R[2], R[3]});
    SB();
  }
}

struct AW { u32x4 f[2][5]; u32 s0; int m[2][5]; static constexpr int slot(int, int) { return -1; } };
template <bool X> struct AD { u32x4 f[2][12]; u32 cur; int m[2][12]; static constexpr int slot(int tf, int s) { return X && tf == 1 && s >= 9 ? 51 + s : -1; } };
template <int HALF, int K> DI void ld_w(const Ctx& c, VM& vm, AW& a) {
  constexpr int tf = K / 5, j = K % 5;
  if constexpr (j == 0 || j == 4) a.s0 = sop(c.bof) + (HALF * 64 + tf) * 20480 + j * KB;
  a.m[tf][j] = ld16<-1, true, (j & 3) * 1024>(c.ra, vm, a.f[tf][j], c.l16, a.s0);
}
template <int NS, int HALF> constexpr int kord(int p) { return NS == 12 && HALF == 1 ? (p + 5) % 12 : p; }
template <int HALF, int K, bool X> DI void ld_d(const Ctx& c, VM& vm, AD<X>& a) {
  constexpr int tf = K / 12, s = kord<12, HALF>(K % 12);
  if constexpr (K % 12 == 0 || (s & 3) == 0) a.cur = sop(c.bhf) + (HALF * 64 + tf) * 192 * 256 + (s & ~3) * KB;
  a.m[tf][s] = ld16<AD<X>::slot(tf, s), true, (s & 3) * 1024>(c.ra, vm, a.f[tf][s], c.l16, a.cur);
}
template <int TF, int S> DI u32x4 get_a(VM& vm, AW& a) { need(vm, a.m[TF][S]); return a.f[TF][S]; }
template <int TF, int S, bool X> DI u32x4 get_a(VM& vm, AD<X>& a) { need(vm, a.m[TF][S]); if constexpr (AD<X>::slot(TF, S) < 0) return a.f[TF][S]; else return u32x4{}; }
template <int HALF, int NS, bool NFM, class BT, class AT, class HL, class HA> DI void gemm_reg(const Ctx& c, VM& vm, BT& B, AT& a, HL hl, HA ha) {
  f32x4 acc[10]; u32x4 o[NS];
  const u32 p0 = vop(c.rw) + (HALF ? L_HY - L_HX : 0), p1 = p0 ^ 16;
  auto wr = [&](int j) __attribute__((always_inline)) { *LP(f32x4, (((j % 5) & 1) ? p1 : p0) + j * KB) = acc[j]; SB(); };
  sfor<0, 10 * NS>(LAMBDA(Gi) {
    constexpr int g = Gi.v, tf = g / (5 * NS), r = g % (5 * NS);
    constexpr bool cm = NFM && tf == 1;
    constexpr int p = cm ? r % NS : r / 5, nf = cm ? r / NS : r % 5, s = kord<NS, HALF>(p);
    if constexpr (nf == 0) { need(vm, B.m[s * 5 + 4]); o[s] = get_a<tf, s>(vm, a);   SB(); }
    mma<p == 0, slotB<BT::ID>(s * 5 + nf), AT::slot(tf, s)>(acc[tf * 5 + nf], B.f[s * 5 + nf], o[s]);
    if constexpr (g % 4 == 1) { SB(); hl(IC<g / 4>{}); SB(); }
    if constexpr (g % 10 == 4) { SB(); ha(IC<g / 10>{}); SB(); }
    if constexpr (g % 10 == 8) { SB(); ha(IC<100 + g / 10>{}); SB(); }
    constexpr int w0 = NS * 5 + 3, st = NFM && NS == 5 ? 2 : 4;
    if constexpr (g >= w0 && g < w0 + 5 * st && (g - w0) % st == 0) { SB(); wr((g - w0) / st); }
    if constexpr (NFM && NS == 12 && tf == 1 && nf >= 2 && p == 8) { SB(); wr(5 + nf - 1); }
    if constexpr (NFM && NS == 12 && tf == 1 && nf == 2 && p == 2) { SB(); wr(5); }
    if constexpr (NFM && NS == 5 && tf == 1 && nf >= 2 && p == 4) { SB(); wr(5 + nf - 2); }
  });
  SB();
  if constexpr (!NFM) { asm volatile("s_nop 3"); SB(); sfor<5, 10>(LAMBDA(J) { wr(J.v); }); }
  else if constexpr (NS == 12) { asm volatile("s_nop 7"); SB(); wr(9); }
  else { asm volatile("s_nop 1"); SB(); wr(8); asm volatile("s_nop 4"); SB(); wr(9); }
}
struct XR { u32x4 m; u32 t; };
#define OAUX 0
struct Red { f32x4 a, b; f32x2 t; u32 pa, pb, pt, vo, vt, vs; };
template <int HALF, int PART> DI void red_pre(const Ctx& c, Red& r, const XR& xr, u32 xoff) {
  if constexpr (PART == 0) { r.a = f32x4{bf_lo(xr.m.x), bf_hi(xr.m.x), bf_lo(xr.m.y), bf_hi(xr.m.y)}; pin(r.a); }
  if constexpr (PART == 1) { r.b = f32x4{bf_lo(xr.m.z), bf_hi(xr.m.z), bf_lo(xr.m.w), bf_hi(xr.m.w)}; pin(r.b); }
  if constexpr (PART == 2) { r.t = f32x2{bf_lo(xr.t), bf_hi(xr.t)}; r.pa = c.rpa; r.pt = c.rpt; if (HALF) { r.pa = vadd(c.rpa, L_HY - L_HX); r.pt = vadd(c.rpt, L_HY - L_HX); } r.pb = r.pa ^ 16; pin(r.t); pin(r.pb); }
  if constexpr (PART == 3) { r.vo = vadd(c.rvo, xoff + HALF * 64 * 20480); r.vt = vadd(c.rvt, xoff + HALF * 64 * 20480); r.vs = vadd(c.rvs, xoff + HALF * 65536); }
}
template <int HALF> DI void red_out(const Ctx& c, VM& vm, Red& r, XR& xr, u16* out) {
  f32x4 pa[4], pb[4]; f32x2 pt[4];
#pragma unroll
  for (int ww = 0; ww < 4; ++ww) { pa[ww] = *LP(const f32x4, r.pa + ww * 10 * KB); pb[ww] = *LP(const f32x4, r.pb + ww * 10 * KB); }
#pragma unroll
  for (int ww = 0; ww < 4; ++ww) pt[ww] = *LP(const f32x2, r.pt + ww * 10 * KB);
  SB();
  const f32x4 a = ((r.a + pa[0]) + pa[1]) + (pa[2] + pa[3]), b = ((r.b + pb[0]) + pb[1]) + (pb[2] + pb[3]);
  xr.m = u32x4{pk2(a.x, a.y), pk2(a.z, a.w), pk2(b.x, b.y), pk2(b.z, b.w)};
  st16c(c, vm, r.vo, xr.m);
  const f32x2 t = ((r.t + pt[0]) + pt[1]) + (pt[2] + pt[3]);
  xr.t = pk2(t.x, t.y);
  st4c(c, vm, r.vt, xr.t);
  if (out) {
    const u32 lane = vop(c.l16) >> 4;
    const rsrc_t ro = __builtin_amdgcn_make_buffer_rsrc(out, 0, S * D * 2, 0x00020000);
    const u32 o = ((c.sw + HALF * 1024 + 8 * c.w + (lane >> 3)) * D + 80 * c.cw) * 2;
    __builtin_amdgcn_raw_buffer_store_b128(xr.m, ro, o + 16 * (lane & 7), 0, OAUX); __builtin_amdgcn_raw_buffer_store_b32(xr.t, ro, o + 128 + 4 * (lane & 7), 0, OAUX);
  }
  const f32x2 sp = (a.xy + a.zw) + (b.xy + b.zw) + t;
  const f32x2 qp = a.xy * a.xy + (a.zw * a.zw + (b.xy * b.xy + (b.zw * b.zw + t * t)));
  float s1 = sp.x + sp.y, s2 = qp.x + qp.y;
  s1 = dpp_add<0xB1>(s1); s2 = dpp_add<0xB1>(s2); s1 = dpp_add<0x4E>(s1); s2 = dpp_add<0x4E>(s2); s1 = dpp_add<0x141>(s1); s2 = dpp_add<0x141>(s2);
  st8c(c, vm, r.vs, u32x2{__float_as_uint(s1), __float_as_uint(s2)});
}

template <int HALF> DI void red_last(const Ctx& c, Red& r, u16* out) {
  f32x4 pa[4], pb[4]; f32x2 pt[4];
#pragma unroll
  for (int ww = 0; ww < 4; ++ww) { pa[ww] = *LP(const f32x4, r.pa + ww * 10 * KB); pb[ww] = *LP(const f32x4, r.pb + ww * 10 * KB); pt[ww] = *LP(const f32x2, r.pt + ww * 10 * KB); }
  const u32 lane = vop(c.l16) >> 4;
  const rsrc_t ro = __builtin_amdgcn_make_buffer_rsrc(out, 0, S * D * 2, 0x00020000);
  const u32 o = ((c.sw + HALF * 1024 + 8 * c.w + (lane >> 3)) * D + 80 * c.cw) * 2;
  const f32x4 a = ((r.a + pa[0]) + pa[1]) + (pa[2] + pa[3]), b = ((r.b + pb[0]) + pb[1]) + (pb[2] + pb[3]);
  const f32x2 t = ((r.t + pt[0]) + pt[1]) + (pt[2] + pt[3]);
  __builtin_amdgcn_raw_buffer_store_b128(u32x4{pk2(a.x, a.y), pk2(a.z, a.w), pk2(b.x, b.y), pk2(b.z, b.w)}, ro, o + 16 * (lane & 7), 0, 0);
  __builtin_amdgcn_raw_buffer_store_b32(pk2(t.x, t.y), ro, o + 128 + 4 * (lane & 7), 0, 0);
}

struct AIn { u32x4 Q0; u32x2 qc; u32 cur; int r0, mark; };
#define LQ 3
#define LW 3
DI u32 wg_wd(u32 RQ, u32 cg) { return ((RQ >> (5 - LW)) << (3 - LW)) + (cg >> LW) + 8 * (((RQ & ((32 >> LW) - 1)) << LW) + (cg & ((1 << LW) - 1))); }
DI u32 wg_qg(u32 RG, u32 cg) { return ((RG >> (5 - LQ)) << (4 - LQ)) + (cg >> LQ) + 8 * (((RG & ((32 >> LQ) - 1)) << LQ) + (cg & ((1 << LQ) - 1))); }
DI u32 a_head(u32 bid) { return 4 * (bid & 7) + ((bid >> 3) & 3); }
DI u32 a_dep(const Ctx& c, int half) {
  const int h = a_head(c.bid), r0 = (half * 8 + (c.bid >> 5)) * 128, clo = 5 * (h >> 1) + 2 * (h & 1), lane = c.l16 >> 4;
  const int j = lane < 12 ? lane : 0, sec = j >> 2, lh = (j >> 1) & 1;
  int row = sec == 0 ? r0 + 32 * (int)c.w : r0 - 256 + 96 * (int)c.w + 64 * (j & 1);
  if (row < 0) row = r0;
  return (wg_qg((row >> 6) & 15, (80 * sec + clo + 2 * lh) >> 4) << 7) | (row >> 10);
}
DI void a_poll(const Ctx& c, VM& vm, Poll& p, u32 fl, u32 dep) { poll_issue(c, vm, p, dep & ~1u, fl + 1 + (dep & 1)); }
template <int HALF, int K> DI void a_stage(const Ctx& c, VM& vm, AIn& in) {
  if constexpr (K == 0) {
    in.r0 = (HALF * 8 + (sop(c.bid) >> 5)) * 128;
    in.cur = A_QKV - 80 * 512 + (in.r0 + 32 * sop(c.w)) * 3840;
    ld16<-1, true, 0>(c.ra, vm, in.Q0, c.av0, in.cur);
  } else if constexpr (K == 1) {
    ld8c<1024>(c.ra, vm, in.qc, c.av1, in.cur + (sop(c.bid) & 8));
    const u32 po = c.m0a + (HALF ? L_HY : L_HX) + 3072 + (vop(c.l16) >> 1);
#pragma unroll
    for (int u = 0; u < 3; ++u) *LP(u32x2, po + u * TP) = u32x2{0x3F803F80u, 0x3F803F80u};
  } else {
    constexpr int u = (K - 2) / 3, j = (K - 2) % 3;
    if constexpr (j == 0) {
      int krow = in.r0 - 256 + 96 * (int)c.w + 32 * u; if (HALF == 0 && krow < 0) krow = in.r0;
      glds_head<(HALF ? L_HY : L_HX) + u * TP, A_QKV>(c, vm, in.cur, c.m0a, krow * 3840, c.av0);
    } else glds_next<j * 1024>(c, vm, in.cur, j == 1 ? c.av1 : c.av2);
    in.mark = vm.n;
  }
}
DI float mx3(float a, float b, float c) { float r; asm("v_max3_f32 %0, %1, %2, %3" : "=v"(r) : "v"(a), "v"(b), "v"(c)); return r; }
DI void smfma0(f32x16& d, u32x4 a, u32x4 b) { asm volatile("v_mfma_f32_32x32x16_bf16 %0, %1, %2, 0" : "=&v"(d) : "v"(a), "v"(b)); }
DI void smfmac(f32x16& d, u32x4 a, u32x4 b, const f32x16& cc) { asm volatile("v_mfma_f32_32x32x16_bf16 %0, %1, %2, %3" : "=&v"(d) : "v"(a), "v"(b), "v"(cc)); }
DI void smfma1(f32x16& d, u32x2 a, u32x2 b) { asm volatile("v_mfma_f32_32x32x8_bf16 %0, %1, %2, %0" : "+v"(d) : "v"(a), "v"(b)); }
DI void omfma(f32x16& d, u32x4 a, u32x4 b) { asm volatile("v_mfma_f32_32x32x16_bf16 %0, %1, %2, %0" : "+v"(d) : "v"(a), "v"(b)); }
#define MX3(a, b, c) __builtin_fmaxf(__builtin_fmaxf((a), (b)), (c))
template <int HALF, class HL, class HA> DI void attn(const Ctx& c, VM& vm, AIn& in, HL hl, HA ha) {
  const u32 l = vop(c.l16); const bool hh = l >= 512;
  const int q0 = (HALF * 8 + (sop(c.bid) >> 5)) * 128 + 32 * c.w;
  const u32 img = c.lb + (HALF ? L_HY : L_HX) + c.w * TP;
  const u32 kp0 = vop(img + l), kp1 = vop(img + c.ak1), mp = vop(c.lb + L_MASK + l);
  const u32 vlo = vop(img + c.avl), vhi = vop(vlo ^ 128);
  const int t0 = (256 - q0) >> 5;
  auto vfrag = [&](int t, int s) __attribute__((always_inline)) { const u32x2 lo = trread(vlo + t * TP + s * 256), hi = trread(vhi + t * TP + s * 256); return u32x4{lo.x, lo.y, hi.x, hi.y}; };
  u32x4 sel[4];
#pragma unroll
  for (int j = 0; j < 4; ++j) sel[j] = *LP(const u32x4, mp + j * KB);
  const u32x4 mkA = *LP(const u32x4, mp + 4 * KB), mkB = *LP(const u32x4, mp + 5 * KB);
  need(vm, in.mark); BARL(); pin(in.Q0); pin(in.qc);
  u32x4 K0[9]; u32x2 K1[9];
  auto kread = [&](int t) __attribute__((always_inline)) { K0[t] = *LP(const u32x4, kp0 + t * TP); K1[t] = *LP(const u32x2, kp1 + t * TP); };
  auto kfix = [&](int t) __attribute__((always_inline)) { if (HALF == 0) { K1[t].x = hh ? (t < t0 ? 0xF149u : 0u) : K1[t].x; pin(K1[t]); } };
  kread(0); kread(8);
  u32x4 Q0 = in.Q0; u32x2 Q1 = hh ? u32x2{HALF == 0 ? 0x3F80u : 0u, 0} : in.qc;
  pin(Q1);
  SB(); kread(1); kread(2); SB();
  f32x16 S[2], M;
  kfix(0); kfix(8);
  SB(); asm volatile("s_nop 0"); smfma0(M, K0[0], Q0);
  SB(); kread(3); kread(4); SB();
  smfma1(M, K1[0], Q1);
  SB(); hl(IC<0>{}); kread(5); SB();
  smfma0(S[0], K0[8], Q0);
  SB(); kread(6); kread(7); SB();
  smfma1(S[0], K1[8], Q1);
  SB(); hl(IC<1>{});
  const u32x4 V0a = vfrag(0, 0), V8a = vfrag(8, 0), V0b = vfrag(0, 1), V8b = vfrag(8, 1);
  SB(); hl(IC<2>{}); ha(IC<10>{}); SB();
#pragma unroll
  for (int t = 1; t < 8; ++t) kfix(t);
  asm volatile("s_nop 1" : "+v"(M), "+v"(S[0]));
  SB();
#pragma unroll
  for (int g = 0; g < 16; ++g) M[g] = __uint_as_float((sel[g >> 2][g & 3] & __float_as_uint(S[0][g])) | (~sel[g >> 2][g & 3] & __float_as_uint(M[g])));
  float m = mx3(mx3(mx3(M[0], M[1], M[2]), mx3(M[3], M[4], M[5]), mx3(M[6], M[7], M[8])), mx3(mx3(M[9], M[10], M[11]), mx3(M[12], M[13], M[14]), M[15]), M[15]);
  { auto t = __builtin_amdgcn_permlane32_swap(__float_as_uint(m), __float_as_uint(m), false, false); m = mx3(__uint_as_float(t[0]), __uint_as_float(t[1]), __uint_as_float(t[1])); }
  f32x16 negm, O;
#pragma unroll
  for (int g = 0; g < 16; ++g) negm[g] = -m;
  pin(negm);
  SB(); ha(IC<0>{}); hl(IC<3>{});
  SB();
#define EM(i) __builtin_amdgcn_exp2f(M[i] - m)
  smfmac(S[1], K0[1], Q0, negm);
  u32x4 P; P.x = vop(pk2(EM(0), EM(1))); P.y = vop(pk2(EM(2), EM(3)));
  SB(); smfma1(S[1], K1[1], Q1);
  P.z = vop(pk2(EM(4), EM(5))); P.w = vop(pk2(EM(6), EM(7)));
  u32x4 A8 = P & mkA, A0 = P & ~mkA; pin(A8); pin(A0);
  P.x = vop(pk2(EM(8), EM(9)));
  SB(); hl(IC<4>{}); SB(); smfma0(O, V0a, A0);
  P.y = vop(pk2(EM(10), EM(11))); P.z = vop(pk2(EM(12), EM(13)));
  SB(); hl(IC<5>{}); ha(IC<9>{}); SB(); omfma(O, V8a, A8);
  P.w = vop(pk2(EM(14), EM(15)));
  u32x4 Pp = P & mkB, B0 = P & ~mkB, Vp = V8b; pin(Pp); pin(B0);
  SB(); hl(IC<6>{}); SB();
#define EX(i) __builtin_amdgcn_exp2f(S[t & 1][i])
  sfor<1, 8>(LAMBDA(T) {
    constexpr int t = T.v;
    const u32x4 Va = vfrag(t, 0), Vb = vfrag(t, 1);
    u32x4 Pa, Pb;
    SB();
    if constexpr (t == 1) omfma(O, V0b, B0); else omfma(O, Vp, Pp);
    Pa.x = vop(pk2(EX(0), EX(1))); Pa.y = vop(pk2(EX(2), EX(3)));
    SB(); hl(IC<4 + 3 * t>{}); SB();
    if constexpr (t < 7) smfmac(S[(t + 1) & 1], K0[t + 1], Q0, negm);
    Pa.z = vop(pk2(EX(4), EX(5))); Pa.w = vop(pk2(EX(6), EX(7)));
    SB(); hl(IC<5 + 3 * t>{}); SB();
    if constexpr (t < 7) smfma1(S[(t + 1) & 1], K1[t + 1], Q1);
    Pb.x = vop(pk2(EX(8), EX(9))); Pb.y = vop(pk2(EX(10), EX(11)));
    SB();
    if constexpr (t == 1) { omfma(O, Vp, Pp); SB(); }
    ha(T);
    SB(); hl(IC<6 + 3 * t>{});
    __builtin_amdgcn_s_waitcnt(0xC07F);
    SB();
    omfma(O, Va, Pa);
    Pb.z = vop(pk2(EX(12), EX(13))); Pb.w = vop(pk2(EX(14), EX(15)));
    Pp = Pb; Vp = Vb;
    SB();
  });
  const u32 vo = vop(c.aob) + HALF * 64 * 20480, vt = hh ? OOR : vo + ((c.bid & 8) ? 8 - 256 : 512);
  asm volatile("s_nop 1" :: "v"(negm));
  omfma(O, Vp, Pp);
  ha(IC<8>{});
  asm volatile("s_nop 11" : "+v"(O) : "v"(vo), "v"(vt));
  if (__builtin_expect(!__all(O[12] < 1.0995116e12f), 0)) {
    f32x16 X;
    auto score = [&](int t) __attribute__((always_inline)) {
      u32x4 k0 = *LP(const u32x4, kp0 + t * TP); u32x2 k1 = *LP(const u32x2, kp1 + t * TP);
      if (HALF == 0) k1.x = hh ? (t < t0 ? 0xF149u : 0u) : k1.x;
      asm volatile("s_nop 1\n\tv_mfma_f32_32x32x16_bf16 %0, %1, %2, 0\n\tv_mfma_f32_32x32x8_bf16 %0, %3, %4, %0\n\ts_nop 11" : "=&v"(X) : "v"(k0), "v"(Q0), "v"(k1), "v"(Q1));
      const u32 x0 = t == 0 ? ~0u : 0u, x1 = (t & 7) ? ~0u : 0u;
#pragma unroll
      for (int j = 0; j < 4; ++j) { const u32x4 ok = (*LP(const u32x4, mp + j * KB) ^ x0) | x1;
#pragma unroll
        for (int e = 0; e < 4; ++e) X[4 * j + e] = __uint_as_float((ok[e] & __float_as_uint(X[4 * j + e])) | (~ok[e] & 0xFF800000u)); }
    };
    float mt = -INFINITY;
#pragma unroll 1
    for (int t = 0; t < 9; ++t) { score(t);
#pragma unroll
      for (int g = 0; g < 16; ++g) mt = __builtin_fmaxf(mt, X[g]); }
    { auto t = __builtin_amdgcn_permlane32_swap(__float_as_uint(mt), __float_as_uint(mt), false, false); mt = __builtin_fmaxf(__uint_as_float(t[0]), __uint_as_float(t[1])); }
#pragma unroll
    for (int g = 0; g < 16; ++g) O[g] = 0.f;
#pragma unroll 1
    for (int t = 0; t < 9; ++t) { score(t);
      u32x4 pa, pb;
#pragma unroll
      for (int g = 0; g < 4; ++g) { pa[g] = pk2(__builtin_amdgcn_exp2f(X[2 * g] - mt), __builtin_amdgcn_exp2f(X[2 * g + 1] - mt)); pb[g] = pk2(__builtin_amdgcn_exp2f(X[8 + 2 * g] - mt), __builtin_amdgcn_exp2f(X[9 + 2 * g] - mt)); }
      const u32x4 va = vfrag(t, 0), vb = vfrag(t, 1);
      asm volatile("s_nop 1\n\tv_mfma_f32_32x32x16_bf16 %0, %1, %2, %0\n\tv_mfma_f32_32x32x16_bf16 %0, %3, %4, %0\n\ts_nop 11" : "+v"(O) : "v"(va), "v"(pa), "v"(vb), "v"(pb)); }
  }
  const float inv = __builtin_amdgcn_rcpf(O[12]);
  st16c(c, vm, vo, u32x4{pk2(O[0] * inv, O[1] * inv), pk2(O[2] * inv, O[3] * inv), pk2(O[4] * inv, O[5] * inv), pk2(O[6] * inv, O[7] * inv)});
  st8c(c, vm, vt, u32x2{pk2(O[8] * inv, O[9] * inv), pk2(O[10] * inv, O[11] * inv)});
}

struct Job { u32x4 v[4]; u32x2 a[4], b[4]; f32x4 cs[4]; u32 dst, cb, stride, s; int mark; };
template <class T> DI void gld(VM& vm, T& d, const void* base, u32 voff) {
  if constexpr (sizeof(T) == 16) asm volatile("global_load_dwordx4 %0, %1, %2" : "=v"(d) : "v"(voff), "s"(base) : "memory");
  else if constexpr (sizeof(T) == 8) asm volatile("global_load_dwordx2 %0, %1, %2" : "=v"(d) : "v"(voff), "s"(base) : "memory");
  else asm volatile("global_load_dword %0, %1, %2" : "=v"(d) : "v"(voff), "s"(base) : "memory");
  ++vm.n;
}
template <int KIND> DI void job_load(const Params& p, VM& vm, Job& j, u32 id, bool valid, u32 w, u32 lane) {
  if (!valid) id = 0;
  const bool dn = KIND == 2 && id >= 50;
  id -= dn ? 50 : 0;
  if constexpr (KIND == 1) { const u32 q = (id * 205) >> 11; j.cb = 2 * q + (w & 1); j.s = 2 * (id - 10 * q) + (w >> 1); }
  else { j.cb = (id * (dn ? 171 : 410)) >> 11; j.s = 4 * (id - (dn ? 12 : 5) * j.cb) + w; }
  constexpr u32 ld = KIND == 0 ? 3840 : KIND == 1 ? 3072 : 1280;
  j.dst = (valid ? (KIND == 0 ? OFF_BQ : KIND == 1 ? OFF_BG : dn ? OFF_BD : OFF_BW) : OOR) + j.s * (KIND == 0 ? 2 : KIND == 1 ? 3 : 5) * KB;
  j.stride = KIND == 0 ? 40 : KIND == 1 ? 60 : dn ? 240 : 100;
  const u32 vo = 32 * j.s * ld + j.cb * (KIND == 1 ? 64 : 128) +
    (lane >> 3) * ld + (lane & 7) * (KIND == 1 ? 8 : 16);
  const void* base = KIND == 0 ? p.wqkv : KIND == 1 ? p.wg : dn ? p.wd : p.wo;
#pragma unroll
  for (int q = 0; q < 4; ++q) {
    if constexpr (KIND == 1) { gld(vm, j.a[q], base, vo + q * 8 * ld); gld(vm, j.b[q], p.wu, vo + q * 8 * ld); } else gld(vm, j.v[q], base, vo + q * 8 * ld);
  }
  j.mark = vm.n;
}
template <int KIND, int PH, int SCR = 80> DI void job_ph(const Ctx& c, VM& vm, Job& j, u32 lane) {
  const u32 L = c.lb + SCR * KB + c.w * 4096;
  if constexpr (PH == 0) {
    need(vm, j.mark);
#pragma unroll
    for (int q = 0; q < 4; ++q) {
      if constexpr (KIND == 1) { pin(j.a[q]); pin(j.b[q]); j.v[q] = u32x4{j.a[q].x, j.b[q].x, j.a[q].y, j.b[q].y}; } else pin(j.v[q]);
      *LP(u32x4, L + (8 * q + (lane >> 3)) * 128 + (((lane & 7) ^ (2 * q)) * 16)) = j.v[q];
    }
  } else if constexpr (PH == 1) {
    const u32 ra = L + (8 * (lane >> 4) + ((lane & 15) >> 2)) * 128, rc = (lane & 3) * (KIND == 0 ? 16 : 8);
#pragma unroll
    for (int f = 0; f < 4; ++f) { const u32 a = ra + ((rc + (f & 1) * (KIND == 0 ? 8 : 32) + (f >> 1) * 64) ^ ((lane >> 4) * 32)); const u32x2 lo = trread(a), hi = trread(a + 4 * 128); j.v[f] = u32x4{lo.x, lo.y, hi.x, hi.y}; }
  } else if constexpr (PH == 2) {
    if constexpr (KIND == 1) {
      const u32x4 ones = {0x3F803F80u, 0x3F803F80u, 0x3F803F80u, 0x3F803F80u};
      asm volatile("s_nop 1\n\tv_mfma_f32_16x16x32_bf16 %0, %4, %8, 0\n\tv_mfma_f32_16x16x32_bf16 %1, %5, %8, 0\n\tv_mfma_f32_16x16x32_bf16 %2, %6, %8, 0\n\tv_mfma_f32_16x16x32_bf16 %3, %7, %8, 0"
                   : "=&v"(j.cs[0]), "=&v"(j.cs[1]), "=&v"(j.cs[2]), "=&v"(j.cs[3]) : "v"(j.v[0]), "v"(j.v[1]), "v"(j.v[2]), "v"(j.v[3]), "v"(ones));
    }
#pragma unroll
    for (int f = 0; f < 4; ++f) { constexpr u32 m = KIND == 0 ? 2 : KIND == 1 ? 3 : 5; const u32 gn = 4 * j.cb + f, q = gn / m;
      ++vm.n; __builtin_amdgcn_raw_buffer_store_b128(j.v[f], c.rs, j.dst + (q * j.stride + gn - q * m) * KB + c.l16, 0, 16); }
  } else if constexpr (KIND == 1) {
    pin(j.cs[0]); pin(j.cs[1]); pin(j.cs[2]); pin(j.cs[3]);
    const u32 fsel = lane & 15;
    u32x4 o;
#pragma unroll
    for (int e = 0; e < 4; ++e) { const float lo = (fsel & 1) ? j.cs[1][e] : j.cs[0][e], hi = (fsel & 1) ? j.cs[3][e] : j.cs[2][e]; o[e] = __float_as_uint((fsel & 2) ? hi : lo); }
    ++vm.n; __builtin_amdgcn_raw_buffer_store_b128(o, c.rs, (fsel < 4 ? OFF_CS + j.s * CS_ROW + j.cb * 256 + fsel * 64 + (lane >> 4) * 16 : OOR) | (j.dst & OOR), 0, 16);
  }
}
DI float red32(float v) {
  v = dpp_add<0x140>(dpp_add<0x141>(dpp_add<0x4E>(dpp_add<0xB1>(v))));
  auto t = __builtin_amdgcn_permlane16_swap(__float_as_uint(v), __float_as_uint(v), false, false); return __uint_as_float(t[0]) + __uint_as_float(t[1]);
}
DI void ln1_stats(const Ctx& c, VM& vm, const u32x4 (&v)[3], u32 lane, u32 lrow) {
  const float e = (lane & 16) ? 0.f : 1.f;
  float f[24], s = 0, s2 = 0;
#pragma unroll
  for (int j = 0; j < 12; ++j) { f[2 * j] = bf_lo(v[j >> 2][j & 3]); f[2 * j + 1] = bf_hi(v[j >> 2][j & 3]); if (j < 8) s += f[2 * j] + f[2 * j + 1]; else s2 += f[2 * j] + f[2 * j + 1]; }
  const float mean = red32(s + e * s2) * (1.0f / D);
  float q = 0, q2 = 0;
#pragma unroll
  for (int j = 0; j < 24; ++j) { const float d = f[j] - mean; if (j < 16) q += d * d; else q2 += d * d; }
  const float sd = rbf(sqrtf(rbf(rbf(red32(q + e * q2) * (1.0f / D)) + 1e-5f)));
  st8c(c, vm, (lane & 24) ? OOR : A_SA + lrow * 64 + (lane >> 5) * 64 + (lane & 7) * 8, u32x2{__float_as_uint(-rbf(mean)), __float_as_uint(1.0f / sd)});
}

constexpr int CARRYA = 4;
#define G decltype(Gp)::v
#define YC 14
#define YP1 8
#define YP2 11
#define FPA 1
#define DXN true
#define FPG 1
#define QBW 21
#define QXS 0x17
#define QXP 1
#define QYS 0x17
#define QYP 1
#define GXS 0x0B
#define GXP 1
#define GYS 0x13
#define GYP 1
#define QP1 8
#define QP2 11
#define QC 14
#define AP1 1
#define AP2 3
#define AC 4
#define DPUB 4
#define DP1 7
#define WC 5
#define WP1 1
#define WP2 4
#define WPUB 2
#define APUB 10
#define DC 12
#define DP2 10

__global__ void __launch_bounds__(256, 1) mega_kernel(Params p) {
  extern __shared__ __attribute__((aligned(16))) unsigned char smem[];
  unsigned long long pc; u32 len;
  asm volatile("s_getpc_b64 %0\n.Lmk_begin:\n\ts_mov_b32 %1, .Lmk_end-.Lmk_begin" : "=s"(pc), "=s"(len));
  Ctx c;
  c.ws = p.ws; c.rs = __builtin_amdgcn_make_buffer_rsrc(p.ws, 0, (int)OFF_ACT, 0x00020000);
  c.ra = __builtin_amdgcn_make_buffer_rsrc(p.ws + OFF_ACT, 0, (int)(2 * LB), 0x00020000);
  c.lb = (u32)(size_t)(LAS unsigned char*)smem;
  c.l16 = (threadIdx.x & 63) * 16; c.w = __builtin_amdgcn_readfirstlane(threadIdx.x >> 6); c.bid = blockIdx.x;
  const u32 x = c.bid & 7, i = c.bid >> 3;
  c.sq = 64 * (((x >> (4 - LQ)) << (5 - LQ)) + (i >> LQ)); c.cg = ((x & ((16 >> LQ) - 1)) << LQ) + (i & ((1 << LQ) - 1));
  c.sw = 32 * (((x >> (3 - LW)) << (5 - LW)) + (i >> LW)); c.cw = ((x & ((8 >> LW) - 1)) << LW) + (i & ((1 << LW) - 1));
  const u32 cgq = c.cg < 15 ? c.cg : 14;
  c.tq = (c.sq >> 4) + c.w;
  c.bof = A_OF + ((c.sw >> 4) * 80 + 20 * c.w) * 256; c.bhf = A_HF + ((c.sw >> 4) * 192 + 48 * c.w) * 256;
  c.m0x = c.lb + c.w * 4 * KB; c.m0a = c.lb + 3 * c.w * TP;
  asm volatile("" ::: "a0", "a255");
  BF<0, 40> Bq; BF<1, 25> Bw; BF<2, 60> Bg; BF<3, 60> Bd;
  Bq.base = OFF_BQ + (cgq * 4 + c.w) * 40 * KB; Bw.base = OFF_BW + (c.cw * 4 + c.w) * 25 * KB;
  Bg.base = OFF_BG + (c.cg * 4 + c.w) * 60 * KB; Bd.base = OFF_BD + (c.cw * 4 + c.w) * 60 * KB;
  XR xr[2];
  Stage sx, sy; Poll pl; AIn ain; VM vm = {}; int mst = 0;
  const u32 lane = threadIdx.x & 63;
  u32 epv; u32x4 lv[3]; Job jg, jn, jq; Raw raw;
  asm volatile("buffer_load_dword %0, off, %1, %2 sc1" : "=v"(epv) : "s"(c.rs), "s"(OFF_EPOCH) : "memory"); ++vm.n;
  const u32 lrow = (x & 1) * 1024 + (x >> 1) * 256 + 8 * i + 2 * c.w;
  { const u32 o = (lrow + (lane >> 5)) * 1280;
    gld(vm, lv[0], p.x, o + (lane & 31) * 16); gld(vm, lv[1], p.x, o + 512 + (lane & 31) * 16); gld(vm, lv[2], p.x, o + 1024 + (lane & 15) * 16); }
  const int mlv = vm.n;
  raw.rx = __builtin_amdgcn_make_buffer_rsrc((void*)p.x, 0, S * D * 2, 0x00020000); raw.rw = __builtin_amdgcn_make_buffer_rsrc((void*)p.wqkv, 0, D * 3 * D * 2, 0x00020000);
  raw.vx = (lane >> 3) * 1280 + (((lane >> 3) ^ lane) & 7) * 16; raw.sx = c.tq * 16 * 1280;
  raw.vw = (lane >> 2) * 3840 + (lane & 3) * 16; raw.sw = (cgq * 4 + c.w) * 64;
  raw.ring = c.lb + 96 * KB + c.w * 4 * KB; raw.ra = raw.ring + (8 * (lane >> 4) + ((lane & 15) >> 2)) * 64 + (lane & 3) * 16;
  c.rc = __builtin_amdgcn_make_buffer_rsrc((void*)pc, 0, (int)len, 0x00020000);
  c.cv = lane < 6 ? ((i * 4 + c.w) * 6 + lane) * 128 : OOR;
  code_touch(c, vm);
  { const u32 t = c.w * 20 + lane, col = (x & 1) * 2048 + (t & 15) * 128;
    asm volatile("buffer_load_dword a255, %0, %1, 0 offen" :: "v"(lane < 20 && col < 3840 ? ((x >> 1) * 160 + i * 5 + (t >> 4)) * 3840 + col : OOR), "s"(raw.rw) : "memory"); ++vm.n; }
  sfor<0, 2>(LAMBDA(K) { raw_x<0, K.v>(c, vm, raw, sx); raw_w<2 * K.v>(vm, raw); raw_w<2 * K.v + 1>(vm, raw); });
  sfor<2, 4>(LAMBDA(K) { raw_x<0, K.v>(c, vm, raw, sx); raw_w<2 * K.v>(vm, raw); raw_w<2 * K.v + 1>(vm, raw); });
  {
    const u32 cc = lane & 7;
    const int z = (int)(lane & 31) - 4 * (int)(lane >> 5) - 8 * (int)c.w;
    *LP(u32x4, c.lb + L_MASK + c.w * KB + c.l16) = u32x4{z >= 0 ? ~0u : 0u, z >= 1 ? ~0u : 0u, z >= 2 ? ~0u : 0u, z >= 3 ? ~0u : 0u};
    *LP(u32x2, c.lb + L_MASK + (4 + (c.w >> 1)) * KB + c.l16 + (c.w & 1) * 8) = u32x2{(z >= 0 ? 0xFFFFu : 0u) | (z >= 1 ? 0xFFFF0000u : 0u), (z >= 2 ? 0xFFFFu : 0u) | (z >= 3 ? 0xFFFF0000u : 0u)};
    const u32 t16 = (8 * c.w + (lane >> 3)) & 15, g = (t16 >> 1) & 3, cg = c.cw, h = 8 * c.w + cc;
    c.dxx = wg_wd(c.tq >> 1, cc) << 7;
    c.doh = (((h >> 2) + 8 * ((h & 3) + 4 * (c.sw >> 7))) << 7) | (wg_qg(c.sw >> 6, 4 * c.w + (lane & 3)) << 23);
    c.dxx |= (lane >= 8 && lane < 13 ? (5 * ((cgq * 4 + c.w) >> 1) + lane - 8) << 7 : c.dxx) << 16;
    c.foff = threadIdx.x == 0 ? OFF_FLAGS + c.bid * 128 : OOR;
    c.daa = a_dep(c, 0) | (a_dep(c, 1) << 16);
    c.eq = (c.cg < 15 ? A_QKV + (16 * c.cg + 4 * c.w) * 512 + c.sq * 3840 : OOR) + (lane >> 4) * 512 + (lane & 15) * 16;
    c.eg = A_HF + ((c.sq >> 4) * 192 + 12 * c.cg + 3 * c.w) * 256 + (lane >> 4) * (192 * 256) + (lane & 15) * 16;
    c.es = c.lb + L_STAT + (lane & 15) * 8;
    c.qs = c.cg < 5 ? 0.22360679774997896f * 1.4426950408889634f : 1.0f;
    { const u32 hd = a_head(c.bid), odd = hd & 1, cA = 5 * (hd >> 1) + 3 * odd, cC = 5 * (hd >> 1) + 2, r16 = (lane & 31) * 16, hh = lane >> 5;
      c.av0 = (80 + cA + hh) * 512 + r16;
      c.av1 = (hh ? 160 + cA : 80 + cC) * 512 + r16 - 1024;
      c.av2 = (hh ? 160 + cC : 161 + cA) * 512 + (r16 ^ (hh ? 128 : 64)) - 2048;
      c.ak1 = 1024 + odd * 8 + r16;
      const u32 pc4 = lane & 3, db = (lane >> 4) & 1, kq = ((lane >> 3) & 4) + ((lane >> 2) & 3);
      const u32 vpl = db ? (pc4 ? 3 : 2) : pc4 & 1;
      c.avl = 1536 + vpl * 512 + (db ? odd : pc4 >> 1) * 8 + ((kq ^ (4 * vpl)) << 4);
      c.aob = A_OF + (((c.bid >> 5) * 8 + 2 * c.w + db) * 80 + cA + hh) * 256 + (lane & 15) * 16;
    }
    c.rw = c.lb + L_HX + c.w * 10 * KB + (lane & 15) * 64 + (((lane >> 4) ^ (lane >> 1)) & 3) * 16;
    const u32 base = c.lb + L_HX + (c.w >> 1) * 5 * KB + t16 * 64, row = c.sw + 8 * c.w + (lane >> 3);
    c.rpa = base + (cc >> 1) * KB + (((2 * (cc & 1)) ^ g ^ ((cc >> 1) & 1)) << 4);
    c.rpt = base + 4 * KB + (((cc >> 1) ^ g) << 4) + (cc & 1) * 8;
    c.rvo = ((row >> 4) * 80 + 10 * cg + cc) * 256 + t16 * 16;
    c.rvt = ((row >> 4) * 80 + 10 * cg + 8 + (cc >> 2)) * 256 + t16 * 16 + (cc & 3) * 4;
    c.rvs = cc == 0 ? (A_SA - A_XA) + row * 64 + cg * 8 : OOR;
  }
  pin(c.cv); pin(c.dxx); pin(c.doh); pin(c.daa); pin(c.foff); pin(c.eq); pin(c.eg); pin(c.es); pin(c.av0); pin(c.av1); pin(c.av2); pin(c.ak1); pin(c.avl); pin(c.aob);
  pin(c.rw); pin(c.rpa); pin(c.rpt); pin(c.rvo); pin(c.rvt); pin(c.rvs);
  c.bof = sop(c.bof); c.bhf = sop(c.bhf); c.m0a = sop(c.m0a); Bq.base = sop(Bq.base); Bw.base = sop(Bw.base); Bg.base = sop(Bg.base); Bd.base = sop(Bd.base);
  need(vm, mlv); pin(epv); pin(lv[0]); pin(lv[1]); pin(lv[2]);
  const u32 ep = __builtin_amdgcn_readfirstlane(epv);
  flag_store(c, vm, ep);
  sfor<4, 6>(LAMBDA(K) { raw_x<0, K.v>(c, vm, raw, sx); raw_w<2 * K.v>(vm, raw); raw_w<2 * K.v + 1>(vm, raw); });
  sfor<6, 8>(LAMBDA(K) { raw_x<0, K.v>(c, vm, raw, sx); });
  ln1_stats(c, vm, lv, lane, lrow);
  const int mls = vm.n;
  job_load<2>(p, vm, jn, c.bid - 86, c.bid >= 86, c.w, lane);
  u32 fl = ep + 2;
  { const int l = 0; (void)l;
    asm volatile("; MARK QX0");
    { f32x4 acc[2][4], cs[2]; EpQ e; u32x2 st0; int mst0 = 0;
      gemm_q0(c, vm, sx, raw, mls, mst, fl, acc, cs, Bq, LAMBDA(Gp) {
        if constexpr (G % 3 < 2 && G / 3 < 14) raw_w<12 + 2 * (G / 3) + G % 3>(vm, raw);
        if constexpr (G % 3 == 2 && G / 3 < 12) raw_x<0, 8 + G / 3>(c, vm, raw, sx);
        if constexpr (G == 38 || G == 41) raw_x<1, (G - 38) / 3>(c, vm, raw, sy);
        at<G, 42, 1, 2>(LAMBDA(K) { raw_x<1, 2 + K.v>(c, vm, raw, sy); });
        at<G, 45, 1, 4>(LAMBDA(K) { raw_x<1, 4 + K.v>(c, vm, raw, sy); });
        at<G, 51, 1, 4>(LAMBDA(K) { raw_x<1, 8 + K.v>(c, vm, raw, sy); });
        at<G, 57, 1, 3>(LAMBDA(K) { raw_x<1, 12 + K.v>(c, vm, raw, sy); });
      }, LAMBDA(S) { constexpr int s = S.v;
        if (s == 0) job_load<1>(p, vm, jg, c.bid, c.bid < 240, c.w, lane);
        if constexpr (s >= JR1 && s < JR1 + 3) job_ph<2, s - JR1>(c, vm, jn, lane);
        if constexpr (s >= JR2 && s < JR2 + 4) { job_ph<1, s - JR2>(c, vm, jg, lane); mst = vm.n; }
        if (s == 7) poll_issue(c, vm, pl, (2 * (c.tq >> 4) + ((lane >> 1) & 1) + 8 * (2 * (c.tq & 15) + (lane & 1))) << 7, fl - 1);
        if (s == 10) poll_again(c, vm, pl);
        if (s == 12) poll_check<true, NAP1>(c, vm, pl);
        if (s == 19) poll_issue(c, vm, pl, OFF_DENSE + ((c.bid & (DREP - 1)) * 256 + threadIdx.x) * DSTR, fl);
        if (s == 12) mst0 = ld8c<0>(c.ra, vm, st0, ((lane >> 4) & 1) * 65536 + ((lane >> 1) & 7) * 128 + (lane & 1) * 64, A_SA + sop(c.tq) * 1024);
        if (s == 17) { need(vm, mst0); pin(st0); *LP(u32x2, c.lb + L_STAT + ((lane >> 4) & 1) * 512 + c.w * 128 + (lane & 15) * 8) = st0; }
        if (s == WARMS) {
  {
    const rsrc_t rw = __builtin_amdgcn_make_buffer_rsrc(p.ws, 0, (int)WS_END, 0x00020000);
    const u32 t = threadIdx.x, id = blockIdx.x * 16 + (t & 15);
    const u32 so = OFF_ACT + ((t >> 4) == 0 ? ((id >> 10) & 1) * LB + ((id >> 11) ? A_SB : A_SA) + (id & 1023) * 128
                                           : ((t >> 4) & 1) * LB + A_OF + ((id >> 5) * 80 + 5 * ((id >> 1) & 15) + 2) * 256 + (id & 1) * 128);
    asm volatile("buffer_load_dword a251, %0, %1, 0 offen sc1" :: "v"(t < 48 ? so : OOR), "s"(rw) : "memory");
  }
        }
      });
      *LP(f32x4, c.lb + L_CS + c.w * 128 + (lane >> 4) * 32) = cs[0]; *LP(f32x4, c.lb + L_CS + c.w * 128 + (lane >> 4) * 32 + 16) = cs[1];
      BARL();
      pre_q<0, 1>(c, e); e.cs[0] = cs[0]; e.cs[1] = cs[1];
      epi_q(c, vm, e, acc); mst = vm.n;
      }
    asm volatile("; MARK QY0");
    sfor<0, 40>(LAMBDA(J) { Bq.m[J.v] = 0; });
    { f32x4 acc[2][4]; EpQ e; u32x4 t[5]; int mt = 0;
      gemm_lds<1, 2, 0x0B, QYP, false>(c, vm, sy, mst, fl + 1, acc, Bq, LAMBDA(Gp) {
        at<G, 0, 1, 5>(LAMBDA(K) { raw_x<1, 15 + K.v>(c, vm, raw, sy); });
        at<G, 5, 1, 2>(LAMBDA(K) { const u32 o = (c.sw + K.v * 1024 + 8 * c.w + (lane >> 3)) * 1280 + 160 * c.cw;
          gld(vm, xr[K.v].m, p.x, o + 16 * (lane & 7)); gld(vm, xr[K.v].t, p.x, o + 128 + 4 * (lane & 7)); });
        at<G, 8, 1, QBW>(LAMBDA(K) { ldB<K.v>(c, vm, Bw); });
        atn<G, 2 * YC + 1, FPG, 11>(LAMBDA(K) { a_stage<0, K.v>(c, vm, ain); });
      }, LAMBDA(S) { constexpr int s = S.v;
        if (s == 0) poll_again(c, vm, pl);
        if (s == YC + 1) { pin(xr[0].m); pin(xr[0].t); pin(xr[1].m); pin(xr[1].t); }
        if (s == 2) poll_check<true, NAP1>(c, vm, pl);
        if (s == 3) {
          const u32 o = lane < 48 ? OFF_CS + c.cg * 768 + c.w * 192 + (lane % 12) * 16 + (lane / 12) * 5 * CS_ROW : OOR;
          sfor<0, 5>(LAMBDA(J) { mt = ld16<-1, true, 0>(c.rs, vm, t[J.v], o, sop(J.v * CS_ROW)); });
        }
        if (s == 10) { need(vm, mt); f32x4 sum = {0, 0, 0, 0}; sfor<0, 5>(LAMBDA(J) { pin(t[J.v]); sum += __builtin_bit_cast(f32x4, t[J.v]); }); *LP(f32x4, c.lb + 64 * KB + c.w * 4096 + c.l16) = sum; }
        if (s == 12) { const u32 rb = c.lb + 64 * KB + c.w * 4096 + (lane % 12) * 16; sfor<0, 4>(LAMBDA(J) { t[J.v] = *LP(const u32x4, rb + J.v * 192); }); }
        if (s == 13) *LP(f32x4, c.lb + L_CS + 512 + c.w * 192 + (lane % 12) * 16) = (__builtin_bit_cast(f32x4, t[0]) + __builtin_bit_cast(f32x4, t[1])) + (__builtin_bit_cast(f32x4, t[2]) + __builtin_bit_cast(f32x4, t[3]));
        if (s == 6) ctouch<0>(c, vm);
        if (s == 5) job_load<0>(p, vm, jq, c.bid, c.bid < 150, c.w, lane);
        if (s == 15) job_ph<0, 0, 64>(c, vm, jq, lane);
        if (s == YP1) a_poll(c, vm, pl, fl, lo16(c.daa));
        if (s == YP2) poll_again(c, vm, pl);
        if (s == YC) poll_check<true>(c, vm, pl);
        if (s == 17) pre_q<1, 0>(c, e);
        if (s == 18) pre_q<1, 1>(c, e);
      });
      sfor<(39 - 2 * YC) * FPG, 11>(LAMBDA(K) { a_stage<0, K.v>(c, vm, ain); });
      epi_q(c, vm, e, acc);
      if (vm.n - ain.mark != CARRYA) carry_count_too_large();
      }
  }
#pragma unroll 1
  for (int l = 0;; ++l) {
    vm.n = 0; vm.done = -1000; ain.mark = -CARRYA; mst = 0;
    sfor<0, QBW>(LAMBDA(J) { Bw.m[J.v] = -CARRYA; });
    Bw.cur = sop(Bw.base) + (QBW & ~3) * KB;
    asm volatile("; MARK AX");
    AIn ain2;
    constexpr int BWR = 25 - QBW;
    attn<0>(c, vm, ain, LAMBDA(Gp) {
      at<G, 0, 1, BWR>(LAMBDA(K) { ldB<25 - BWR + K.v>(c, vm, Bw); });
      at<G, BWR, 1, 6 + 3 * AC - BWR>(LAMBDA(K) { ldB<K.v>(c, vm, Bg); });
      atn<G, 6 + 3 * AC, FPA, 11>(LAMBDA(K) { a_stage<1, K.v>(c, vm, ain2); });
    }, LAMBDA(S) { constexpr int s = S.v;
      if (s == APUB) publish(c, vm, mst, fl + 2);
      if (s == AP1) a_poll(c, vm, pl, fl, hi16(c.daa));
      if (s == AP2) poll_again(c, vm, pl);
      if (s == AC) { poll_check<true>(c, vm, pl); }
      if (s == 8) { sfor<(22 - 3 * AC) * FPA, 11>(LAMBDA(K) { a_stage<1, K.v>(c, vm, ain2); }); }
    });
    mst = vm.n;
    asm volatile("; MARK AY");
    AW aw;
    attn<1>(c, vm, ain2, LAMBDA(Gp) {
      at<G, 0, 1, 6 + 3 * AC>(LAMBDA(K) { if constexpr (6 + 3 * AC - BWR + K.v < 38) ldB<6 + 3 * AC - BWR + K.v>(c, vm, Bg); });
      atn<G, 6 + 3 * AC, FPA, 10>(LAMBDA(K) { ld_w<0, K.v>(c, vm, aw); });
    }, LAMBDA(S) { constexpr int s = S.v;
      if (s == APUB) publish(c, vm, mst, fl + 3);
      if (s == 0) ctouch<1>(c, vm);
      if (s == AP1) poll_issue(c, vm, pl, lo16(c.doh), fl + 3);
      if (s == AP2) poll_again(c, vm, pl);
      if (s == AC) { poll_check<true>(c, vm, pl); }
      if (s == 8) { sfor<(22 - 3 * AC) * FPA, 10>(LAMBDA(K) { ld_w<0, K.v>(c, vm, aw); }); }
    });
    mst = vm.n;
    asm volatile("; MARK WX");
    constexpr int BG0 = 12 + 6 * AC - BWR < 38 ? 12 + 6 * AC - BWR : 38;
    static_assert(44 - BG0 <= 13, "");
    { Red r;
      gemm_reg<0, 5, true>(c, vm, Bw, aw, LAMBDA(Gp) { at<G, 0, 1, 12>(LAMBDA(K) { ldB<BG0 + K.v>(c, vm, Bg); }); }, LAMBDA(S) { if constexpr (S.v >= 100 && S.v < 104) red_pre<0, S.v - 100>(c, r, xr[0], A_XB); if (S.v == WPUB) { publish(c, vm, mst, fl + 4); } });
      BARL();
      poll_issue(c, vm, pl, lo16(c.doh), fl + 4);
      red_out<0>(c, vm, r, xr[0], nullptr); mst = vm.n;
      poll_again(c, vm, pl);
      }
    asm volatile("; MARK WY");
    { Red r;
      sfor<0, 4>(LAMBDA(K) { red_pre<1, K.v>(c, r, xr[1], A_XB); });
      publish(c, vm, mst, fl + 5);
      poll_check<true>(c, vm, pl);
      sfor<0, 10>(LAMBDA(K) { ld_w<1, K.v>(c, vm, aw); });
      if (WP1 < 0) poll_issue(c, vm, pl, lo16(c.dxx), fl + 5);
      loadB<BG0 + 12, BG0 + 16>(c, vm, Bg);
      if (l == 0) { VM v2 = {}; const u32 lane = vop(c.l16) >> 4; job_ph<0, 1, 64>(c, v2, jq, lane); job_ph<0, 2, 64>(c, v2, jq, lane); }
      constexpr int WG = (WC * 10 + 4) / 4 + 1, WN = WC < 5 ? (2 * (13 - WG) < 9 ? 2 * (13 - WG) : 9) : 0, WB = WC < 5 ? WG : 13;
      gemm_reg<1, 5, true>(c, vm, Bw, aw, LAMBDA(Gp) {
        at<G, 0, 1, (44 - BG0 < WB ? 44 - BG0 : WB)>(LAMBDA(K) { ldB<BG0 + 16 + K.v>(c, vm, Bg); });
        if constexpr (G >= WG && 2 * (G - WG) < WN) { stage<0, 2 * (G - WG)>(c, vm, sx, A_XB, c.tq); if constexpr (2 * (G - WG) + 1 < WN) stage<0, 2 * (G - WG) + 1>(c, vm, sx, A_XB, c.tq); }
      }, LAMBDA(S) { constexpr int s = S.v; if (s == WP1) poll_issue(c, vm, pl, lo16(c.dxx), fl + 5); if (s == WP2) poll_again(c, vm, pl); if (s == WC) poll_check<true>(c, vm, pl); });
      if (WC == 5) poll_check<true>(c, vm, pl);
      stages<0, WN, 9>(c, vm, sx, A_XB, c.tq);
      BARL();
      red_out<1>(c, vm, r, xr[1], nullptr); mst = vm.n;
      stages<0, 9, 13>(c, vm, sx, 0, 0);
      sfor<BG0 + 16 + WB, 60>(LAMBDA(K) { ldB<K.v>(c, vm, Bg); });
      }
    asm volatile("; MARK GX");
    { f32x4 acc[3][4]; EpG e;
      gemm_lds<0, 3, GXS, GXP, true>(c, vm, sx, mst, fl + 6, acc, Bg, LAMBDA(Gp) {
        at<G, 0, 1, 8>(LAMBDA(K) { stage<0, 13 + K.v>(c, vm, sx, 0, 0); });
        at56<G, 9, 25>(LAMBDA(K) { ldB<K.v>(c, vm, Bd); });
        at56<G, 40, 17>(LAMBDA(K) { stage<1, K.v>(c, vm, sy, A_XB, c.tq); });
      }, LAMBDA(S) { constexpr int s = S.v;
        if (s == 4) ctouch<2>(c, vm);
        if (s == 8) poll_issue(c, vm, pl, hi16(c.dxx), fl + 6);
        if (s == 10) poll_again(c, vm, pl);
        if (s == 13) { poll_check<true>(c, vm, pl); }
        if (s == 17) pre_g<0, 0>(c, e);
        if (s == 18) pre_g<0, 1>(c, e);
      });
      epi_g(c, vm, e, acc); mst = vm.n; }
    asm volatile("; MARK GY");
    AD<true> ad;
    { f32x4 acc[3][4]; EpG e;
      gemm_lds<1, 3, GYS, GYP, true>(c, vm, sy, mst, fl + 7, acc, Bg, LAMBDA(Gp) {
        at<G, 0, 1, 4>(LAMBDA(K) { stage<1, 17 + K.v>(c, vm, sy, 0, 0); });
        at56<G, 4, 35>(LAMBDA(K) { ldB<25 + K.v>(c, vm, Bd); });
        at56<G, 46, 12>(LAMBDA(K) { ld_d<0, K.v>(c, vm, ad); });
      }, LAMBDA(S) { constexpr int s = S.v;
        if (s == 5) ctouch<3>(c, vm);
        if (s == 9) poll_issue(c, vm, pl, hi16(c.doh), fl + 7);
        if (s == 11) poll_again(c, vm, pl);
        if (s == 14) { poll_check<true>(c, vm, pl); }
        if (s == 17) pre_g<1, 0>(c, e);
        if (s == 18) pre_g<1, 1>(c, e);
      });
      epi_g(c, vm, e, acc); mst = vm.n; sfor<12, 14>(LAMBDA(K) { ld_d<0, K.v>(c, vm, ad); }); }
    asm volatile("; MARK DX");
    if (l) Bq.base = OOR;
    AD<false> ad2;
    constexpr int DG = (DC * 10 + 4) / 4 + 1, DN = DC < 12 ? 30 - DG : 0;
    { Red r;
      gemm_reg<0, 12, DXN>(c, vm, Bd, ad, LAMBDA(Gp) {
        at<G, 0, 1, 10>(LAMBDA(K) { ld_d<0, 14 + K.v>(c, vm, ad); });
        at<G, 11, 1, 10>(LAMBDA(K) { ldB<K.v>(c, vm, Bq); });
        at<G, DG, 1, DN>(LAMBDA(K) { ld_d<1, K.v>(c, vm, ad2); });
      }, LAMBDA(S) { constexpr int s = S.v;
        if (s == DPUB) publish(c, vm, mst, fl + 8);
        if (s == 5) ctouch<4>(c, vm);
        if (s == DP1) poll_issue(c, vm, pl, hi16(c.doh), fl + 8);
        if constexpr (s >= 100 && s < 104) red_pre<0, s - 100>(c, r, xr[0], LB + A_XA);
        if (s == DP2) poll_again(c, vm, pl);
        if (s == DC) poll_check<true>(c, vm, pl);
      });
      if (DC == 12) poll_check<true>(c, vm, pl);
      sfor<DN, 6>(LAMBDA(K) { ld_d<1, K.v>(c, vm, ad2); });
      BARL();
      red_out<0>(c, vm, r, xr[0], l ? p.out : nullptr); mst = vm.n;
      sfor<(DN > 6 ? DN : 6), 12>(LAMBDA(K) { ld_d<1, K.v>(c, vm, ad2); });
      }
    asm volatile("; MARK DY");
    Red ry;
      gemm_reg<1, 12, false>(c, vm, Bd, ad2, LAMBDA(Gp) {
        at<G, 0, 1, 12>(LAMBDA(K) { ld_d<1, 12 + K.v>(c, vm, ad2); });
        at<G, 17, 1, 13 - DN>(LAMBDA(K) { ldB<10 + K.v>(c, vm, Bq); });
        at<G, DG, 1, DN>(LAMBDA(K) { stage<0, K.v>(c, vm, sx, LB + A_XA, c.tq); });
      }, LAMBDA(S) { constexpr int s = S.v;
        if (s == DPUB) publish(c, vm, mst, fl + 9);
        if (s == DP1) poll_issue(c, vm, pl, lo16(c.dxx), l ? fl : fl + 9);
        if constexpr (s >= 100 && s < 104) red_pre<1, s - 100>(c, ry, xr[1], LB + A_XA);
        if (s == DP2) poll_again(c, vm, pl);
        if (s == DC) poll_check<true>(c, vm, pl);
      });
      if (l) { BARL(); red_last<1>(c, ry, p.out); break; }
      if (DC == 12) poll_check<true>(c, vm, pl);
      stages<0, DN, 9>(c, vm, sx, LB + A_XA, c.tq);
      BARL();
      red_out<1>(c, vm, ry, xr[1], nullptr); mst = vm.n;
      stages<0, 9, 13>(c, vm, sx, 0, 0);
    c.ra = __builtin_amdgcn_make_buffer_rsrc(p.ws + OFF_ACT + LB, 0, (int)LB, 0x00020000);
    sx.src -= LB; fl += 10;
    ++vm.n; __builtin_amdgcn_raw_buffer_store_b32(ep + 64, c.rs, c.foff | (c.bid ? OOR : 0), OFF_EPOCH, 16);
    asm volatile("; MARK QX");
    { f32x4 acc[2][4]; EpQ e;
      gemm_lds<0, 2, QXS, QXP, true>(c, vm, sx, mst, fl, acc, Bq, LAMBDA(Gp) {
        at<G, 0, 1, 8>(LAMBDA(K) { stage<0, 13 + K.v>(c, vm, sx, 0, 0); });
        at<G, 8, 1, 17 + DN>(LAMBDA(K) { ldB<23 - DN + K.v>(c, vm, Bq); });
        at<G, 2 * QC + 1, 1, 13>(LAMBDA(K) { stage<1, K.v>(c, vm, sy, A_XA, c.tq); });
      }, LAMBDA(S) { constexpr int s = S.v;
        if (s == QP1) poll_issue(c, vm, pl, lo16(c.dxx), fl);
        if (s == QP2) poll_again(c, vm, pl);
        if (s == QC) { poll_check<true>(c, vm, pl); }
        if (s == 17) pre_q<0, 0>(c, e);
        if (s == 18) pre_q<0, 1>(c, e);
      });
      epi_q(c, vm, e, acc); mst = vm.n; stages<1, 39 - 2 * QC, 13>(c, vm, sy, 0, 0); }
    asm volatile("; MARK QY");
    { f32x4 acc[2][4]; EpQ e;
      gemm_lds<1, 2, QYS, QYP, true>(c, vm, sy, mst, fl + 1, acc, Bq, LAMBDA(Gp) {
        at<G, 0, 1, 8>(LAMBDA(K) { stage<1, 13 + K.v>(c, vm, sy, 0, 0); });
        at<G, 8, 1, QBW>(LAMBDA(K) { ldB<K.v>(c, vm, Bw); });
        atn<G, 2 * YC + 1, FPG, 11>(LAMBDA(K) { a_stage<0, K.v>(c, vm, ain); });
      }, LAMBDA(S) { constexpr int s = S.v;
        if (s == YP1) a_poll(c, vm, pl, fl, lo16(c.daa));
        if (s == YP2) poll_again(c, vm, pl);
        if (s == YC) { poll_check<true>(c, vm, pl); }
        if (s == 17) pre_q<1, 0>(c, e);
        if (s == 18) pre_q<1, 1>(c, e);
      });
      sfor<(39 - 2 * YC) * FPG, 11>(LAMBDA(K) { a_stage<0, K.v>(c, vm, ain); });
      epi_q(c, vm, e, acc); }
    if (vm.n - ain.mark != CARRYA) carry_count_too_large();
    asm volatile("; MARK END");
  }
  asm volatile(".Lmk_end:");
  asm volatile("s_waitcnt vmcnt(0)" ::: "memory");
}

extern "C" void kernel_launch(void* const* d_in, const int* in_sizes, int n_in, void* d_out, int out_size, void* d_ws, size_t ws_size, hipStream_t stream) {
  Params p;
  p.x = (const u16*)d_in[0]; p.wqkv = (const u16*)d_in[1]; p.wo = (const u16*)d_in[2];
  p.wg = (const u16*)d_in[4]; p.wu = (const u16*)d_in[5]; p.wd = (const u16*)d_in[6];
  p.out = (u16*)d_out; p.ws = (unsigned char*)d_ws;
  (void)hipFuncSetAttribute((const void*)mega_kernel, hipFuncAttributeMaxDynamicSharedMemorySize, LDS_BYTES);
  mega_kernel<<<256, 256, LDS_BYTES, stream>>>(p);
}
```
